```python
import torch

def _softmax(x, axis=-1):
    return torch.softmax(x, dim=axis)
def _gelu(x):
    return torch.nn.functional.gelu(x, approximate="tanh")
def _silu(x):
    return torch.nn.functional.silu(x)
def _sigmoid(x):
    return torch.sigmoid(x)
def _layernorm(x, axis=-1, eps=1e-5):
    m = x.mean(dim=axis, keepdim=True)
    v = x.var(dim=axis, keepdim=True, unbiased=False)
    return (x - m) / torch.sqrt(v + eps)
def _scatter_add(tgt, idx, src, axis=0):
    return tgt.clone().index_add_(axis, idx, src)
def _segment_sum(src, seg, n):
    out = torch.zeros((n,) + src.shape[1:], dtype=src.dtype)
    return out.index_add_(0, seg, src)
def _pad(x, pw):
    flat = tuple(v for pair in reversed(pw) for v in pair)
    return torch.nn.functional.pad(x, flat)

kernel_name = 'standalone__mha_softmax__near_ridge'

def setup_inputs():

    _w = (lambda a: (lambda t: t.to(torch.bfloat16) if t.is_floating_point() else t)(torch.as_tensor(a)))
    B, H, Sq, Sk, D = 8, 32, 496, 1024, 128
    return {
        "q": _w((lambda *s: torch.randn(*s, dtype=torch.bfloat16) * 0.8)(B, H, Sq, D)),
        "k": _w((lambda *s: torch.randn(*s, dtype=torch.bfloat16) * 0.8)(B, H, Sk, D)),
        "v": _w((lambda *s: torch.randn(*s, dtype=torch.bfloat16) * 0.05)(B, H, Sk, D)),
    }

def reference(q, k, v):
    scale = q.shape[-1] ** -0.5
    scores = torch.einsum("bhqd,bhkd->bhqk", q, k) * scale
    p = _softmax(scores, axis=-1)
    return torch.einsum("bhqk,bhkd->bhqd", p, v)
```

```cpp
#include <hip/hip_runtime.h>
#include <hip/hip_bf16.h>
#include <cstdio>

using bf16 = __hip_bfloat16;
constexpr int   D = 128, NW = 8, QBLK = 32, KVBLK = 64;
constexpr float SCALE = 0.088388347648318440f;
#define ATTN_THR 8.f
constexpr float THR = ATTN_THR;
#define ATTN_LAYOUT 0
constexpr int LAYOUT = ATTN_LAYOUT;
#define ATTN_SDEPTH 2
constexpr int SDEPTH = ATTN_SDEPTH;
static_assert(SDEPTH == 1 || SDEPTH == 2, "SDEPTH is 1 or 2");
#define ATTN_B 8
#define ATTN_HQ 32
#define ATTN_HKV 32
#define ATTN_S 512
#define ATTN_SKV 1024
constexpr int B = ATTN_B, HQ = ATTN_HQ, HKV = ATTN_HKV, S = ATTN_S, SKV = ATTN_SKV;
constexpr int LDQ = LAYOUT ? HQ * D : D, LDK = LAYOUT ? HKV * D : D, LDO = LDQ;
static_assert(S % (QBLK * NW) == 0 && SKV % (2 * KVBLK) == 0 && HQ % HKV == 0, "S % 256 == 0, SKV % 128 == 0 (even tile count), HQ % HKV == 0");
constexpr size_t SHM_V = KVBLK * D * 2, SHM_K = KVBLK * D * 2, SHM_ATTN = 2 * SHM_V + 2 * SHM_K + NW * 64 * 4;

using bf16x8 = __attribute__((ext_vector_type(8))) short;
using s16x4  = __attribute__((ext_vector_type(4))) short;
using f32x16 = __attribute__((ext_vector_type(16))) float;
using f32x8  = __attribute__((ext_vector_type(8))) float;
using u32x4  = __attribute__((ext_vector_type(4))) unsigned;
#define KSWZ(row, colB) ((row) * 256 + ((colB) ^ (((row) & 7) << 4)))
#define SBAR() __builtin_amdgcn_sched_barrier(0)
__device__ __forceinline__ int crow(int r, int hi) { return (r & 3) + 8 * (r >> 2) + 4 * hi; }
__device__ __forceinline__ unsigned cvtpk(float lo, float hi) {
  unsigned r; asm volatile("v_cvt_pk_bf16_f32 %0, %1, %2" : "=v"(r) : "v"(lo), "v"(hi)); return r;
}
template <typename TIn> struct Stage;
template <> struct Stage<bf16>  { using T = bf16x8;
  __device__ static __forceinline__ T ld8(const bf16* p) { return *reinterpret_cast<const bf16x8*>(p); }
  __device__ static __forceinline__ bf16x8 tobf(T x) { return x; } };
template <> struct Stage<float> { using T = f32x8;
  __device__ static __forceinline__ T ld8(const float* p) { return *reinterpret_cast<const f32x8*>(p); }
  __device__ static __forceinline__ bf16x8 tobf(T x) {
    u32x4 w = {cvtpk(x[0], x[1]), cvtpk(x[2], x[3]), cvtpk(x[4], x[5]), cvtpk(x[6], x[7])}; return *reinterpret_cast<bf16x8*>(&w); } };

__device__ __forceinline__ void partialSM(f32x16& p0, f32x16& p1, float& m_reg, float& mn, float& alpha) {
  constexpr float C = SCALE * 1.4426950408889634f;
  float pmax = p0[0]; for (int r = 1; r < 16; ++r) pmax = fmaxf(pmax, p0[r]); for (int r = 0; r < 16; ++r) pmax = fmaxf(pmax, p1[r]);
  { auto rr = __builtin_amdgcn_permlane32_swap(__float_as_uint(pmax), __float_as_uint(pmax), false, false);
    pmax = fmaxf(__uint_as_float(rr[0]), __uint_as_float(rr[1])); }
  if (__builtin_expect(__all(pmax - m_reg <= THR / SCALE), 1)) { mn = m_reg; alpha = 1.f; }
  else { mn = fmaxf(m_reg, pmax); alpha = __builtin_amdgcn_exp2f((m_reg - mn) * C); m_reg = mn; }
  float mnC = -mn * C;
  for (int r = 0; r < 16; ++r) p0[r] = fmaf(p0[r], C, mnC); for (int r = 0; r < 16; ++r) p1[r] = fmaf(p1[r], C, mnC);
  for (int r = 0; r < 16; ++r) p0[r] = __builtin_amdgcn_exp2f(p0[r]);
}
__device__ __forceinline__ void finishSM(f32x16& p0, f32x16& p1, float alpha, float& l_reg, bf16x8& pa0, bf16x8& pa1, bf16x8& pa2, bf16x8& pa3) {
  for (int r = 0; r < 16; ++r) p1[r] = __builtin_amdgcn_exp2f(p1[r]);
  float ps = 0; for (int r = 0; r < 16; ++r) ps += p0[r]; for (int r = 0; r < 16; ++r) ps += p1[r];
  { auto rr = __builtin_amdgcn_permlane32_swap(__float_as_uint(ps), __float_as_uint(ps), false, false);
    ps = __uint_as_float(rr[0]) + __uint_as_float(rr[1]); }
  l_reg = l_reg * alpha + ps;
#define PK4(P, BASE, OUT) do { unsigned a0 = cvtpk(P[BASE + 0], P[BASE + 1]), a1 = cvtpk(P[BASE + 2], P[BASE + 3]);   \
    unsigned b0 = cvtpk(P[BASE + 4], P[BASE + 5]), b1 = cvtpk(P[BASE + 6], P[BASE + 7]);                              \
    auto r0 = __builtin_amdgcn_permlane32_swap(a0, b0, false, false); auto r1 = __builtin_amdgcn_permlane32_swap(a1, b1, false, false); \
    u32x4 w = {r0[0], r1[0], r0[1], r1[1]}; OUT = *reinterpret_cast<bf16x8*>(&w); } while (0)
  PK4(p0, 0, pa0); PK4(p0, 8, pa1); PK4(p1, 0, pa2); PK4(p1, 8, pa3);
#undef PK4
}
__device__ __forceinline__ void qkt(f32x16& p0, f32x16& p1, const bf16* Ks, const bf16x8* qr, int r32, int hi) {
  p0 = f32x16{}; p1 = f32x16{};
  for (int d0 = 0; d0 < 8; ++d0) { int cb = (d0 * 16 + hi * 8) * 2;
    bf16x8 b0 = *reinterpret_cast<const bf16x8*>((const char*)Ks + KSWZ(r32, cb));
    bf16x8 b1 = *reinterpret_cast<const bf16x8*>((const char*)Ks + KSWZ(32 + r32, cb));
    p0 = __builtin_amdgcn_mfma_f32_32x32x16_bf16(b0, qr[d0], p0, 0, 0, 0);
    p1 = __builtin_amdgcn_mfma_f32_32x32x16_bf16(b1, qr[d0], p1, 0, 0, 0); }
}
__device__ __forceinline__ int v_st(int k, int c) { const int kk = (k & ~0xC) | ((k & 4) << 1) | ((k & 8) >> 1); return ((kk >> 3) * 4 + (c >> 5)) * 512 + ((kk & 7) * 32 + (c & 31)) * 2; }
__device__ __forceinline__ int v_rd_base(int lane) { return ((lane & 3) << 3) | (((lane >> 2) & 3) << 6) | (((lane >> 4) & 1) << 5) | (((lane >> 5) & 1) << 8); }
constexpr int v_rd_off(int d0, int ks, int half) { return d0 * 512 + ks * 4096 + half * 2048; }
template <int OFF> __device__ __forceinline__ s16x4 tr_read(int vb) {
  s16x4 r; asm volatile("ds_read_b64_tr_b16 %0, %1 offset:%2" : "=&v"(r) : "v"(vb), "i"(OFF) : "memory"); return r;
}
template <int D0> __device__ __forceinline__ void pv_one(f32x16& od, int vb, bf16x8 pa0, bf16x8 pa1, bf16x8 pa2, bf16x8 pa3) {
  const s16x4 l0 = tr_read<v_rd_off(D0, 0, 0)>(vb), h0 = tr_read<v_rd_off(D0, 0, 1)>(vb), l1 = tr_read<v_rd_off(D0, 1, 0)>(vb), h1 = tr_read<v_rd_off(D0, 1, 1)>(vb);
  const s16x4 l2 = tr_read<v_rd_off(D0, 2, 0)>(vb), h2 = tr_read<v_rd_off(D0, 2, 1)>(vb), l3 = tr_read<v_rd_off(D0, 3, 0)>(vb), h3 = tr_read<v_rd_off(D0, 3, 1)>(vb);
  asm volatile("s_waitcnt lgkmcnt(0)" ::: "memory"); SBAR();
#define PK(L, H) (bf16x8){L[0], L[1], L[2], L[3], H[0], H[1], H[2], H[3]}
  od = __builtin_amdgcn_mfma_f32_32x32x16_bf16(pa0, PK(l0, h0), od, 0, 0, 0);
  od = __builtin_amdgcn_mfma_f32_32x32x16_bf16(pa1, PK(l1, h1), od, 0, 0, 0);
  od = __builtin_amdgcn_mfma_f32_32x32x16_bf16(pa2, PK(l2, h2), od, 0, 0, 0);
  od = __builtin_amdgcn_mfma_f32_32x32x16_bf16(pa3, PK(l3, h3), od, 0, 0, 0);
#undef PK
}
__device__ __forceinline__ void pv_d0(f32x16* o, int vb, bf16x8 pa0, bf16x8 pa1, bf16x8 pa2, bf16x8 pa3) {
  pv_one<0>(o[0], vb, pa0, pa1, pa2, pa3); pv_one<1>(o[1], vb, pa0, pa1, pa2, pa3); pv_one<2>(o[2], vb, pa0, pa1, pa2, pa3); pv_one<3>(o[3], vb, pa0, pa1, pa2, pa3);
}

template <typename TQ>
__device__ __forceinline__ void attn_dense_body(const TQ* __restrict__ Qb, const bf16* __restrict__ Kh, const bf16* __restrict__ Vh,
                                                bf16* __restrict__ Ob, int seq, char* lds, int nrows) {
  using St = Stage<bf16>; using SQ = Stage<TQ>;
  const int tid = threadIdx.x, wid = tid >> 6, lane = tid & 63, r32 = lane & 31, hi = lane >> 5;
  bf16* V_lds = (bf16*)lds; bf16* K_lds = (bf16*)(lds + 2 * SHM_V);
  float* ws = (float*)(lds + 2 * SHM_V + 2 * SHM_K) + wid * 64; float* li_l = ws; float* al_l = ws + 32;
  float m_reg = -1e30f, l_reg = 0; f32x16 o[4] = {}; bf16x8 qr[8];
  const int qrow_ = min(wid * QBLK + r32, nrows - 1); const TQ* Qw = Qb + (long)qrow_ * LDQ + hi * 8;
#pragma unroll
  for (int d0 = 0; d0 < 8; ++d0) qr[d0] = SQ::tobf(SQ::ld8(Qw + d0 * 16));
  const int sr = tid >> 4, sc = (tid & 15) * 8, vst0 = v_st(sr, sc), vst1 = v_st(32 + sr, sc);
  const int vb0 = (int)(uintptr_t)V_lds + v_rd_base(lane);
  struct { typename St::T vs0, vs1, ks0, ks1; } sr_[SDEPTH];
#define SLOAD(i, k0) do { sr_[i].vs0 = St::ld8(&Vh[(long)((k0) + sr) * LDK + sc]); sr_[i].vs1 = St::ld8(&Vh[(long)((k0) + 32 + sr) * LDK + sc]); \
    sr_[i].ks0 = St::ld8(&Kh[(long)((k0) + sr) * LDK + sc]); sr_[i].ks1 = St::ld8(&Kh[(long)((k0) + 32 + sr) * LDK + sc]); } while (0)
#define SWRITE(b, i) do { *(bf16x8*)((char*)V_lds + (b) * SHM_V + vst0) = St::tobf(sr_[i].vs0);          \
    *(bf16x8*)((char*)V_lds + (b) * SHM_V + vst1) = St::tobf(sr_[i].vs1); int kc = sc * 2;               \
    *(bf16x8*)((char*)K_lds + (b) * SHM_K + KSWZ(sr, kc)) = St::tobf(sr_[i].ks0);                       \
    *(bf16x8*)((char*)K_lds + (b) * SHM_K + KSWZ(32 + sr, kc)) = St::tobf(sr_[i].ks1); } while (0)
#define SWAIT() do { if constexpr (SDEPTH == 2) asm volatile("s_waitcnt vmcnt(4)" ::: "memory"); else asm volatile("s_waitcnt vmcnt(0)" ::: "memory"); } while (0)
#define RESC(a) do { if (__any((a) < 1.f)) { if (hi == 0) al_l[r32] = (a); asm volatile("s_waitcnt lgkmcnt(0)" ::: "memory"); \
    for (int d = 0; d < 4; ++d) for (int r = 0; r < 16; ++r) o[d][r] *= al_l[crow(r, hi)]; } } while (0)
  f32x16 pA0, pA1, pB0, pB1; float mnA, mnB, alA, alB; bf16x8 pa0, pa1, pa2, pa3; const int NT = seq / KVBLK;
  constexpr int SE = 0, SO = SDEPTH - 1;
  SLOAD(SE, 0); asm volatile("s_waitcnt vmcnt(0)" ::: "memory"); SWRITE(0, SE); __syncthreads();
  qkt(pA0, pA1, K_lds, qr, r32, hi); partialSM(pA0, pA1, m_reg, mnA, alA);
  SLOAD(SO, KVBLK); if constexpr (SDEPTH == 2) { if (2 < NT) SLOAD(SE, 2 * KVBLK); }
  SWAIT(); SWRITE(1, SO); __syncthreads();
  for (int j = 1; j + 1 < NT; j += 2) {
    SBAR(); qkt(pB0, pB1, (bf16*)((char*)K_lds + SHM_K), qr, r32, hi);
    finishSM(pA0, pA1, alA, l_reg, pa0, pa1, pa2, pa3); SBAR();
    SLOAD(SO, (j + SDEPTH) * KVBLK); SBAR();
    pv_d0(o, vb0, pa0, pa1, pa2, pa3); partialSM(pB0, pB1, m_reg, mnB, alB);
    __syncthreads(); SWAIT(); SWRITE(0, SE);
    RESC(alB); __syncthreads();
    SBAR(); qkt(pA0, pA1, K_lds, qr, r32, hi);
    finishSM(pB0, pB1, alB, l_reg, pa0, pa1, pa2, pa3); SBAR();
    if (SDEPTH == 1 || j + 3 < NT) SLOAD(SE, (j + 1 + SDEPTH) * KVBLK); SBAR();
    pv_d0(o, vb0 + (int)SHM_V, pa0, pa1, pa2, pa3); partialSM(pA0, pA1, m_reg, mnA, alA);
    __syncthreads(); SWAIT(); SWRITE(1, SO);
    RESC(alA); __syncthreads();
  }
  SBAR(); qkt(pB0, pB1, (bf16*)((char*)K_lds + SHM_K), qr, r32, hi);
  finishSM(pA0, pA1, alA, l_reg, pa0, pa1, pa2, pa3); SBAR();
  pv_d0(o, vb0, pa0, pa1, pa2, pa3); partialSM(pB0, pB1, m_reg, mnB, alB);
  __syncthreads(); RESC(alB);
  finishSM(pB0, pB1, alB, l_reg, pa0, pa1, pa2, pa3); SBAR();
  pv_d0(o, vb0 + (int)SHM_V, pa0, pa1, pa2, pa3);
  if (hi == 0) li_l[r32] = l_reg; asm volatile("s_waitcnt lgkmcnt(0)" ::: "memory");
  float rli[16];
#pragma unroll
  for (int r = 0; r < 16; ++r) rli[r] = __builtin_amdgcn_rcpf(li_l[crow(r, hi)]);
  bf16* Ow = Ob + (long)(wid * QBLK) * LDO;
#pragma unroll
  for (int r = 0; r < 16; ++r) { int orow = crow(r, hi);
    if (wid * QBLK + orow < nrows) for (int d0 = 0; d0 < 4; ++d0) Ow[(long)orow * LDO + d0 * 32 + r32] = __float2bfloat16(o[d0][r] * rli[r]); }
#undef SLOAD
#undef SWRITE
#undef SWAIT
#undef RESC
}

__global__ __launch_bounds__(64 * NW, 1)
void attn_fwd_dense(const bf16* __restrict__ Q, const bf16* __restrict__ K, const bf16* __restrict__ V, bf16* __restrict__ O) {
  extern __shared__ __attribute__((aligned(16))) char shm[];
  const int bid = blockIdx.x;
  const long head = (bid & 7) + 8 * (bid >> 4); const int half = (bid >> 3) & 1;
  const long q0 = (head * 496 + half * 256) * D, k0 = head * 1024 * (long)D;
  attn_dense_body<bf16>(Q + q0, K + k0, V + k0, O + q0, 1024, shm, half ? 240 : 256);
}
extern "C" void kernel_launch(void* const* d_in, const int* in_sizes, int n_in, void* d_out, int out_size, void* d_ws, size_t ws_size, hipStream_t stream) {
  hipFuncSetAttribute((const void*)attn_fwd_dense, hipFuncAttributeMaxDynamicSharedMemorySize, (int)SHM_ATTN);
  hipLaunchKernelGGL(attn_fwd_dense, dim3(512), dim3(64 * NW), SHM_ATTN, stream,
                     (const bf16*)d_in[0], (const bf16*)d_in[1], (const bf16*)d_in[2], (bf16*)d_out);
}
```

```cpp
#include <hip/hip_runtime.h>
#include <hip/hip_bf16.h>
#include <cstdio>

using bf16 = __hip_bfloat16;
constexpr int   D = 128, NW = 8, QBLK = 32, KVBLK = 64;
constexpr float SCALE = 0.088388347648318440f;
#define ATTN_THR 8.f
constexpr float THR = ATTN_THR;
#define ATTN_LAYOUT 0
constexpr int LAYOUT = ATTN_LAYOUT;
#define ATTN_SDEPTH 2
constexpr int SDEPTH = ATTN_SDEPTH;
static_assert(SDEPTH == 1 || SDEPTH == 2, "SDEPTH is 1 or 2");
#define ATTN_B 8
#define ATTN_HQ 32
#define ATTN_HKV 32
#define ATTN_S 512
#define ATTN_SKV 1024
constexpr int B = ATTN_B, HQ = ATTN_HQ, HKV = ATTN_HKV, S = ATTN_S, SKV = ATTN_SKV;
constexpr int LDQ = LAYOUT ? HQ * D : D, LDK = LAYOUT ? HKV * D : D, LDO = LDQ;
static_assert(S % (QBLK * NW) == 0 && SKV % (2 * KVBLK) == 0 && HQ % HKV == 0, "S % 256 == 0, SKV % 128 == 0 (even tile count), HQ % HKV == 0");
constexpr size_t SHM_V = KVBLK * D * 2, SHM_K = KVBLK * D * 2, SHM_ATTN = 2 * SHM_V + 2 * SHM_K + NW * 64 * 4;

using bf16x8 = __attribute__((ext_vector_type(8))) short;
using s16x4  = __attribute__((ext_vector_type(4))) short;
using f32x16 = __attribute__((ext_vector_type(16))) float;
using f32x8  = __attribute__((ext_vector_type(8))) float;
using u32x4  = __attribute__((ext_vector_type(4))) unsigned;
#define KSWZ(row, colB) ((row) * 256 + ((colB) ^ (((row) & 7) << 4)))
#define SBAR() __builtin_amdgcn_sched_barrier(0)
__device__ __forceinline__ int crow(int r, int hi) { return (r & 3) + 8 * (r >> 2) + 4 * hi; }
__device__ __forceinline__ unsigned cvtpk(float lo, float hi) {
  unsigned r; asm volatile("v_cvt_pk_bf16_f32 %0, %1, %2" : "=v"(r) : "v"(lo), "v"(hi)); return r;
}
template <typename TIn> struct Stage;
template <> struct Stage<bf16>  { using T = bf16x8;
  __device__ static __forceinline__ T ld8(const bf16* p) { return *reinterpret_cast<const bf16x8*>(p); }
  __device__ static __forceinline__ bf16x8 tobf(T x) { return x; } };
template <> struct Stage<float> { using T = f32x8;
  __device__ static __forceinline__ T ld8(const float* p) { return *reinterpret_cast<const f32x8*>(p); }
  __device__ static __forceinline__ bf16x8 tobf(T x) {
    u32x4 w = {cvtpk(x[0], x[1]), cvtpk(x[2], x[3]), cvtpk(x[4], x[5]), cvtpk(x[6], x[7])}; return *reinterpret_cast<bf16x8*>(&w); } };

__device__ __forceinline__ void partialSM(f32x16& p0, f32x16& p1, float& m_reg, float& mn, float& alpha) {
  constexpr float C = SCALE * 1.4426950408889634f;
  float pmax = p0[0]; for (int r = 1; r < 16; ++r) pmax = fmaxf(pmax, p0[r]); for (int r = 0; r < 16; ++r) pmax = fmaxf(pmax, p1[r]);
  { auto rr = __builtin_amdgcn_permlane32_swap(__float_as_uint(pmax), __float_as_uint(pmax), false, false);
    pmax = fmaxf(__uint_as_float(rr[0]), __uint_as_float(rr[1])); }
  if (__builtin_expect(__all(pmax - m_reg <= THR / SCALE), 1)) { mn = m_reg; alpha = 1.f; }
  else { mn = fmaxf(m_reg, pmax); alpha = __builtin_amdgcn_exp2f((m_reg - mn) * C); m_reg = mn; }
  float mnC = -mn * C;
  for (int r = 0; r < 16; ++r) p0[r] = fmaf(p0[r], C, mnC); for (int r = 0; r < 16; ++r) p1[r] = fmaf(p1[r], C, mnC);
  for (int r = 0; r < 16; ++r) p0[r] = __builtin_amdgcn_exp2f(p0[r]);
}
__device__ __forceinline__ void finishSM(f32x16& p0, f32x16& p1, float alpha, float& l_reg, bf16x8& pa0, bf16x8& pa1, bf16x8& pa2, bf16x8& pa3) {
  for (int r = 0; r < 16; ++r) p1[r] = __builtin_amdgcn_exp2f(p1[r]);
  float ps = 0; for (int r = 0; r < 16; ++r) ps += p0[r]; for (int r = 0; r < 16; ++r) ps += p1[r];
  { auto rr = __builtin_amdgcn_permlane32_swap(__float_as_uint(ps), __float_as_uint(ps), false, false);
    ps = __uint_as_float(rr[0]) + __uint_as_float(rr[1]); }
  l_reg = l_reg * alpha + ps;
#define PK4(P, BASE, OUT) do { unsigned a0 = cvtpk(P[BASE + 0], P[BASE + 1]), a1 = cvtpk(P[BASE + 2], P[BASE + 3]);   \
    unsigned b0 = cvtpk(P[BASE + 4], P[BASE + 5]), b1 = cvtpk(P[BASE + 6], P[BASE + 7]);                              \
    auto r0 = __builtin_amdgcn_permlane32_swap(a0, b0, false, false); auto r1 = __builtin_amdgcn_permlane32_swap(a1, b1, false, false); \
    u32x4 w = {r0[0], r1[0], r0[1], r1[1]}; OUT = *reinterpret_cast<bf16x8*>(&w); } while (0)
  PK4(p0, 0, pa0); PK4(p0, 8, pa1); PK4(p1, 0, pa2); PK4(p1, 8, pa3);
#undef PK4
}
__device__ __forceinline__ void qkt(f32x16& p0, f32x16& p1, const bf16* Ks, const bf16x8* qr, int r32, int hi) {
  p0 = f32x16{}; p1 = f32x16{};
  for (int d0 = 0; d0 < 8; ++d0) { int cb = (d0 * 16 + hi * 8) * 2;
    bf16x8 b0 = *reinterpret_cast<const bf16x8*>((const char*)Ks + KSWZ(r32, cb));
    bf16x8 b1 = *reinterpret_cast<const bf16x8*>((const char*)Ks + KSWZ(32 + r32, cb));
    p0 = __builtin_amdgcn_mfma_f32_32x32x16_bf16(b0, qr[d0], p0, 0, 0, 0);
    p1 = __builtin_amdgcn_mfma_f32_32x32x16_bf16(b1, qr[d0], p1, 0, 0, 0); }
}
__device__ __forceinline__ int v_st(int k, int c) { const int kk = (k & ~0xC) | ((k & 4) << 1) | ((k & 8) >> 1); return ((kk >> 3) * 4 + (c >> 5)) * 512 + ((kk & 7) * 32 + (c & 31)) * 2; }
__device__ __forceinline__ int v_rd_base(int lane) { return ((lane & 3) << 3) | (((lane >> 2) & 3) << 6) | (((lane >> 4) & 1) << 5) | (((lane >> 5) & 1) << 8); }
constexpr int v_rd_off(int d0, int ks, int half) { return d0 * 512 + ks * 4096 + half * 2048; }
template <int OFF> __device__ __forceinline__ s16x4 tr_read(int vb) {
  s16x4 r; asm volatile("ds_read_b64_tr_b16 %0, %1 offset:%2" : "=&v"(r) : "v"(vb), "i"(OFF) : "memory"); return r;
}
template <int D0> __device__ __forceinline__ void pv_one(f32x16& od, int vb, bf16x8 pa0, bf16x8 pa1, bf16x8 pa2, bf16x8 pa3) {
  const s16x4 l0 = tr_read<v_rd_off(D0, 0, 0)>(vb), h0 = tr_read<v_rd_off(D0, 0, 1)>(vb), l1 = tr_read<v_rd_off(D0, 1, 0)>(vb), h1 = tr_read<v_rd_off(D0, 1, 1)>(vb);
  const s16x4 l2 = tr_read<v_rd_off(D0, 2, 0)>(vb), h2 = tr_read<v_rd_off(D0, 2, 1)>(vb), l3 = tr_read<v_rd_off(D0, 3, 0)>(vb), h3 = tr_read<v_rd_off(D0, 3, 1)>(vb);
  asm volatile("s_waitcnt lgkmcnt(0)" ::: "memory"); SBAR();
#define PK(L, H) (bf16x8){L[0], L[1], L[2], L[3], H[0], H[1], H[2], H[3]}
  od = __builtin_amdgcn_mfma_f32_32x32x16_bf16(pa0, PK(l0, h0), od, 0, 0, 0);
  od = __builtin_amdgcn_mfma_f32_32x32x16_bf16(pa1, PK(l1, h1), od, 0, 0, 0);
  od = __builtin_amdgcn_mfma_f32_32x32x16_bf16(pa2, PK(l2, h2), od, 0, 0, 0);
  od = __builtin_amdgcn_mfma_f32_32x32x16_bf16(pa3, PK(l3, h3), od, 0, 0, 0);
#undef PK
}
__device__ __forceinline__ void pv_d0(f32x16* o, int vb, bf16x8 pa0, bf16x8 pa1, bf16x8 pa2, bf16x8 pa3) {
  pv_one<0>(o[0], vb, pa0, pa1, pa2, pa3); pv_one<1>(o[1], vb, pa0, pa1, pa2, pa3); pv_one<2>(o[2], vb, pa0, pa1, pa2, pa3); pv_one<3>(o[3], vb, pa0, pa1, pa2, pa3);
}

template <typename TQ>
__device__ __forceinline__ void attn_dense_body(const TQ* __restrict__ Qb, const bf16* __restrict__ Kh, const bf16* __restrict__ Vh,
                                                bf16* __restrict__ Ob, int seq, char* lds, int nrows) {
  using St = Stage<bf16>; using SQ = Stage<TQ>;
  const int tid = threadIdx.x, wid = tid >> 6, lane = tid & 63, r32 = lane & 31, hi = lane >> 5;
  bf16* V_lds = (bf16*)lds; bf16* K_lds = (bf16*)(lds + 2 * SHM_V);
  float* ws = (float*)(lds + 2 * SHM_V + 2 * SHM_K) + wid * 64; float* li_l = ws; float* al_l = ws + 32;
  float m_reg = -1e30f, l_reg = 0; f32x16 o[4] = {}; bf16x8 qr[8];
  const int qrow_ = min(wid * QBLK + r32, nrows - 1); const TQ* Qw = Qb + (long)qrow_ * LDQ + hi * 8;
#pragma unroll
  for (int d0 = 0; d0 < 8; ++d0) qr[d0] = SQ::tobf(SQ::ld8(Qw + d0 * 16));
  const int sr = tid >> 4, sc = (tid & 15) * 8, vst0 = v_st(sr, sc), vst1 = v_st(32 + sr, sc);
  const int vb0 = (int)(uintptr_t)V_lds + v_rd_base(lane);
  struct { typename St::T vs0, vs1, ks0, ks1; } sr_[SDEPTH];
#define SLOAD(i, k0) do { sr_[i].vs0 = St::ld8(&Vh[(long)((k0) + sr) * LDK + sc]); sr_[i].vs1 = St::ld8(&Vh[(long)((k0) + 32 + sr) * LDK + sc]); \
    sr_[i].ks0 = St::ld8(&Kh[(long)((k0) + sr) * LDK + sc]); sr_[i].ks1 = St::ld8(&Kh[(long)((k0) + 32 + sr) * LDK + sc]); } while (0)
#define SWRITE(b, i) do { *(bf16x8*)((char*)V_lds + (b) * SHM_V + vst0) = St::tobf(sr_[i].vs0);          \
    *(bf16x8*)((char*)V_lds + (b) * SHM_V + vst1) = St::tobf(sr_[i].vs1); int kc = sc * 2;               \
    *(bf16x8*)((char*)K_lds + (b) * SHM_K + KSWZ(sr, kc)) = St::tobf(sr_[i].ks0);                       \
    *(bf16x8*)((char*)K_lds + (b) * SHM_K + KSWZ(32 + sr, kc)) = St::tobf(sr_[i].ks1); } while (0)
#define SWAIT() do { if constexpr (SDEPTH == 2) asm volatile("s_waitcnt vmcnt(4)" ::: "memory"); else asm volatile("s_waitcnt vmcnt(0)" ::: "memory"); } while (0)
#define RESC(a) do { if (__any((a) < 1.f)) { if (hi == 0) al_l[r32] = (a); asm volatile("s_waitcnt lgkmcnt(0)" ::: "memory"); \
    for (int d = 0; d < 4; ++d) for (int r = 0; r < 16; ++r) o[d][r] *= al_l[crow(r, hi)]; } } while (0)
  f32x16 pA0, pA1, pB0, pB1; float mnA, mnB, alA, alB; bf16x8 pa0, pa1, pa2, pa3; const int NT = seq / KVBLK;
  constexpr int SE = 0, SO = SDEPTH - 1;
  SLOAD(SE, 0); asm volatile("s_waitcnt vmcnt(0)" ::: "memory"); SWRITE(0, SE); __syncthreads();
  qkt(pA0, pA1, K_lds, qr, r32, hi); partialSM(pA0, pA1, m_reg, mnA, alA);
  SLOAD(SO, KVBLK); if constexpr (SDEPTH == 2) { if (2 < NT) SLOAD(SE, 2 * KVBLK); }
  SWAIT(); SWRITE(1, SO); __syncthreads();
  for (int j = 1; j + 1 < NT; j += 2) {
    SBAR(); qkt(pB0, pB1, (bf16*)((char*)K_lds + SHM_K), qr, r32, hi);
    finishSM(pA0, pA1, alA, l_reg, pa0, pa1, pa2, pa3); SBAR();
    SLOAD(SO, (j + SDEPTH) * KVBLK); SBAR();
    pv_d0(o, vb0, pa0, pa1, pa2, pa3); partialSM(pB0, pB1, m_reg, mnB, alB);
    __syncthreads(); SWAIT(); SWRITE(0, SE);
    RESC(alB); __syncthreads();
    SBAR(); qkt(pA0, pA1, K_lds, qr, r32, hi);
    finishSM(pB0, pB1, alB, l_reg, pa0, pa1, pa2, pa3); SBAR();
    if (SDEPTH == 1 || j + 3 < NT) SLOAD(SE, (j + 1 + SDEPTH) * KVBLK); SBAR();
    pv_d0(o, vb0 + (int)SHM_V, pa0, pa1, pa2, pa3); partialSM(pA0, pA1, m_reg, mnA, alA);
    __syncthreads(); SWAIT(); SWRITE(1, SO);
    RESC(alA); __syncthreads();
  }
  SBAR(); qkt(pB0, pB1, (bf16*)((char*)K_lds + SHM_K), qr, r32, hi);
  finishSM(pA0, pA1, alA, l_reg, pa0, pa1, pa2, pa3); SBAR();
  pv_d0(o, vb0, pa0, pa1, pa2, pa3); partialSM(pB0, pB1, m_reg, mnB, alB);
  __syncthreads(); RESC(alB);
  finishSM(pB0, pB1, alB, l_reg, pa0, pa1, pa2, pa3); SBAR();
  pv_d0(o, vb0 + (int)SHM_V, pa0, pa1, pa2, pa3);
  if (hi == 0) li_l[r32] = l_reg; asm volatile("s_waitcnt lgkmcnt(0)" ::: "memory");
  float rli[16];
#pragma unroll
  for (int r = 0; r < 16; ++r) rli[r] = __builtin_amdgcn_rcpf(li_l[crow(r, hi)]);
  bf16* Ow = Ob + (long)(wid * QBLK) * LDO;
#pragma unroll
  for (int r = 0; r < 16; ++r) { int orow = crow(r, hi);
    if (wid * QBLK + orow < nrows) for (int d0 = 0; d0 < 4; ++d0) Ow[(long)orow * LDO + d0 * 32 + r32] = __float2bfloat16(o[d0][r] * rli[r]); }
#undef SLOAD
#undef SWRITE
#undef SWAIT
#undef RESC
}

__global__ __launch_bounds__(64 * NW, 1)
void attn_fwd_dense(const bf16* __restrict__ Q, const bf16* __restrict__ K, const bf16* __restrict__ V, bf16* __restrict__ O) {
  extern __shared__ __attribute__((aligned(16))) char shm[];
  const int bid = blockIdx.x;
  const long head = (bid & 7) + 8 * (bid >> 4); const int half = (bid >> 3) & 1;
  const long q0 = (head * 496 + half * 256) * D, k0 = head * 1024 * (long)D;
  attn_dense_body<bf16>(Q + q0, K + k0, V + k0, O + q0, 1024, shm, half ? 240 : 256);
}

typedef unsigned u32x4_ __attribute__((ext_vector_type(4)));
template<int MODE>
__global__ __launch_bounds__(256) void probe(u32x4_* p, long n16_per_wg, unsigned* sink, int amp) {
  __syncthreads();
  unsigned long long t0 = __builtin_amdgcn_s_memrealtime();
  u32x4_* q = p + blockIdx.x * n16_per_wg + threadIdx.x;
  u32x4_ acc = {0,0,0,0};
  if (MODE < 2) {
    for (long i = 0; i < n16_per_wg; i += 256 * 8) {
      u32x4_ v[8];
      #pragma unroll
      for (int j = 0; j < 8; ++j) { long k = i + j * 256; if (k + threadIdx.x < n16_per_wg) v[j] = MODE == 1 ? __builtin_nontemporal_load(q + k) : q[k]; else v[j] = acc; }
      #pragma unroll
      for (int j = 0; j < 8; ++j) acc ^= v[j];
    }
    if (acc[0] == 0x12345678 && acc[1] == 0x9abcdef0 && acc[2] == 0x31415926 && acc[3] == 0x27182818) sink[0] = 1;
  } else {
    for (long i = threadIdx.x; i < n16_per_wg; i += 256) { if (MODE == 3) __builtin_nontemporal_store(acc, q + i - threadIdx.x); else q[i - threadIdx.x] = acc; }
  }
  asm volatile("s_waitcnt vmcnt(0)" ::: "memory");
  __syncthreads();
  unsigned long long t1 = __builtin_amdgcn_s_memrealtime();
  unsigned long long tend = t0 + (t1 - t0) * amp;
  while (__builtin_amdgcn_s_memrealtime() < tend) __builtin_amdgcn_s_sleep(8);
}
extern "C" void kernel_launch(void* const* d_in, const int* in_sizes, int n_in, void* d_out, int out_size, void* d_ws, size_t ws_size, hipStream_t stream) {
  hipFuncSetAttribute((const void*)attn_fwd_dense, hipFuncAttributeMaxDynamicSharedMemorySize, (int)SHM_ATTN);
  hipLaunchKernelGGL(probe<2>, dim3(256), dim3(256), 0, stream, (u32x4_*)(d_out), (long)(out_size) * 2 / 16 / 256, (unsigned*)d_ws, 20);
  hipLaunchKernelGGL(attn_fwd_dense, dim3(512), dim3(64 * NW), SHM_ATTN, stream,
                     (const bf16*)d_in[0], (const bf16*)d_in[1], (const bf16*)d_in[2], (bf16*)d_out);
}
```

```cpp
#include <hip/hip_runtime.h>
#include <hip/hip_bf16.h>

typedef short bf16x8_t __attribute__((ext_vector_type(8)));
typedef float f32x4_t __attribute__((ext_vector_type(4)));
typedef unsigned u32x4_t __attribute__((ext_vector_type(4)));

typedef float f32x2_t __attribute__((ext_vector_type(2)));
typedef __bf16 bf16x2_t __attribute__((ext_vector_type(2)));
__device__ __forceinline__ unsigned pack_bf16(float lo, float hi) {
  const f32x2_t x = {lo, hi};
  return __builtin_bit_cast(unsigned, __builtin_convertvector(x, bf16x2_t));
}

typedef __bf16 bf16v8_t __attribute__((ext_vector_type(8)));
#define MFMA16(a, b, c) __builtin_amdgcn_mfma_f32_16x16x32_bf16(__builtin_bit_cast(bf16v8_t, a), __builtin_bit_cast(bf16v8_t, b), c, 0, 0, 0)
__device__ __forceinline__ void true_max_rows(const unsigned short* __restrict__ q, const unsigned short* __restrict__ k,
                                              const unsigned short* __restrict__ v, unsigned short* __restrict__ o, int ntile, unsigned lane) {
  const unsigned c = lane & 15u, g = lane >> 4;
  const float cs = 0.12751743082f;
  for (int jb = 0; jb < 8; jb += 4) {
    bf16x8_t qf[4][4];
    f32x4_t acc[4][8];
    float m[4], l[4];
#pragma unroll
    for (int u = 0; u < 4; ++u) {
      const int j = jb + u < ntile ? jb + u : ntile - 1;
#pragma unroll
      for (int ks = 0; ks < 4; ++ks) qf[u][ks] = *(const bf16x8_t*)(q + (64 * j + c) * 128 + 32 * ks + 8 * g);
#pragma unroll
      for (int dt = 0; dt < 8; ++dt) acc[u][dt] = f32x4_t{0.f, 0.f, 0.f, 0.f};
      m[u] = -INFINITY; l[u] = 0.f;
    }
    for (int t = 0; t < 32; ++t) {
      bf16x8_t kf[2][4], vr[8], vf[8];
#pragma unroll
      for (int e = 0; e < 8; ++e) {
        kf[e >> 2][e & 3] = *(const bf16x8_t*)(k + (32 * t + 16 * (e >> 2) + c) * 128 + 32 * (e & 3) + 8 * g);
        vr[e] = *(const bf16x8_t*)(v + (32 * t + 16 * (e >> 2) + 4 * g + (e & 3)) * 128 + 8 * c);
      }
#pragma unroll
      for (int dt = 0; dt < 8; ++dt)
#pragma unroll
        for (int e = 0; e < 8; ++e) vf[dt][e] = vr[e][dt];
#pragma unroll
      for (int u = 0; u < 4; ++u) {
        f32x4_t s[2] = {f32x4_t{0.f, 0.f, 0.f, 0.f}, f32x4_t{0.f, 0.f, 0.f, 0.f}};
#pragma unroll
        for (int e = 0; e < 8; ++e) s[e >> 2] = MFMA16(kf[e >> 2][e & 3], qf[u][e & 3], s[e >> 2]);
        float mx = fmaxf(fmaxf(fmaxf(s[0][0], s[0][1]), fmaxf(s[0][2], s[0][3])), fmaxf(fmaxf(s[1][0], s[1][1]), fmaxf(s[1][2], s[1][3])));
        mx = fmaxf(mx, __shfl_xor(mx, 16)); mx = fmaxf(mx, __shfl_xor(mx, 32));
        const float mn = fmaxf(m[u], mx);
        const float alpha = __builtin_amdgcn_exp2f((m[u] - mn) * cs);
        m[u] = mn;
        float p[8], ps = 0.f;
#pragma unroll
        for (int e = 0; e < 8; ++e) { p[e] = __builtin_amdgcn_exp2f((s[e >> 2][e & 3] - mn) * cs); ps += p[e]; }
        l[u] = l[u] * alpha + ps;
        const u32x4_t pw = {pack_bf16(p[0], p[1]), pack_bf16(p[2], p[3]), pack_bf16(p[4], p[5]), pack_bf16(p[6], p[7])};
#pragma unroll
        for (int dt = 0; dt < 8; ++dt) acc[u][dt] = MFMA16(vf[dt], pw, acc[u][dt] * alpha);
      }
    }
#pragma unroll
    for (int u = 0; u < 4; ++u) {
      float lt = l[u];
      lt += __shfl_xor(lt, 16); lt += __shfl_xor(lt, 32);
      const float r = 1.0f / lt;
      if (jb + u < ntile) {
#pragma unroll
        for (int e = 0; e < 4; ++e) {
          const u32x4_t w = {pack_bf16(acc[u][0][e] * r, acc[u][1][e] * r), pack_bf16(acc[u][2][e] * r, acc[u][3][e] * r),
                             pack_bf16(acc[u][4][e] * r, acc[u][5][e] * r), pack_bf16(acc[u][6][e] * r, acc[u][7][e] * r)};
          *(u32x4_t*)(o + (64 * (jb + u) + c) * 128 + 8 * (4 * g + e)) = w;
        }
      }
    }
  }
}

__global__ __launch_bounds__(256, 1) void mha_fwd(const unsigned short* __restrict__ q, const unsigned short* __restrict__ k,
                                                  const unsigned short* __restrict__ v, unsigned short* __restrict__ o) {
  __shared__ __attribute__((aligned(16))) char lds[163840];
  const unsigned wid = __builtin_amdgcn_readfirstlane(threadIdx.x >> 6);
  unsigned flag;
  asm volatile(
    "LBEG_%=:\n"
    "s_mov_b32 s52, %[wid]\n"
    "v_mbcnt_lo_u32_b32 v0, -1, 0\n"
    "v_mbcnt_hi_u32_b32 v0, -1, v0\n"
    "s_load_dwordx8 s[16:23], %[ka], 0x0\n"
    "s_getpc_b64 s[24:25]\n"
    "LPC%=_0:\n"
    "s_lshr_b32 s68, %[blk], 3\n"
    "s_lshl_b32 s68, s68, 2\n"
    "s_add_u32 s68, s68, s52\n"
    "s_lshl_b32 s68, s68, 10\n"
    "s_sub_u32 s24, s24, LPC%=_0-LBEG_%=\n"
    "s_subb_u32 s25, s25, 0\n"
    "v_min_u32 v5, 7, v0\n"
    "v_lshl_add_u32 v5, v5, 7, s68\n"
    "v_min_u32 v5, LEND_%=-LBEG_%=-4, v5\n"
    "global_load_dword v6, v5, s[24:25]\n"
    "v_lshrrev_b32 v1, 3, v0\n"
    "v_and_b32 v2, 7, v0\n"
    "v_and_b32 v3, 6, v1\n"
    "v_xor_b32 v2, v2, v3\n"
    "v_lshlrev_b32 v2, 4, v2\n"
    "v_lshl_add_u32 v252, v1, 8, v2\n"
    "s_lshr_b32 s68, s52, 1\n"
    "s_lshl_b32 s68, s68, 12\n"
    "s_and_b32 s69, s52, 1\n"
    "s_lshl_b32 s64, s69, 10\n"
    "s_add_u32 s64, s64, s68\n"
    "s_lshl_b32 s65, s69, 7\n"
    "s_add_u32 s65, s65, s68\n"
    "s_lshl_b32 s66, s52, 13\n"
    "s_lshl_b32 s67, s52, 12\n"
    "s_mov_b32 s53, 0x3e0293ee\n"
    "s_mov_b32 s34, 0x2b800000\n"
    "s_mov_b32 s35, 0x53800000\n"
    "s_mov_b64 s[62:63], 0\n"
    "v_and_b32 v1, 15, v0\n"
    "v_lshrrev_b32 v2, 4, v0\n"
    "v_and_b32 v3, 6, v1\n"
    "v_xor_b32 v3, v3, v2\n"
    "v_lshlrev_b32 v3, 4, v3\n"
    "v_and_b32 v4, 7, v1\n"
    "v_lshl_add_u32 v3, v4, 7, v3\n"
    "v_lshrrev_b32 v4, 3, v1\n"
    "v_lshl_add_u32 v248, v4, 11, v3\n"
    "v_xor_b32 v249, 64, v248\n"
    "v_lshlrev_b32 v3, 4, v2\n"
    "v_lshl_add_u32 v255, v1, 8, v3\n"
    "v_bfe_u32 v1, v0, 2, 3\n"
    "v_and_b32 v2, 3, v0\n"
    "v_and_b32 v3, 6, v1\n"
    "v_xor_b32 v3, v3, v2\n"
    "v_lshlrev_b32 v3, 4, v3\n"
    "v_lshl_add_u32 v3, v1, 7, v3\n"
    "v_lshrrev_b32 v4, 5, v0\n"
    "v_lshl_add_u32 v3, v4, 11, v3\n"
    "v_add_u32 v250, 0x2000, v3\n"
    "v_xor_b32 v251, 64, v250\n"
    "v_mov_b32 v240, 0\n"
    "v_mov_b32 v241, 0\n"
    "v_mov_b32 v242, 0\n"
    "v_mov_b32 v243, 0\n"
    "v_mov_b32 v244, 0\n"
    "v_mov_b32 v245, 0\n"
    "v_mov_b32 v246, 0\n"
    "v_mov_b32 v247, 0\n"
    "v_bfe_u32 v1, v0, 2, 2\n"
    "v_lshrrev_b32 v2, 4, v0\n"
    "v_cmp_eq_u32 vcc, v1, v2\n"
    "v_and_b32 v3, 1, v0\n"
    "v_lshlrev_b32 v3, 4, v3\n"
    "v_mov_b32 v1, 0x3e03\n"
    "v_mov_b32 v2, 0xb9d8\n"
    "v_lshlrev_b32 v1, v3, v1\n"
    "v_lshlrev_b32 v2, v3, v2\n"
    "v_cndmask_b32 v1, 0, v1, vcc\n"
    "v_cndmask_b32 v2, 0, v2, vcc\n"
    "v_and_b32 v3, 2, v0\n"
    "v_cmp_eq_u32 vcc, 0, v3\n"
    "s_nop 1\n"
    "v_cndmask_b32 v162, 0, v1, vcc\n"
    "v_cndmask_b32 v163, v1, 0, vcc\n"
    "v_cndmask_b32 v166, 0, v2, vcc\n"
    "v_cndmask_b32 v167, v2, 0, vcc\n"
    "v_mov_b32 v160, 0\n"
    "v_mov_b32 v161, 0\n"
    "v_mov_b32 v164, 0\n"
    "v_mov_b32 v165, 0\n"
    "v_mov_b32 v168, 0\n"
    "v_mov_b32 v169, 0\n"
    "s_and_b32 s26, %[blk], 7\n"
    "s_lshl_b32 s26, s26, 5\n"
    "s_lshr_b32 s27, %[blk], 3\n"
    "s_or_b32 s26, s26, s27\n"
    "s_waitcnt lgkmcnt(0)\n"
    "s_mul_i32 s68, s26, 126976\n"
    "s_lshl_b32 s69, s26, 18\n"
    "s_add_u32 s40, s18, s69\n"
    "s_addc_u32 s41, s19, 0\n"
    "s_mov_b32 s42, 0x40000\n"
    "s_mov_b32 s43, 0x20000\n"
    "s_and_b32 s41, s41, 0xffff\n"
    "s_add_u32 s36, s16, s68\n"
    "s_addc_u32 s37, s17, 0\n"
    "s_mov_b32 s38, 126976\n"
    "s_mov_b32 s39, 0x20000\n"
    "s_and_b32 s37, s37, 0xffff\n"
    "s_add_u32 s44, s20, s69\n"
    "s_addc_u32 s45, s21, 0\n"
    "s_mov_b32 s46, 0x40000\n"
    "s_mov_b32 s47, 0x20000\n"
    "s_and_b32 s45, s45, 0xffff\n"
    "s_add_u32 s48, s22, s68\n"
    "s_addc_u32 s49, s23, 0\n"
    "s_mov_b32 s50, 126976\n"
    "s_mov_b32 s51, 0x20000\n"
    "s_and_b32 s49, s49, 0xffff\n"
    "s_cmp_eq_u32 s52, 3\n"
    "s_cbranch_scc1 LW3_%=\n"
    "s_add_u32 s69, s67, 0\n"
    "s_add_u32 m0, s66, 131072\n"
    "s_nop 0\n"
    "buffer_load_dwordx4 v252, s[36:39], s69 offen offset:0 nt lds\n"
    "buffer_load_dwordx4 v252, s[36:39], s69 offen offset:2048 nt lds\n"
    "s_add_u32 m0, s66, 131968\n"
    "s_nop 0\n"
    "buffer_load_dwordx4 v252, s[36:39], s69 offen offset:128 nt lds\n"
    "buffer_load_dwordx4 v252, s[36:39], s69 offen offset:2176 nt lds\n"
    "s_add_u32 m0, s64, 0\n"
    "s_add_u32 s69, s65, 0\n"
    "buffer_load_dwordx4 v252, s[40:43], s69 offen offset:0 nt lds\n"
    "buffer_load_dwordx4 v252, s[40:43], s69 offen offset:2048 nt lds\n"
    "s_sleep 12\n"
    "s_add_u32 m0, s64, 8192\n"
    "s_add_u32 s69, s65, 0\n"
    "buffer_load_dwordx4 v252, s[44:47], s69 offen offset:0 nt lds\n"
    "buffer_load_dwordx4 v252, s[44:47], s69 offen offset:2048 nt lds\n"
    "s_add_u32 s69, s67, 16384\n"
    "s_add_u32 m0, s66, 135168\n"
    "s_nop 0\n"
    "buffer_load_dwordx4 v252, s[36:39], s69 offen offset:0 nt lds\n"
    "buffer_load_dwordx4 v252, s[36:39], s69 offen offset:2048 nt lds\n"
    "s_add_u32 m0, s66, 136064\n"
    "s_nop 0\n"
    "buffer_load_dwordx4 v252, s[36:39], s69 offen offset:128 nt lds\n"
    "buffer_load_dwordx4 v252, s[36:39], s69 offen offset:2176 nt lds\n"
    "s_nop 0\n"
    "s_waitcnt vmcnt(6)\n"
    "s_barrier\n"
    "s_waitcnt vmcnt(63)\n"
    "s_add_u32 s68, s66, 131072\n"
    "v_add_u32 v253, s68, v248\n"
    "s_add_u32 s68, s66, 131072\n"
    "v_add_u32 v254, s68, v249\n"
    "ds_read_b128 a[0:3], v253 offset:0\n"
    "ds_read_b128 a[4:7], v254 offset:0\n"
    "ds_read_b128 a[8:11], v253 offset:1024\n"
    "ds_read_b128 a[12:15], v254 offset:1024\n"
    "s_add_u32 s69, s67, 32768\n"
    "s_add_u32 m0, s66, 65536\n"
    "s_nop 0\n"
    "buffer_load_dwordx4 v252, s[36:39], s69 offen offset:0 nt lds\n"
    "buffer_load_dwordx4 v252, s[36:39], s69 offen offset:2048 nt lds\n"
    "s_add_u32 m0, s66, 66432\n"
    "s_nop 0\n"
    "buffer_load_dwordx4 v252, s[36:39], s69 offen offset:128 nt lds\n"
    "buffer_load_dwordx4 v252, s[36:39], s69 offen offset:2176 nt lds\n"
    "s_add_u32 m0, s64, 16384\n"
    "s_add_u32 s69, s65, 8192\n"
    "buffer_load_dwordx4 v252, s[40:43], s69 offen offset:0 nt lds\n"
    "buffer_load_dwordx4 v252, s[40:43], s69 offen offset:2048 nt lds\n"
    "s_add_u32 m0, s64, 24576\n"
    "s_add_u32 s69, s65, 8192\n"
    "buffer_load_dwordx4 v252, s[44:47], s69 offen offset:0 nt lds\n"
    "buffer_load_dwordx4 v252, s[44:47], s69 offen offset:2048 nt lds\n"
    "ds_read_b128 a[192:195], v248 offset:0\n"
    "ds_read_b128 a[196:199], v249 offset:0\n"
    "ds_read_b128 a[200:203], v248 offset:1024\n"
    "ds_read_b128 a[204:207], v249 offset:1024\n"
    "ds_read_b128 a[208:211], v248 offset:4096\n"
    "ds_read_b128 a[212:215], v249 offset:4096\n"
    "ds_read_b128 a[216:219], v248 offset:5120\n"
    "ds_read_b128 a[220:223], v249 offset:5120\n"
    "s_waitcnt lgkmcnt(11)\n"
    "v_mfma_f32_16x16x32_bf16 v[176:179], v[162:165], a[0:3], 0\n"
    "s_mov_b32 s55, 32768\n"
    "v_mfma_f32_16x16x32_bf16 v[176:179], v[166:169], a[0:3], v[176:179]\n"
    "v_mfma_f32_16x16x32_bf16 v[180:183], v[160:163], a[0:3], 0\n"
    "v_mfma_f32_16x16x32_bf16 v[180:183], v[164:167], a[0:3], v[180:183]\n"
    "s_waitcnt lgkmcnt(10)\n"
    "v_mfma_f32_16x16x32_bf16 v[184:187], v[162:165], a[4:7], 0\n"
    "v_cvt_pk_bf16_f32 v176, v176, v177\n"
    "v_cvt_pk_bf16_f32 v178, v178, v179\n"
    "v_accvgpr_write_b32 a0, v176\n"
    "v_mfma_f32_16x16x32_bf16 v[184:187], v[166:169], a[4:7], v[184:187]\n"
    "v_accvgpr_write_b32 a1, v178\n"
    "v_mfma_f32_16x16x32_bf16 v[188:191], v[160:163], a[4:7], 0\n"
    "v_cvt_pk_bf16_f32 v180, v180, v181\n"
    "v_cvt_pk_bf16_f32 v182, v182, v183\n"
    "v_accvgpr_write_b32 a2, v180\n"
    "v_mfma_f32_16x16x32_bf16 v[188:191], v[164:167], a[4:7], v[188:191]\n"
    "s_waitcnt lgkmcnt(9)\n"
    "v_accvgpr_write_b32 a3, v182\n"
    "v_mfma_f32_16x16x32_bf16 v[176:179], v[162:165], a[8:11], 0\n"
    "v_cvt_pk_bf16_f32 v184, v184, v185\n"
    "v_cvt_pk_bf16_f32 v186, v186, v187\n"
    "v_accvgpr_write_b32 a4, v184\n"
    "v_mfma_f32_16x16x32_bf16 v[176:179], v[166:169], a[8:11], v[176:179]\n"
    "v_accvgpr_write_b32 a5, v186\n"
    "v_mfma_f32_16x16x32_bf16 v[180:183], v[160:163], a[8:11], 0\n"
    "v_cvt_pk_bf16_f32 v188, v188, v189\n"
    "v_cvt_pk_bf16_f32 v190, v190, v191\n"
    "v_accvgpr_write_b32 a6, v188\n"
    "v_mfma_f32_16x16x32_bf16 v[180:183], v[164:167], a[8:11], v[180:183]\n"
    "s_waitcnt lgkmcnt(8)\n"
    "v_accvgpr_write_b32 a7, v190\n"
    "v_mfma_f32_16x16x32_bf16 v[184:187], v[162:165], a[12:15], 0\n"
    "v_cvt_pk_bf16_f32 v176, v176, v177\n"
    "v_cvt_pk_bf16_f32 v178, v178, v179\n"
    "v_accvgpr_write_b32 a8, v176\n"
    "v_mfma_f32_16x16x32_bf16 v[184:187], v[166:169], a[12:15], v[184:187]\n"
    "v_accvgpr_write_b32 a9, v178\n"
    "v_mfma_f32_16x16x32_bf16 v[188:191], v[160:163], a[12:15], 0\n"
    "v_cvt_pk_bf16_f32 v180, v180, v181\n"
    "v_cvt_pk_bf16_f32 v182, v182, v183\n"
    "v_accvgpr_write_b32 a10, v180\n"
    "v_mfma_f32_16x16x32_bf16 v[188:191], v[164:167], a[12:15], v[188:191]\n"
    "s_waitcnt lgkmcnt(7)\n"
    "s_waitcnt vmcnt(8)\n"
    "s_add_u32 s68, s66, 135168\n"
    "v_add_u32 v253, s68, v248\n"
    "s_add_u32 s68, s66, 135168\n"
    "v_add_u32 v254, s68, v249\n"
    "ds_read_b128 a[16:19], v253 offset:0\n"
    "ds_read_b128 a[20:23], v254 offset:0\n"
    "ds_read_b128 a[24:27], v253 offset:1024\n"
    "ds_read_b128 a[28:31], v254 offset:1024\n"
    "s_add_u32 s69, s67, 49152\n"
    "s_add_u32 m0, s66, 69632\n"
    "s_nop 0\n"
    "buffer_load_dwordx4 v252, s[36:39], s69 offen offset:0 nt lds\n"
    "buffer_load_dwordx4 v252, s[36:39], s69 offen offset:2048 nt lds\n"
    "s_add_u32 m0, s66, 70528\n"
    "s_nop 0\n"
    "buffer_load_dwordx4 v252, s[36:39], s69 offen offset:128 nt lds\n"
    "buffer_load_dwordx4 v252, s[36:39], s69 offen offset:2176 nt lds\n"
    "v_mfma_f32_16x16x32_bf16 v[216:219], a[192:195], a[0:3], 0\n"
    "s_waitcnt lgkmcnt(10)\n"
    "v_accvgpr_write_b32 a11, v182\n"
    "v_cvt_pk_bf16_f32 v184, v184, v185\n"
    "v_cvt_pk_bf16_f32 v186, v186, v187\n"
    "v_mfma_f32_16x16x32_bf16 v[216:219], a[196:199], a[4:7], v[216:219]\n"
    "s_waitcnt lgkmcnt(9)\n"
    "v_accvgpr_write_b32 a12, v184\n"
    "v_accvgpr_write_b32 a13, v186\n"
    "v_mfma_f32_16x16x32_bf16 v[216:219], a[200:203], a[8:11], v[216:219]\n"
    "s_waitcnt lgkmcnt(8)\n"
    "v_cvt_pk_bf16_f32 v188, v188, v189\n"
    "v_cvt_pk_bf16_f32 v190, v190, v191\n"
    "v_accvgpr_write_b32 a14, v188\n"
    "v_accvgpr_write_b32 a15, v190\n"
    "s_nop 1\n"
    "v_mfma_f32_16x16x32_bf16 v[216:219], a[204:207], a[12:15], v[216:219]\n"
    "s_waitcnt lgkmcnt(3)\n"
    "s_waitcnt vmcnt(63)\n"
    "s_barrier\n"
    "ds_read_b64_tr_b16 v[192:193], v250 offset:0\n"
    "ds_read_b64_tr_b16 v[194:195], v250 offset:4096\n"
    "ds_read_b64_tr_b16 v[196:197], v250 offset:8\n"
    "ds_read_b64_tr_b16 v[198:199], v250 offset:4104\n"
    "ds_read_b64_tr_b16 v[200:201], v251 offset:0\n"
    "ds_read_b64_tr_b16 v[202:203], v251 offset:4096\n"
    "ds_read_b64_tr_b16 v[204:205], v251 offset:8\n"
    "ds_read_b64_tr_b16 v[206:207], v251 offset:4104\n"
    "ds_read_b64_tr_b16 v[208:209], v250 offset:1024\n"
    "ds_read_b64_tr_b16 v[210:211], v250 offset:5120\n"
    "ds_read_b64_tr_b16 v[212:213], v250 offset:1032\n"
    "ds_read_b64_tr_b16 v[214:215], v250 offset:5128\n"
    "ds_read_b64_tr_b16 a[248:249], v251 offset:1024\n"
    "ds_read_b64_tr_b16 a[250:251], v251 offset:5120\n"
    "ds_read_b64_tr_b16 a[252:253], v251 offset:1032\n"
    "ds_read_b64_tr_b16 a[254:255], v251 offset:5128\n"
    "v_mfma_f32_16x16x32_bf16 v[176:179], v[162:165], a[16:19], 0\n"
    "v_mfma_f32_16x16x32_bf16 v[176:179], v[166:169], a[16:19], v[176:179]\n"
    "v_mfma_f32_16x16x32_bf16 v[180:183], v[160:163], a[16:19], 0\n"
    "v_exp_f32 v216, v216\n"
    "v_mfma_f32_16x16x32_bf16 v[180:183], v[164:167], a[16:19], v[180:183]\n"
    "v_exp_f32 v217, v217\n"
    "v_add_f32 v240, v240, v216\n"
    "v_mfma_f32_16x16x32_bf16 v[220:223], a[208:211], a[0:3], 0\n"
    "v_cvt_pk_bf16_f32 v176, v176, v177\n"
    "v_cvt_pk_bf16_f32 v178, v178, v179\n"
    "v_accvgpr_write_b32 a16, v176\n"
    "v_mfma_f32_16x16x32_bf16 v[220:223], a[212:215], a[4:7], v[220:223]\n"
    "v_accvgpr_write_b32 a17, v178\n"
    "v_exp_f32 v218, v218\n"
    "v_mfma_f32_16x16x32_bf16 v[220:223], a[216:219], a[8:11], v[220:223]\n"
    "v_cvt_pk_bf16_f32 v180, v180, v181\n"
    "v_cvt_pk_bf16_f32 v182, v182, v183\n"
    "v_accvgpr_write_b32 a18, v180\n"
    "v_mfma_f32_16x16x32_bf16 v[220:223], a[220:223], a[12:15], v[220:223]\n"
    "s_waitcnt lgkmcnt(15)\n"
    "v_accvgpr_write_b32 a19, v182\n"
    "v_exp_f32 v219, v219\n"
    "v_mfma_f32_16x16x32_bf16 v[184:187], v[162:165], a[20:23], 0\n"
    "v_cvt_pk_bf16_f32 v232, v216, v217\n"
    "v_cvt_pk_bf16_f32 v233, v218, v219\n"
    "v_add_f32 v240, v240, v217\n"
    "v_mfma_f32_16x16x32_bf16 v[184:187], v[166:169], a[20:23], v[184:187]\n"
    "v_add_f32 v240, v240, v218\n"
    "v_add_f32 v240, v240, v219\n"
    "v_mfma_f32_16x16x32_bf16 v[188:191], v[160:163], a[20:23], 0\n"
    "v_exp_f32 v220, v220\n"
    "v_mfma_f32_16x16x32_bf16 v[188:191], v[164:167], a[20:23], v[188:191]\n"
    "v_exp_f32 v221, v221\n"
    "v_add_f32 v240, v240, v220\n"
    "v_mfma_f32_16x16x32_bf16 v[176:179], v[162:165], a[24:27], 0\n"
    "v_cvt_pk_bf16_f32 v184, v184, v185\n"
    "v_cvt_pk_bf16_f32 v186, v186, v187\n"
    "v_accvgpr_write_b32 a20, v184\n"
    "v_mfma_f32_16x16x32_bf16 v[176:179], v[166:169], a[24:27], v[176:179]\n"
    "v_accvgpr_write_b32 a21, v186\n"
    "v_exp_f32 v222, v222\n"
    "v_mfma_f32_16x16x32_bf16 v[180:183], v[160:163], a[24:27], 0\n"
    "v_cvt_pk_bf16_f32 v188, v188, v189\n"
    "v_cvt_pk_bf16_f32 v190, v190, v191\n"
    "v_accvgpr_write_b32 a22, v188\n"
    "v_mfma_f32_16x16x32_bf16 v[180:183], v[164:167], a[24:27], v[180:183]\n"
    "v_accvgpr_write_b32 a23, v190\n"
    "v_exp_f32 v223, v223\n"
    "v_mfma_f32_16x16x32_bf16 v[184:187], v[162:165], a[28:31], 0\n"
    "v_cvt_pk_bf16_f32 v176, v176, v177\n"
    "v_cvt_pk_bf16_f32 v178, v178, v179\n"
    "v_accvgpr_write_b32 a24, v176\n"
    "v_mfma_f32_16x16x32_bf16 v[184:187], v[166:169], a[28:31], v[184:187]\n"
    "v_accvgpr_write_b32 a25, v178\n"
    "v_cvt_pk_bf16_f32 v234, v220, v221\n"
    "v_cvt_pk_bf16_f32 v235, v222, v223\n"
    "v_mfma_f32_16x16x32_bf16 v[188:191], v[160:163], a[28:31], 0\n"
    "v_cvt_pk_bf16_f32 v180, v180, v181\n"
    "v_cvt_pk_bf16_f32 v182, v182, v183\n"
    "v_accvgpr_write_b32 a26, v180\n"
    "v_mfma_f32_16x16x32_bf16 v[188:191], v[164:167], a[28:31], v[188:191]\n"
    "v_accvgpr_write_b32 a27, v182\n"
    "v_add_f32 v240, v240, v221\n"
    "v_add_f32 v240, v240, v222\n"
    "v_mfma_f32_16x16x32_bf16 v[224:227], a[192:195], a[16:19], 0\n"
    "v_add_f32 v240, v240, v223\n"
    "v_cvt_pk_bf16_f32 v184, v184, v185\n"
    "v_cvt_pk_bf16_f32 v186, v186, v187\n"
    "v_accvgpr_write_b32 a28, v184\n"
    "v_accvgpr_write_b32 a29, v186\n"
    "s_waitcnt vmcnt(4)\n"
    "s_barrier\n"
    "ds_read_b64_tr_b16 a[224:225], v250 offset:16384\n"
    "ds_read_b64_tr_b16 a[226:227], v250 offset:20480\n"
    "ds_read_b64_tr_b16 a[228:229], v250 offset:16392\n"
    "ds_read_b64_tr_b16 a[230:231], v250 offset:20488\n"
    "ds_read_b64_tr_b16 a[232:233], v251 offset:16384\n"
    "ds_read_b64_tr_b16 a[234:235], v251 offset:20480\n"
    "ds_read_b64_tr_b16 a[236:237], v251 offset:16392\n"
    "ds_read_b64_tr_b16 a[238:239], v251 offset:20488\n"
    "ds_read_b64_tr_b16 a[240:241], v250 offset:17408\n"
    "ds_read_b64_tr_b16 a[242:243], v250 offset:21504\n"
    "ds_read_b64_tr_b16 a[244:245], v250 offset:17416\n"
    "ds_read_b64_tr_b16 a[246:247], v250 offset:21512\n"
    "ds_read_b128 a[192:195], v248 offset:16384\n"
    "v_mfma_f32_16x16x32_bf16 v[224:227], a[196:199], a[20:23], v[224:227]\n"
    "ds_read_b128 a[196:199], v249 offset:16384\n"
    "v_mfma_f32_16x16x32_bf16 v[224:227], a[200:203], a[24:27], v[224:227]\n"
    "v_cvt_pk_bf16_f32 v188, v188, v189\n"
    "v_cvt_pk_bf16_f32 v190, v190, v191\n"
    "v_accvgpr_write_b32 a30, v188\n"
    "v_accvgpr_write_b32 a31, v190\n"
    "s_nop 1\n"
    "v_mfma_f32_16x16x32_bf16 v[224:227], a[204:207], a[28:31], v[224:227]\n"
    "ds_read_b128 a[200:203], v248 offset:17408\n"
    "ds_read_b128 a[204:207], v249 offset:17408\n"
    "v_mfma_f32_16x16x32_bf16 v[228:231], a[208:211], a[16:19], 0\n"
    "ds_read_b128 a[208:211], v248 offset:20480\n"
    "v_mfma_f32_16x16x32_bf16 v[228:231], a[212:215], a[20:23], v[228:231]\n"
    "ds_read_b128 a[212:215], v249 offset:20480\n"
    "v_mfma_f32_16x16x32_bf16 v[228:231], a[216:219], a[24:27], v[228:231]\n"
    "ds_read_b128 a[216:219], v248 offset:21504\n"
    "v_exp_f32 v224, v224\n"
    "v_mfma_f32_16x16x32_bf16 v[228:231], a[220:223], a[28:31], v[228:231]\n"
    "s_waitcnt lgkmcnt(15)\n"
    "ds_read_b128 a[220:223], v249 offset:21504\n"
    "v_exp_f32 v225, v225\n"
    "v_mfma_f32_16x16x32_bf16 a[128:131], v[192:195], v[232:235], 0\n"
    "v_exp_f32 v226, v226\n"
    "v_cvt_pk_bf16_f32 v236, v224, v225\n"
    "v_mfma_f32_16x16x32_bf16 a[132:135], v[196:199], v[232:235], 0\n"
    "v_exp_f32 v227, v227\n"
    "v_add_f32 v241, v241, v224\n"
    "v_mfma_f32_16x16x32_bf16 a[136:139], v[200:203], v[232:235], 0\n"
    "v_exp_f32 v228, v228\n"
    "v_cvt_pk_bf16_f32 v237, v226, v227\n"
    "v_mfma_f32_16x16x32_bf16 a[140:143], v[204:207], v[232:235], 0\n"
    "v_exp_f32 v229, v229\n"
    "v_add_f32 v241, v241, v225\n"
    "v_mfma_f32_16x16x32_bf16 a[144:147], v[208:211], v[232:235], 0\n"
    "v_exp_f32 v230, v230\n"
    "v_cvt_pk_bf16_f32 v238, v228, v229\n"
    "v_mfma_f32_16x16x32_bf16 a[148:151], v[212:215], v[232:235], 0\n"
    "v_exp_f32 v231, v231\n"
    "v_add_f32 v241, v241, v226\n"
    "v_mfma_f32_16x16x32_bf16 a[152:155], a[248:251], v[232:235], 0\n"
    "v_cvt_pk_bf16_f32 v239, v230, v231\n"
    "v_add_f32 v241, v241, v227\n"
    "v_add_f32 v241, v241, v228\n"
    "v_mfma_f32_16x16x32_bf16 a[156:159], a[252:255], v[232:235], 0\n"
    "s_waitcnt lgkmcnt(7)\n"
    "v_add_f32 v241, v241, v229\n"
    "v_add_f32 v241, v241, v230\n"
    "v_add_f32 v241, v241, v231\n"
    "v_mfma_f32_16x16x32_bf16 v[216:219], a[192:195], a[0:3], 0\n"
    "s_waitcnt lgkmcnt(6)\n"
    "v_mfma_f32_16x16x32_bf16 v[216:219], a[196:199], a[4:7], v[216:219]\n"
    "s_waitcnt lgkmcnt(5)\n"
    "v_mfma_f32_16x16x32_bf16 v[216:219], a[200:203], a[8:11], v[216:219]\n"
    "s_waitcnt lgkmcnt(4)\n"
    "v_mfma_f32_16x16x32_bf16 v[216:219], a[204:207], a[12:15], v[216:219]\n"
    "s_waitcnt lgkmcnt(3)\n"
    "v_mfma_f32_16x16x32_bf16 v[220:223], a[208:211], a[0:3], 0\n"
    "s_waitcnt lgkmcnt(2)\n"
    "v_mfma_f32_16x16x32_bf16 v[220:223], a[212:215], a[4:7], v[220:223]\n"
    "s_waitcnt lgkmcnt(1)\n"
    "v_mfma_f32_16x16x32_bf16 v[220:223], a[216:219], a[8:11], v[220:223]\n"
    "s_waitcnt lgkmcnt(0)\n"
    "v_exp_f32 v216, v216\n"
    "v_mfma_f32_16x16x32_bf16 v[220:223], a[220:223], a[12:15], v[220:223]\n"
    "v_exp_f32 v217, v217\n"
    "v_add_f32 v240, v240, v216\n"
    "v_mfma_f32_16x16x32_bf16 a[160:163], v[192:195], v[236:239], 0\n"
    "v_exp_f32 v218, v218\n"
    "v_cvt_pk_bf16_f32 v232, v216, v217\n"
    "v_mfma_f32_16x16x32_bf16 a[164:167], v[196:199], v[236:239], 0\n"
    "v_exp_f32 v219, v219\n"
    "v_add_f32 v240, v240, v217\n"
    "v_mfma_f32_16x16x32_bf16 a[168:171], v[200:203], v[236:239], 0\n"
    "v_exp_f32 v220, v220\n"
    "v_cvt_pk_bf16_f32 v233, v218, v219\n"
    "v_mfma_f32_16x16x32_bf16 a[172:175], v[204:207], v[236:239], 0\n"
    "v_exp_f32 v221, v221\n"
    "v_add_f32 v240, v240, v218\n"
    "v_mfma_f32_16x16x32_bf16 a[176:179], v[208:211], v[236:239], 0\n"
    "v_exp_f32 v222, v222\n"
    "v_cvt_pk_bf16_f32 v234, v220, v221\n"
    "v_mfma_f32_16x16x32_bf16 a[180:183], v[212:215], v[236:239], 0\n"
    "v_exp_f32 v223, v223\n"
    "v_add_f32 v240, v240, v219\n"
    "v_mfma_f32_16x16x32_bf16 a[184:187], a[248:251], v[236:239], 0\n"
    "ds_read_b64_tr_b16 a[248:249], v251 offset:17408\n"
    "ds_read_b64_tr_b16 a[250:251], v251 offset:21504\n"
    "v_cvt_pk_bf16_f32 v235, v222, v223\n"
    "v_mfma_f32_16x16x32_bf16 a[188:191], a[252:255], v[236:239], 0\n"
    "s_waitcnt vmcnt(63)\n"
    "s_add_u32 s68, s66, 65536\n"
    "v_add_u32 v253, s68, v248\n"
    "s_add_u32 s68, s66, 65536\n"
    "v_add_u32 v254, s68, v249\n"
    "ds_read_b128 a[32:35], v253 offset:0\n"
    "ds_read_b128 a[36:39], v254 offset:0\n"
    "ds_read_b128 a[40:43], v253 offset:1024\n"
    "ds_read_b128 a[44:47], v254 offset:1024\n"
    "s_add_u32 s69, s67, 65536\n"
    "s_add_u32 m0, s66, 131072\n"
    "s_nop 0\n"
    "buffer_load_dwordx4 v252, s[36:39], s69 offen offset:0 nt lds\n"
    "buffer_load_dwordx4 v252, s[36:39], s69 offen offset:2048 nt lds\n"
    "s_add_u32 m0, s66, 131968\n"
    "s_nop 0\n"
    "buffer_load_dwordx4 v252, s[36:39], s69 offen offset:128 nt lds\n"
    "buffer_load_dwordx4 v252, s[36:39], s69 offen offset:2176 nt lds\n"
    "v_mfma_f32_16x16x32_bf16 v[224:227], a[192:195], a[16:19], 0\n"
    "ds_read_b64_tr_b16 a[252:253], v251 offset:17416\n"
    "ds_read_b64_tr_b16 a[254:255], v251 offset:21512\n"
    "v_add_f32 v240, v240, v220\n"
    "v_mfma_f32_16x16x32_bf16 v[224:227], a[196:199], a[20:23], v[224:227]\n"
    "v_add_f32 v240, v240, v221\n"
    "v_add_f32 v240, v240, v222\n"
    "v_add_f32 v240, v240, v223\n"
    "v_mfma_f32_16x16x32_bf16 v[224:227], a[200:203], a[24:27], v[224:227]\n"
    "ds_read_b64_tr_b16 v[192:193], v250 offset:0\n"
    "ds_read_b64_tr_b16 v[194:195], v250 offset:4096\n"
    "ds_read_b64_tr_b16 v[196:197], v250 offset:8\n"
    "v_mfma_f32_16x16x32_bf16 v[224:227], a[204:207], a[28:31], v[224:227]\n"
    "s_waitcnt lgkmcnt(8)\n"
    "ds_read_b64_tr_b16 v[198:199], v250 offset:4104\n"
    "ds_read_b64_tr_b16 v[200:201], v251 offset:0\n"
    "ds_read_b64_tr_b16 v[202:203], v251 offset:4096\n"
    "v_mfma_f32_16x16x32_bf16 v[0:3], v[162:165], a[32:35], 0\n"
    "ds_read_b64_tr_b16 v[204:205], v251 offset:8\n"
    "ds_read_b64_tr_b16 v[206:207], v251 offset:4104\n"
    "ds_read_b64_tr_b16 v[208:209], v250 offset:1024\n"
    "v_mfma_f32_16x16x32_bf16 v[0:3], v[166:169], a[32:35], v[0:3]\n"
    "ds_read_b64_tr_b16 v[210:211], v250 offset:5120\n"
    "ds_read_b64_tr_b16 v[212:213], v250 offset:1032\n"
    "ds_read_b64_tr_b16 v[214:215], v250 offset:5128\n"
    "v_mfma_f32_16x16x32_bf16 v[4:7], v[160:163], a[32:35], 0\n"
    "v_exp_f32 v224, v224\n"
    "v_mfma_f32_16x16x32_bf16 v[4:7], v[164:167], a[32:35], v[4:7]\n"
    "v_exp_f32 v225, v225\n"
    "v_add_f32 v241, v241, v224\n"
    "v_mfma_f32_16x16x32_bf16 v[228:231], a[208:211], a[16:19], 0\n"
    "v_cvt_pk_bf16_f32 v0, v0, v1\n"
    "v_cvt_pk_bf16_f32 v2, v2, v3\n"
    "v_accvgpr_write_b32 a32, v0\n"
    "v_mfma_f32_16x16x32_bf16 v[228:231], a[212:215], a[20:23], v[228:231]\n"
    "v_accvgpr_write_b32 a33, v2\n"
    "v_exp_f32 v226, v226\n"
    "v_mfma_f32_16x16x32_bf16 v[228:231], a[216:219], a[24:27], v[228:231]\n"
    "v_cvt_pk_bf16_f32 v4, v4, v5\n"
    "v_cvt_pk_bf16_f32 v6, v6, v7\n"
    "v_accvgpr_write_b32 a34, v4\n"
    "v_mfma_f32_16x16x32_bf16 v[228:231], a[220:223], a[28:31], v[228:231]\n"
    "s_waitcnt lgkmcnt(15)\n"
    "v_accvgpr_write_b32 a35, v6\n"
    "v_exp_f32 v227, v227\n"
    "v_mfma_f32_16x16x32_bf16 v[8:11], v[162:165], a[36:39], 0\n"
    "v_cvt_pk_bf16_f32 v236, v224, v225\n"
    "v_cvt_pk_bf16_f32 v237, v226, v227\n"
    "v_add_f32 v241, v241, v225\n"
    "v_mfma_f32_16x16x32_bf16 v[8:11], v[166:169], a[36:39], v[8:11]\n"
    "v_add_f32 v241, v241, v226\n"
    "v_add_f32 v241, v241, v227\n"
    "v_mfma_f32_16x16x32_bf16 v[12:15], v[160:163], a[36:39], 0\n"
    "v_exp_f32 v228, v228\n"
    "v_mfma_f32_16x16x32_bf16 v[12:15], v[164:167], a[36:39], v[12:15]\n"
    "v_exp_f32 v229, v229\n"
    "v_add_f32 v241, v241, v228\n"
    "v_mfma_f32_16x16x32_bf16 v[0:3], v[162:165], a[40:43], 0\n"
    "v_cvt_pk_bf16_f32 v8, v8, v9\n"
    "v_cvt_pk_bf16_f32 v10, v10, v11\n"
    "v_accvgpr_write_b32 a36, v8\n"
    "v_mfma_f32_16x16x32_bf16 v[0:3], v[166:169], a[40:43], v[0:3]\n"
    "v_accvgpr_write_b32 a37, v10\n"
    "v_exp_f32 v230, v230\n"
    "v_mfma_f32_16x16x32_bf16 v[4:7], v[160:163], a[40:43], 0\n"
    "v_cvt_pk_bf16_f32 v12, v12, v13\n"
    "v_cvt_pk_bf16_f32 v14, v14, v15\n"
    "v_accvgpr_write_b32 a38, v12\n"
    "v_mfma_f32_16x16x32_bf16 v[4:7], v[164:167], a[40:43], v[4:7]\n"
    "v_accvgpr_write_b32 a39, v14\n"
    "v_exp_f32 v231, v231\n"
    "v_mfma_f32_16x16x32_bf16 a[128:131], a[224:227], v[232:235], a[128:131]\n"
    "v_cvt_pk_bf16_f32 v0, v0, v1\n"
    "v_cvt_pk_bf16_f32 v2, v2, v3\n"
    "v_accvgpr_write_b32 a40, v0\n"
    "v_mfma_f32_16x16x32_bf16 a[132:135], a[228:231], v[232:235], a[132:135]\n"
    "v_accvgpr_write_b32 a41, v2\n"
    "v_cvt_pk_bf16_f32 v238, v228, v229\n"
    "v_cvt_pk_bf16_f32 v239, v230, v231\n"
    "v_mfma_f32_16x16x32_bf16 a[136:139], a[232:235], v[232:235], a[136:139]\n"
    "v_cvt_pk_bf16_f32 v4, v4, v5\n"
    "v_cvt_pk_bf16_f32 v6, v6, v7\n"
    "v_accvgpr_write_b32 a42, v4\n"
    "v_mfma_f32_16x16x32_bf16 a[140:143], a[236:239], v[232:235], a[140:143]\n"
    "s_waitcnt lgkmcnt(14)\n"
    "v_accvgpr_write_b32 a43, v6\n"
    "v_add_f32 v241, v241, v229\n"
    "v_add_f32 v241, v241, v230\n"
    "v_mfma_f32_16x16x32_bf16 v[8:11], v[162:165], a[44:47], 0\n"
    "v_add_f32 v241, v241, v231\n"
    "v_mfma_f32_16x16x32_bf16 v[8:11], v[166:169], a[44:47], v[8:11]\n"
    "v_mfma_f32_16x16x32_bf16 v[12:15], v[160:163], a[44:47], 0\n"
    "v_mfma_f32_16x16x32_bf16 v[12:15], v[164:167], a[44:47], v[12:15]\n"
    "v_mfma_f32_16x16x32_bf16 a[144:147], a[240:243], v[232:235], a[144:147]\n"
    "v_cvt_pk_bf16_f32 v8, v8, v9\n"
    "v_cvt_pk_bf16_f32 v10, v10, v11\n"
    "v_accvgpr_write_b32 a44, v8\n"
    "v_mfma_f32_16x16x32_bf16 a[148:151], a[244:247], v[232:235], a[148:151]\n"
    "v_accvgpr_write_b32 a45, v10\n"
    "v_mfma_f32_16x16x32_bf16 a[152:155], a[248:251], v[232:235], a[152:155]\n"
    "s_waitcnt lgkmcnt(12)\n"
    "v_cvt_pk_bf16_f32 v12, v12, v13\n"
    "v_cvt_pk_bf16_f32 v14, v14, v15\n"
    "v_accvgpr_write_b32 a46, v12\n"
    "v_mfma_f32_16x16x32_bf16 a[156:159], a[252:255], v[232:235], a[156:159]\n"
    "s_waitcnt vmcnt(4)\n"
    "s_add_u32 s68, s66, 69632\n"
    "v_add_u32 v253, s68, v248\n"
    "s_add_u32 s68, s66, 69632\n"
    "v_add_u32 v254, s68, v249\n"
    "ds_read_b128 a[48:51], v253 offset:0\n"
    "ds_read_b128 a[52:55], v254 offset:0\n"
    "ds_read_b128 a[56:59], v253 offset:1024\n"
    "ds_read_b128 a[60:63], v254 offset:1024\n"
    "s_add_u32 s69, s67, 81920\n"
    "s_add_u32 m0, s66, 135168\n"
    "s_nop 0\n"
    "buffer_load_dwordx4 v252, s[36:39], s69 offen offset:0 nt lds\n"
    "buffer_load_dwordx4 v252, s[36:39], s69 offen offset:2048 nt lds\n"
    "s_add_u32 m0, s66, 136064\n"
    "s_nop 0\n"
    "buffer_load_dwordx4 v252, s[36:39], s69 offen offset:128 nt lds\n"
    "buffer_load_dwordx4 v252, s[36:39], s69 offen offset:2176 nt lds\n"
    "v_mfma_f32_16x16x32_bf16 v[216:219], a[192:195], a[32:35], 0\n"
    "v_accvgpr_write_b32 a47, v14\n"
    "v_mfma_f32_16x16x32_bf16 v[216:219], a[196:199], a[36:39], v[216:219]\n"
    "v_mfma_f32_16x16x32_bf16 v[216:219], a[200:203], a[40:43], v[216:219]\n"
    "v_mfma_f32_16x16x32_bf16 v[216:219], a[204:207], a[44:47], v[216:219]\n"
    "s_waitcnt lgkmcnt(3)\n"
    "v_mfma_f32_16x16x32_bf16 v[32:35], v[162:165], a[48:51], 0\n"
    "v_mfma_f32_16x16x32_bf16 v[32:35], v[166:169], a[48:51], v[32:35]\n"
    "v_mfma_f32_16x16x32_bf16 v[36:39], v[160:163], a[48:51], 0\n"
    "v_exp_f32 v216, v216\n"
    "v_mfma_f32_16x16x32_bf16 v[36:39], v[164:167], a[48:51], v[36:39]\n"
    "v_exp_f32 v217, v217\n"
    "v_add_f32 v242, v242, v216\n"
    "v_mfma_f32_16x16x32_bf16 v[220:223], a[208:211], a[32:35], 0\n"
    "v_cvt_pk_bf16_f32 v32, v32, v33\n"
    "v_cvt_pk_bf16_f32 v34, v34, v35\n"
    "v_accvgpr_write_b32 a48, v32\n"
    "v_mfma_f32_16x16x32_bf16 v[220:223], a[212:215], a[36:39], v[220:223]\n"
    "v_accvgpr_write_b32 a49, v34\n"
    "v_exp_f32 v218, v218\n"
    "v_mfma_f32_16x16x32_bf16 v[220:223], a[216:219], a[40:43], v[220:223]\n"
    "v_cvt_pk_bf16_f32 v36, v36, v37\n"
    "v_cvt_pk_bf16_f32 v38, v38, v39\n"
    "v_accvgpr_write_b32 a50, v36\n"
    "v_mfma_f32_16x16x32_bf16 v[220:223], a[220:223], a[44:47], v[220:223]\n"
    "s_waitcnt lgkmcnt(2)\n"
    "v_accvgpr_write_b32 a51, v38\n"
    "v_exp_f32 v219, v219\n"
    "v_mfma_f32_16x16x32_bf16 v[40:43], v[162:165], a[52:55], 0\n"
    "v_cvt_pk_bf16_f32 v232, v216, v217\n"
    "v_cvt_pk_bf16_f32 v233, v218, v219\n"
    "v_add_f32 v242, v242, v217\n"
    "v_mfma_f32_16x16x32_bf16 v[40:43], v[166:169], a[52:55], v[40:43]\n"
    "v_add_f32 v242, v242, v218\n"
    "v_add_f32 v242, v242, v219\n"
    "v_mfma_f32_16x16x32_bf16 v[44:47], v[160:163], a[52:55], 0\n"
    "v_exp_f32 v220, v220\n"
    "v_mfma_f32_16x16x32_bf16 v[44:47], v[164:167], a[52:55], v[44:47]\n"
    "s_waitcnt lgkmcnt(1)\n"
    "v_exp_f32 v221, v221\n"
    "v_add_f32 v242, v242, v220\n"
    "v_mfma_f32_16x16x32_bf16 v[32:35], v[162:165], a[56:59], 0\n"
    "v_cvt_pk_bf16_f32 v40, v40, v41\n"
    "v_cvt_pk_bf16_f32 v42, v42, v43\n"
    "v_accvgpr_write_b32 a52, v40\n"
    "v_mfma_f32_16x16x32_bf16 v[32:35], v[166:169], a[56:59], v[32:35]\n"
    "v_accvgpr_write_b32 a53, v42\n"
    "v_exp_f32 v222, v222\n"
    "v_mfma_f32_16x16x32_bf16 v[36:39], v[160:163], a[56:59], 0\n"
    "v_cvt_pk_bf16_f32 v44, v44, v45\n"
    "v_cvt_pk_bf16_f32 v46, v46, v47\n"
    "v_accvgpr_write_b32 a54, v44\n"
    "v_mfma_f32_16x16x32_bf16 v[36:39], v[164:167], a[56:59], v[36:39]\n"
    "v_accvgpr_write_b32 a55, v46\n"
    "v_exp_f32 v223, v223\n"
    "v_mfma_f32_16x16x32_bf16 a[160:163], a[224:227], v[236:239], a[160:163]\n"
    "v_cvt_pk_bf16_f32 v32, v32, v33\n"
    "v_cvt_pk_bf16_f32 v34, v34, v35\n"
    "v_accvgpr_write_b32 a56, v32\n"
    "v_mfma_f32_16x16x32_bf16 a[164:167], a[228:231], v[236:239], a[164:167]\n"
    "v_accvgpr_write_b32 a57, v34\n"
    "v_cvt_pk_bf16_f32 v234, v220, v221\n"
    "v_cvt_pk_bf16_f32 v235, v222, v223\n"
    "v_mfma_f32_16x16x32_bf16 a[168:171], a[232:235], v[236:239], a[168:171]\n"
    "v_cvt_pk_bf16_f32 v36, v36, v37\n"
    "v_cvt_pk_bf16_f32 v38, v38, v39\n"
    "v_accvgpr_write_b32 a58, v36\n"
    "v_mfma_f32_16x16x32_bf16 a[172:175], a[236:239], v[236:239], a[172:175]\n"
    "s_waitcnt lgkmcnt(0)\n"
    "v_accvgpr_write_b32 a59, v38\n"
    "v_add_f32 v242, v242, v221\n"
    "v_add_f32 v242, v242, v222\n"
    "v_mfma_f32_16x16x32_bf16 v[40:43], v[162:165], a[60:63], 0\n"
    "v_add_f32 v242, v242, v223\n"
    "v_mfma_f32_16x16x32_bf16 v[40:43], v[166:169], a[60:63], v[40:43]\n"
    "v_mfma_f32_16x16x32_bf16 v[44:47], v[160:163], a[60:63], 0\n"
    "v_mfma_f32_16x16x32_bf16 v[44:47], v[164:167], a[60:63], v[44:47]\n"
    "v_mfma_f32_16x16x32_bf16 a[176:179], a[240:243], v[236:239], a[176:179]\n"
    "v_cvt_pk_bf16_f32 v40, v40, v41\n"
    "v_cvt_pk_bf16_f32 v42, v42, v43\n"
    "v_accvgpr_write_b32 a60, v40\n"
    "v_mfma_f32_16x16x32_bf16 a[180:183], a[244:247], v[236:239], a[180:183]\n"
    "v_accvgpr_write_b32 a61, v42\n"
    "v_mfma_f32_16x16x32_bf16 a[184:187], a[248:251], v[236:239], a[184:187]\n"
    "v_cvt_pk_bf16_f32 v44, v44, v45\n"
    "v_cvt_pk_bf16_f32 v46, v46, v47\n"
    "v_accvgpr_write_b32 a62, v44\n"
    "v_mfma_f32_16x16x32_bf16 a[188:191], a[252:255], v[236:239], a[188:191]\n"
    "v_accvgpr_write_b32 a63, v46\n"
    "v_mfma_f32_16x16x32_bf16 v[224:227], a[192:195], a[48:51], 0\n"
    "ds_read_b128 a[192:195], v248 offset:0\n"
    "v_mfma_f32_16x16x32_bf16 v[224:227], a[196:199], a[52:55], v[224:227]\n"
    "ds_read_b128 a[196:199], v249 offset:0\n"
    "v_mfma_f32_16x16x32_bf16 v[224:227], a[200:203], a[56:59], v[224:227]\n"
    "ds_read_b128 a[200:203], v248 offset:1024\n"
    "v_mfma_f32_16x16x32_bf16 v[224:227], a[204:207], a[60:63], v[224:227]\n"
    "ds_read_b128 a[204:207], v249 offset:1024\n"
    "v_mfma_f32_16x16x32_bf16 v[228:231], a[208:211], a[48:51], 0\n"
    "ds_read_b128 a[208:211], v248 offset:4096\n"
    "v_mfma_f32_16x16x32_bf16 v[228:231], a[212:215], a[52:55], v[228:231]\n"
    "ds_read_b128 a[212:215], v249 offset:4096\n"
    "v_mfma_f32_16x16x32_bf16 v[228:231], a[216:219], a[56:59], v[228:231]\n"
    "ds_read_b128 a[216:219], v248 offset:5120\n"
    "v_exp_f32 v224, v224\n"
    "v_mfma_f32_16x16x32_bf16 v[228:231], a[220:223], a[60:63], v[228:231]\n"
    "ds_read_b128 a[220:223], v249 offset:5120\n"
    "v_exp_f32 v225, v225\n"
    "v_mfma_f32_16x16x32_bf16 v[0:3], a[224:227], v[232:235], 0\n"
    "v_exp_f32 v226, v226\n"
    "v_cvt_pk_bf16_f32 v236, v224, v225\n"
    "v_mfma_f32_16x16x32_bf16 v[4:7], a[228:231], v[232:235], 0\n"
    "v_exp_f32 v227, v227\n"
    "v_add_f32 v243, v243, v224\n"
    "v_mfma_f32_16x16x32_bf16 v[8:11], a[232:235], v[232:235], 0\n"
    "v_exp_f32 v228, v228\n"
    "v_cvt_pk_bf16_f32 v237, v226, v227\n"
    "v_mfma_f32_16x16x32_bf16 v[12:15], a[236:239], v[232:235], 0\n"
    "v_exp_f32 v229, v229\n"
    "v_add_f32 v243, v243, v225\n"
    "v_mfma_f32_16x16x32_bf16 v[16:19], a[240:243], v[232:235], 0\n"
    "v_exp_f32 v230, v230\n"
    "v_cvt_pk_bf16_f32 v238, v228, v229\n"
    "v_mfma_f32_16x16x32_bf16 v[20:23], a[244:247], v[232:235], 0\n"
    "v_exp_f32 v231, v231\n"
    "v_add_f32 v243, v243, v226\n"
    "v_mfma_f32_16x16x32_bf16 v[24:27], a[248:251], v[232:235], 0\n"
    "v_cvt_pk_bf16_f32 v239, v230, v231\n"
    "v_add_f32 v243, v243, v227\n"
    "v_add_f32 v243, v243, v228\n"
    "v_mfma_f32_16x16x32_bf16 v[28:31], a[252:255], v[232:235], 0\n"
    "s_waitcnt lgkmcnt(7)\n"
    "v_add_f32 v243, v243, v229\n"
    "v_add_f32 v243, v243, v230\n"
    "v_add_f32 v243, v243, v231\n"
    "v_mfma_f32_16x16x32_bf16 v[216:219], a[192:195], a[32:35], 0\n"
    "s_waitcnt lgkmcnt(6)\n"
    "v_mfma_f32_16x16x32_bf16 v[216:219], a[196:199], a[36:39], v[216:219]\n"
    "s_waitcnt lgkmcnt(5)\n"
    "v_mfma_f32_16x16x32_bf16 v[216:219], a[200:203], a[40:43], v[216:219]\n"
    "s_waitcnt lgkmcnt(4)\n"
    "v_mfma_f32_16x16x32_bf16 v[216:219], a[204:207], a[44:47], v[216:219]\n"
    "s_waitcnt lgkmcnt(3)\n"
    "v_mfma_f32_16x16x32_bf16 v[220:223], a[208:211], a[32:35], 0\n"
    "s_waitcnt lgkmcnt(2)\n"
    "v_mfma_f32_16x16x32_bf16 v[220:223], a[212:215], a[36:39], v[220:223]\n"
    "s_waitcnt lgkmcnt(1)\n"
    "v_mfma_f32_16x16x32_bf16 v[220:223], a[216:219], a[40:43], v[220:223]\n"
    "s_waitcnt lgkmcnt(0)\n"
    "v_exp_f32 v216, v216\n"
    "v_mfma_f32_16x16x32_bf16 v[220:223], a[220:223], a[44:47], v[220:223]\n"
    "v_exp_f32 v217, v217\n"
    "v_add_f32 v242, v242, v216\n"
    "v_mfma_f32_16x16x32_bf16 v[32:35], a[224:227], v[236:239], 0\n"
    "v_exp_f32 v218, v218\n"
    "v_cvt_pk_bf16_f32 v232, v216, v217\n"
    "v_mfma_f32_16x16x32_bf16 v[36:39], a[228:231], v[236:239], 0\n"
    "v_exp_f32 v219, v219\n"
    "v_add_f32 v242, v242, v217\n"
    "v_mfma_f32_16x16x32_bf16 v[40:43], a[232:235], v[236:239], 0\n"
    "v_exp_f32 v220, v220\n"
    "v_cvt_pk_bf16_f32 v233, v218, v219\n"
    "v_mfma_f32_16x16x32_bf16 v[44:47], a[236:239], v[236:239], 0\n"
    "v_exp_f32 v221, v221\n"
    "v_add_f32 v242, v242, v218\n"
    "v_mfma_f32_16x16x32_bf16 v[48:51], a[240:243], v[236:239], 0\n"
    "v_exp_f32 v222, v222\n"
    "v_cvt_pk_bf16_f32 v234, v220, v221\n"
    "v_mfma_f32_16x16x32_bf16 v[52:55], a[244:247], v[236:239], 0\n"
    "v_exp_f32 v223, v223\n"
    "v_add_f32 v242, v242, v219\n"
    "v_mfma_f32_16x16x32_bf16 v[56:59], a[248:251], v[236:239], 0\n"
    "ds_read_b64_tr_b16 a[248:249], v251 offset:1024\n"
    "ds_read_b64_tr_b16 a[250:251], v251 offset:5120\n"
    "v_cvt_pk_bf16_f32 v235, v222, v223\n"
    "v_mfma_f32_16x16x32_bf16 v[60:63], a[252:255], v[236:239], 0\n"
    "s_waitcnt vmcnt(4)\n"
    "s_add_u32 s68, s66, 131072\n"
    "v_add_u32 v253, s68, v248\n"
    "s_add_u32 s68, s66, 131072\n"
    "v_add_u32 v254, s68, v249\n"
    "ds_read_b128 a[64:67], v253 offset:0\n"
    "ds_read_b128 a[68:71], v254 offset:0\n"
    "ds_read_b128 a[72:75], v253 offset:1024\n"
    "ds_read_b128 a[76:79], v254 offset:1024\n"
    "s_add_u32 s69, s67, 98304\n"
    "s_add_u32 m0, s66, 65536\n"
    "s_nop 0\n"
    "buffer_load_dwordx4 v252, s[36:39], s69 offen offset:0 nt lds\n"
    "buffer_load_dwordx4 v252, s[36:39], s69 offen offset:2048 nt lds\n"
    "s_add_u32 m0, s66, 66432\n"
    "s_nop 0\n"
    "buffer_load_dwordx4 v252, s[36:39], s69 offen offset:128 nt lds\n"
    "buffer_load_dwordx4 v252, s[36:39], s69 offen offset:2176 nt lds\n"
    "v_mfma_f32_16x16x32_bf16 v[224:227], a[192:195], a[48:51], 0\n"
    "ds_read_b64_tr_b16 a[252:253], v251 offset:1032\n"
    "ds_read_b64_tr_b16 a[254:255], v251 offset:5128\n"
    "v_add_f32 v242, v242, v220\n"
    "v_mfma_f32_16x16x32_bf16 v[224:227], a[196:199], a[52:55], v[224:227]\n"
    "v_add_f32 v242, v242, v221\n"
    "v_add_f32 v242, v242, v222\n"
    "v_add_f32 v242, v242, v223\n"
    "v_mfma_f32_16x16x32_bf16 v[224:227], a[200:203], a[56:59], v[224:227]\n"
    "ds_read_b64_tr_b16 a[224:225], v250 offset:16384\n"
    "ds_read_b64_tr_b16 a[226:227], v250 offset:20480\n"
    "ds_read_b64_tr_b16 a[228:229], v250 offset:16392\n"
    "v_mfma_f32_16x16x32_bf16 v[224:227], a[204:207], a[60:63], v[224:227]\n"
    "s_waitcnt lgkmcnt(8)\n"
    "ds_read_b64_tr_b16 a[230:231], v250 offset:20488\n"
    "ds_read_b64_tr_b16 a[232:233], v251 offset:16384\n"
    "ds_read_b64_tr_b16 a[234:235], v251 offset:20480\n"
    "v_mfma_f32_16x16x32_bf16 v[64:67], v[162:165], a[64:67], 0\n"
    "ds_read_b64_tr_b16 a[236:237], v251 offset:16392\n"
    "ds_read_b64_tr_b16 a[238:239], v251 offset:20488\n"
    "ds_read_b64_tr_b16 a[240:241], v250 offset:17408\n"
    "v_mfma_f32_16x16x32_bf16 v[64:67], v[166:169], a[64:67], v[64:67]\n"
    "ds_read_b64_tr_b16 a[242:243], v250 offset:21504\n"
    "ds_read_b64_tr_b16 a[244:245], v250 offset:17416\n"
    "ds_read_b64_tr_b16 a[246:247], v250 offset:21512\n"
    "v_mfma_f32_16x16x32_bf16 v[68:71], v[160:163], a[64:67], 0\n"
    "v_exp_f32 v224, v224\n"
    "v_mfma_f32_16x16x32_bf16 v[68:71], v[164:167], a[64:67], v[68:71]\n"
    "v_exp_f32 v225, v225\n"
    "v_add_f32 v243, v243, v224\n"
    "v_mfma_f32_16x16x32_bf16 v[228:231], a[208:211], a[48:51], 0\n"
    "v_cvt_pk_bf16_f32 v64, v64, v65\n"
    "v_cvt_pk_bf16_f32 v66, v66, v67\n"
    "v_accvgpr_write_b32 a64, v64\n"
    "v_mfma_f32_16x16x32_bf16 v[228:231], a[212:215], a[52:55], v[228:231]\n"
    "v_accvgpr_write_b32 a65, v66\n"
    "v_exp_f32 v226, v226\n"
    "v_mfma_f32_16x16x32_bf16 v[228:231], a[216:219], a[56:59], v[228:231]\n"
    "v_cvt_pk_bf16_f32 v68, v68, v69\n"
    "v_cvt_pk_bf16_f32 v70, v70, v71\n"
    "v_accvgpr_write_b32 a66, v68\n"
    "v_mfma_f32_16x16x32_bf16 v[228:231], a[220:223], a[60:63], v[228:231]\n"
    "s_waitcnt lgkmcnt(15)\n"
    "v_accvgpr_write_b32 a67, v70\n"
    "v_exp_f32 v227, v227\n"
    "v_mfma_f32_16x16x32_bf16 v[72:75], v[162:165], a[68:71], 0\n"
    "v_cvt_pk_bf16_f32 v236, v224, v225\n"
    "v_cvt_pk_bf16_f32 v237, v226, v227\n"
    "v_add_f32 v243, v243, v225\n"
    "v_mfma_f32_16x16x32_bf16 v[72:75], v[166:169], a[68:71], v[72:75]\n"
    "v_add_f32 v243, v243, v226\n"
    "v_add_f32 v243, v243, v227\n"
    "v_mfma_f32_16x16x32_bf16 v[76:79], v[160:163], a[68:71], 0\n"
    "v_exp_f32 v228, v228\n"
    "v_mfma_f32_16x16x32_bf16 v[76:79], v[164:167], a[68:71], v[76:79]\n"
    "v_exp_f32 v229, v229\n"
    "v_add_f32 v243, v243, v228\n"
    "v_mfma_f32_16x16x32_bf16 v[64:67], v[162:165], a[72:75], 0\n"
    "v_cvt_pk_bf16_f32 v72, v72, v73\n"
    "v_cvt_pk_bf16_f32 v74, v74, v75\n"
    "v_accvgpr_write_b32 a68, v72\n"
    "v_mfma_f32_16x16x32_bf16 v[64:67], v[166:169], a[72:75], v[64:67]\n"
    "v_accvgpr_write_b32 a69, v74\n"
    "v_exp_f32 v230, v230\n"
    "v_mfma_f32_16x16x32_bf16 v[68:71], v[160:163], a[72:75], 0\n"
    "v_cvt_pk_bf16_f32 v76, v76, v77\n"
    "v_cvt_pk_bf16_f32 v78, v78, v79\n"
    "v_accvgpr_write_b32 a70, v76\n"
    "v_mfma_f32_16x16x32_bf16 v[68:71], v[164:167], a[72:75], v[68:71]\n"
    "v_accvgpr_write_b32 a71, v78\n"
    "v_exp_f32 v231, v231\n"
    "v_mfma_f32_16x16x32_bf16 v[0:3], v[192:195], v[232:235], v[0:3]\n"
    "v_cvt_pk_bf16_f32 v64, v64, v65\n"
    "v_cvt_pk_bf16_f32 v66, v66, v67\n"
    "v_accvgpr_write_b32 a72, v64\n"
    "v_mfma_f32_16x16x32_bf16 v[4:7], v[196:199], v[232:235], v[4:7]\n"
    "v_accvgpr_write_b32 a73, v66\n"
    "v_cvt_pk_bf16_f32 v238, v228, v229\n"
    "v_cvt_pk_bf16_f32 v239, v230, v231\n"
    "v_mfma_f32_16x16x32_bf16 v[8:11], v[200:203], v[232:235], v[8:11]\n"
    "v_cvt_pk_bf16_f32 v68, v68, v69\n"
    "v_cvt_pk_bf16_f32 v70, v70, v71\n"
    "v_accvgpr_write_b32 a74, v68\n"
    "v_mfma_f32_16x16x32_bf16 v[12:15], v[204:207], v[232:235], v[12:15]\n"
    "s_waitcnt lgkmcnt(14)\n"
    "v_accvgpr_write_b32 a75, v70\n"
    "v_add_f32 v243, v243, v229\n"
    "v_add_f32 v243, v243, v230\n"
    "v_mfma_f32_16x16x32_bf16 v[72:75], v[162:165], a[76:79], 0\n"
    "v_add_f32 v243, v243, v231\n"
    "v_mfma_f32_16x16x32_bf16 v[72:75], v[166:169], a[76:79], v[72:75]\n"
    "v_mfma_f32_16x16x32_bf16 v[76:79], v[160:163], a[76:79], 0\n"
    "v_mfma_f32_16x16x32_bf16 v[76:79], v[164:167], a[76:79], v[76:79]\n"
    "v_mfma_f32_16x16x32_bf16 v[16:19], v[208:211], v[232:235], v[16:19]\n"
    "v_cvt_pk_bf16_f32 v72, v72, v73\n"
    "v_cvt_pk_bf16_f32 v74, v74, v75\n"
    "v_accvgpr_write_b32 a76, v72\n"
    "v_mfma_f32_16x16x32_bf16 v[20:23], v[212:215], v[232:235], v[20:23]\n"
    "v_accvgpr_write_b32 a77, v74\n"
    "v_mfma_f32_16x16x32_bf16 v[24:27], a[248:251], v[232:235], v[24:27]\n"
    "s_waitcnt lgkmcnt(12)\n"
    "v_cvt_pk_bf16_f32 v76, v76, v77\n"
    "v_cvt_pk_bf16_f32 v78, v78, v79\n"
    "v_accvgpr_write_b32 a78, v76\n"
    "v_mfma_f32_16x16x32_bf16 v[28:31], a[252:255], v[232:235], v[28:31]\n"
    "s_waitcnt vmcnt(4)\n"
    "s_add_u32 s68, s66, 135168\n"
    "v_add_u32 v253, s68, v248\n"
    "s_add_u32 s68, s66, 135168\n"
    "v_add_u32 v254, s68, v249\n"
    "ds_read_b128 a[80:83], v253 offset:0\n"
    "ds_read_b128 a[84:87], v254 offset:0\n"
    "ds_read_b128 a[88:91], v253 offset:1024\n"
    "ds_read_b128 a[92:95], v254 offset:1024\n"
    "s_add_u32 s69, s67, 114688\n"
    "s_add_u32 m0, s66, 69632\n"
    "s_nop 0\n"
    "buffer_load_dwordx4 v252, s[36:39], s69 offen offset:0 nt lds\n"
    "buffer_load_dwordx4 v252, s[36:39], s69 offen offset:2048 nt lds\n"
    "s_add_u32 m0, s66, 70528\n"
    "s_nop 0\n"
    "buffer_load_dwordx4 v252, s[36:39], s69 offen offset:128 nt lds\n"
    "buffer_load_dwordx4 v252, s[36:39], s69 offen offset:2176 nt lds\n"
    "v_mfma_f32_16x16x32_bf16 v[216:219], a[192:195], a[64:67], 0\n"
    "v_accvgpr_write_b32 a79, v78\n"
    "v_mfma_f32_16x16x32_bf16 v[216:219], a[196:199], a[68:71], v[216:219]\n"
    "v_mfma_f32_16x16x32_bf16 v[216:219], a[200:203], a[72:75], v[216:219]\n"
    "v_mfma_f32_16x16x32_bf16 v[216:219], a[204:207], a[76:79], v[216:219]\n"
    "s_waitcnt lgkmcnt(3)\n"
    "v_mfma_f32_16x16x32_bf16 v[96:99], v[162:165], a[80:83], 0\n"
    "v_mfma_f32_16x16x32_bf16 v[96:99], v[166:169], a[80:83], v[96:99]\n"
    "v_mfma_f32_16x16x32_bf16 v[100:103], v[160:163], a[80:83], 0\n"
    "v_exp_f32 v216, v216\n"
    "v_mfma_f32_16x16x32_bf16 v[100:103], v[164:167], a[80:83], v[100:103]\n"
    "v_exp_f32 v217, v217\n"
    "v_add_f32 v244, v244, v216\n"
    "v_mfma_f32_16x16x32_bf16 v[220:223], a[208:211], a[64:67], 0\n"
    "v_cvt_pk_bf16_f32 v96, v96, v97\n"
    "v_cvt_pk_bf16_f32 v98, v98, v99\n"
    "v_accvgpr_write_b32 a80, v96\n"
    "v_mfma_f32_16x16x32_bf16 v[220:223], a[212:215], a[68:71], v[220:223]\n"
    "v_accvgpr_write_b32 a81, v98\n"
    "v_exp_f32 v218, v218\n"
    "v_mfma_f32_16x16x32_bf16 v[220:223], a[216:219], a[72:75], v[220:223]\n"
    "v_cvt_pk_bf16_f32 v100, v100, v101\n"
    "v_cvt_pk_bf16_f32 v102, v102, v103\n"
    "v_accvgpr_write_b32 a82, v100\n"
    "v_mfma_f32_16x16x32_bf16 v[220:223], a[220:223], a[76:79], v[220:223]\n"
    "s_waitcnt lgkmcnt(2)\n"
    "v_accvgpr_write_b32 a83, v102\n"
    "v_exp_f32 v219, v219\n"
    "v_mfma_f32_16x16x32_bf16 v[104:107], v[162:165], a[84:87], 0\n"
    "v_cvt_pk_bf16_f32 v232, v216, v217\n"
    "v_cvt_pk_bf16_f32 v233, v218, v219\n"
    "v_add_f32 v244, v244, v217\n"
    "v_mfma_f32_16x16x32_bf16 v[104:107], v[166:169], a[84:87], v[104:107]\n"
    "v_add_f32 v244, v244, v218\n"
    "v_add_f32 v244, v244, v219\n"
    "v_mfma_f32_16x16x32_bf16 v[108:111], v[160:163], a[84:87], 0\n"
    "v_exp_f32 v220, v220\n"
    "v_mfma_f32_16x16x32_bf16 v[108:111], v[164:167], a[84:87], v[108:111]\n"
    "s_waitcnt lgkmcnt(1)\n"
    "v_exp_f32 v221, v221\n"
    "v_add_f32 v244, v244, v220\n"
    "v_mfma_f32_16x16x32_bf16 v[96:99], v[162:165], a[88:91], 0\n"
    "v_cvt_pk_bf16_f32 v104, v104, v105\n"
    "v_cvt_pk_bf16_f32 v106, v106, v107\n"
    "v_accvgpr_write_b32 a84, v104\n"
    "v_mfma_f32_16x16x32_bf16 v[96:99], v[166:169], a[88:91], v[96:99]\n"
    "v_accvgpr_write_b32 a85, v106\n"
    "v_exp_f32 v222, v222\n"
    "v_mfma_f32_16x16x32_bf16 v[100:103], v[160:163], a[88:91], 0\n"
    "v_cvt_pk_bf16_f32 v108, v108, v109\n"
    "v_cvt_pk_bf16_f32 v110, v110, v111\n"
    "v_accvgpr_write_b32 a86, v108\n"
    "v_mfma_f32_16x16x32_bf16 v[100:103], v[164:167], a[88:91], v[100:103]\n"
    "v_accvgpr_write_b32 a87, v110\n"
    "v_exp_f32 v223, v223\n"
    "v_mfma_f32_16x16x32_bf16 v[32:35], v[192:195], v[236:239], v[32:35]\n"
    "v_cvt_pk_bf16_f32 v96, v96, v97\n"
    "v_cvt_pk_bf16_f32 v98, v98, v99\n"
    "v_accvgpr_write_b32 a88, v96\n"
    "v_mfma_f32_16x16x32_bf16 v[36:39], v[196:199], v[236:239], v[36:39]\n"
    "v_accvgpr_write_b32 a89, v98\n"
    "v_cvt_pk_bf16_f32 v234, v220, v221\n"
    "v_cvt_pk_bf16_f32 v235, v222, v223\n"
    "v_mfma_f32_16x16x32_bf16 v[40:43], v[200:203], v[236:239], v[40:43]\n"
    "v_cvt_pk_bf16_f32 v100, v100, v101\n"
    "v_cvt_pk_bf16_f32 v102, v102, v103\n"
    "v_accvgpr_write_b32 a90, v100\n"
    "v_mfma_f32_16x16x32_bf16 v[44:47], v[204:207], v[236:239], v[44:47]\n"
    "s_waitcnt lgkmcnt(0)\n"
    "v_accvgpr_write_b32 a91, v102\n"
    "v_add_f32 v244, v244, v221\n"
    "v_add_f32 v244, v244, v222\n"
    "v_mfma_f32_16x16x32_bf16 v[104:107], v[162:165], a[92:95], 0\n"
    "v_add_f32 v244, v244, v223\n"
    "v_mfma_f32_16x16x32_bf16 v[104:107], v[166:169], a[92:95], v[104:107]\n"
    "v_mfma_f32_16x16x32_bf16 v[108:111], v[160:163], a[92:95], 0\n"
    "v_mfma_f32_16x16x32_bf16 v[108:111], v[164:167], a[92:95], v[108:111]\n"
    "v_mfma_f32_16x16x32_bf16 v[48:51], v[208:211], v[236:239], v[48:51]\n"
    "v_cvt_pk_bf16_f32 v104, v104, v105\n"
    "v_cvt_pk_bf16_f32 v106, v106, v107\n"
    "v_accvgpr_write_b32 a92, v104\n"
    "v_mfma_f32_16x16x32_bf16 v[52:55], v[212:215], v[236:239], v[52:55]\n"
    "v_accvgpr_write_b32 a93, v106\n"
    "v_mfma_f32_16x16x32_bf16 v[56:59], a[248:251], v[236:239], v[56:59]\n"
    "v_cvt_pk_bf16_f32 v108, v108, v109\n"
    "v_cvt_pk_bf16_f32 v110, v110, v111\n"
    "v_accvgpr_write_b32 a94, v108\n"
    "v_mfma_f32_16x16x32_bf16 v[60:63], a[252:255], v[236:239], v[60:63]\n"
    "v_accvgpr_write_b32 a95, v110\n"
    "v_mfma_f32_16x16x32_bf16 v[224:227], a[192:195], a[80:83], 0\n"
    "ds_read_b128 a[192:195], v248 offset:16384\n"
    "v_mfma_f32_16x16x32_bf16 v[224:227], a[196:199], a[84:87], v[224:227]\n"
    "ds_read_b128 a[196:199], v249 offset:16384\n"
    "v_mfma_f32_16x16x32_bf16 v[224:227], a[200:203], a[88:91], v[224:227]\n"
    "ds_read_b128 a[200:203], v248 offset:17408\n"
    "v_mfma_f32_16x16x32_bf16 v[224:227], a[204:207], a[92:95], v[224:227]\n"
    "ds_read_b128 a[204:207], v249 offset:17408\n"
    "v_mfma_f32_16x16x32_bf16 v[228:231], a[208:211], a[80:83], 0\n"
    "ds_read_b128 a[208:211], v248 offset:20480\n"
    "v_mfma_f32_16x16x32_bf16 v[228:231], a[212:215], a[84:87], v[228:231]\n"
    "ds_read_b128 a[212:215], v249 offset:20480\n"
    "v_mfma_f32_16x16x32_bf16 v[228:231], a[216:219], a[88:91], v[228:231]\n"
    "ds_read_b128 a[216:219], v248 offset:21504\n"
    "v_exp_f32 v224, v224\n"
    "v_mfma_f32_16x16x32_bf16 v[228:231], a[220:223], a[92:95], v[228:231]\n"
    "ds_read_b128 a[220:223], v249 offset:21504\n"
    "v_exp_f32 v225, v225\n"
    "v_mfma_f32_16x16x32_bf16 v[64:67], v[192:195], v[232:235], 0\n"
    "v_exp_f32 v226, v226\n"
    "v_cvt_pk_bf16_f32 v236, v224, v225\n"
    "v_mfma_f32_16x16x32_bf16 v[68:71], v[196:199], v[232:235], 0\n"
    "v_exp_f32 v227, v227\n"
    "v_add_f32 v245, v245, v224\n"
    "v_mfma_f32_16x16x32_bf16 v[72:75], v[200:203], v[232:235], 0\n"
    "v_exp_f32 v228, v228\n"
    "v_cvt_pk_bf16_f32 v237, v226, v227\n"
    "v_mfma_f32_16x16x32_bf16 v[76:79], v[204:207], v[232:235], 0\n"
    "v_exp_f32 v229, v229\n"
    "v_add_f32 v245, v245, v225\n"
    "v_mfma_f32_16x16x32_bf16 v[80:83], v[208:211], v[232:235], 0\n"
    "v_exp_f32 v230, v230\n"
    "v_cvt_pk_bf16_f32 v238, v228, v229\n"
    "v_mfma_f32_16x16x32_bf16 v[84:87], v[212:215], v[232:235], 0\n"
    "v_exp_f32 v231, v231\n"
    "v_add_f32 v245, v245, v226\n"
    "v_mfma_f32_16x16x32_bf16 v[88:91], a[248:251], v[232:235], 0\n"
    "v_cvt_pk_bf16_f32 v239, v230, v231\n"
    "v_add_f32 v245, v245, v227\n"
    "v_add_f32 v245, v245, v228\n"
    "v_mfma_f32_16x16x32_bf16 v[92:95], a[252:255], v[232:235], 0\n"
    "s_waitcnt lgkmcnt(7)\n"
    "v_add_f32 v245, v245, v229\n"
    "v_add_f32 v245, v245, v230\n"
    "v_add_f32 v245, v245, v231\n"
    "v_mfma_f32_16x16x32_bf16 v[216:219], a[192:195], a[64:67], 0\n"
    "s_waitcnt lgkmcnt(6)\n"
    "v_mfma_f32_16x16x32_bf16 v[216:219], a[196:199], a[68:71], v[216:219]\n"
    "s_waitcnt lgkmcnt(5)\n"
    "v_mfma_f32_16x16x32_bf16 v[216:219], a[200:203], a[72:75], v[216:219]\n"
    "s_waitcnt lgkmcnt(4)\n"
    "v_mfma_f32_16x16x32_bf16 v[216:219], a[204:207], a[76:79], v[216:219]\n"
    "s_waitcnt lgkmcnt(3)\n"
    "v_mfma_f32_16x16x32_bf16 v[220:223], a[208:211], a[64:67], 0\n"
    "s_waitcnt lgkmcnt(2)\n"
    "v_mfma_f32_16x16x32_bf16 v[220:223], a[212:215], a[68:71], v[220:223]\n"
    "s_waitcnt lgkmcnt(1)\n"
    "v_mfma_f32_16x16x32_bf16 v[220:223], a[216:219], a[72:75], v[220:223]\n"
    "s_waitcnt lgkmcnt(0)\n"
    "v_exp_f32 v216, v216\n"
    "v_mfma_f32_16x16x32_bf16 v[220:223], a[220:223], a[76:79], v[220:223]\n"
    "v_exp_f32 v217, v217\n"
    "v_add_f32 v244, v244, v216\n"
    "v_mfma_f32_16x16x32_bf16 v[96:99], v[192:195], v[236:239], 0\n"
    "v_exp_f32 v218, v218\n"
    "v_cvt_pk_bf16_f32 v232, v216, v217\n"
    "v_mfma_f32_16x16x32_bf16 v[100:103], v[196:199], v[236:239], 0\n"
    "v_exp_f32 v219, v219\n"
    "v_add_f32 v244, v244, v217\n"
    "v_mfma_f32_16x16x32_bf16 v[104:107], v[200:203], v[236:239], 0\n"
    "v_exp_f32 v220, v220\n"
    "v_cvt_pk_bf16_f32 v233, v218, v219\n"
    "v_mfma_f32_16x16x32_bf16 v[108:111], v[204:207], v[236:239], 0\n"
    "v_exp_f32 v221, v221\n"
    "v_add_f32 v244, v244, v218\n"
    "v_mfma_f32_16x16x32_bf16 v[112:115], v[208:211], v[236:239], 0\n"
    "v_exp_f32 v222, v222\n"
    "v_cvt_pk_bf16_f32 v234, v220, v221\n"
    "v_mfma_f32_16x16x32_bf16 v[116:119], v[212:215], v[236:239], 0\n"
    "v_exp_f32 v223, v223\n"
    "v_add_f32 v244, v244, v219\n"
    "v_mfma_f32_16x16x32_bf16 v[120:123], a[248:251], v[236:239], 0\n"
    "ds_read_b64_tr_b16 a[248:249], v251 offset:17408\n"
    "ds_read_b64_tr_b16 a[250:251], v251 offset:21504\n"
    "v_cvt_pk_bf16_f32 v235, v222, v223\n"
    "v_mfma_f32_16x16x32_bf16 v[124:127], a[252:255], v[236:239], 0\n"
    "s_waitcnt vmcnt(4)\n"
    "s_add_u32 s68, s66, 65536\n"
    "v_add_u32 v253, s68, v248\n"
    "s_add_u32 s68, s66, 65536\n"
    "v_add_u32 v254, s68, v249\n"
    "ds_read_b128 a[96:99], v253 offset:0\n"
    "ds_read_b128 a[100:103], v254 offset:0\n"
    "ds_read_b128 a[104:107], v253 offset:1024\n"
    "ds_read_b128 a[108:111], v254 offset:1024\n"
    "s_add_u32 m0, s64, 32768\n"
    "s_add_u32 s69, s65, 16384\n"
    "buffer_load_dwordx4 v252, s[40:43], s69 offen offset:0 nt lds\n"
    "buffer_load_dwordx4 v252, s[40:43], s69 offen offset:2048 nt lds\n"
    "s_add_u32 m0, s64, 40960\n"
    "s_add_u32 s69, s65, 16384\n"
    "buffer_load_dwordx4 v252, s[44:47], s69 offen offset:0 nt lds\n"
    "buffer_load_dwordx4 v252, s[44:47], s69 offen offset:2048 nt lds\n"
    "v_mfma_f32_16x16x32_bf16 v[224:227], a[192:195], a[80:83], 0\n"
    "ds_read_b64_tr_b16 a[252:253], v251 offset:17416\n"
    "ds_read_b64_tr_b16 a[254:255], v251 offset:21512\n"
    "v_add_f32 v244, v244, v220\n"
    "v_mfma_f32_16x16x32_bf16 v[224:227], a[196:199], a[84:87], v[224:227]\n"
    "v_add_f32 v244, v244, v221\n"
    "v_add_f32 v244, v244, v222\n"
    "v_add_f32 v244, v244, v223\n"
    "v_mfma_f32_16x16x32_bf16 v[224:227], a[200:203], a[88:91], v[224:227]\n"
    "ds_read_b64_tr_b16 v[192:193], v250 offset:0\n"
    "ds_read_b64_tr_b16 v[194:195], v250 offset:4096\n"
    "ds_read_b64_tr_b16 v[196:197], v250 offset:8\n"
    "v_mfma_f32_16x16x32_bf16 v[224:227], a[204:207], a[92:95], v[224:227]\n"
    "s_waitcnt lgkmcnt(8)\n"
    "ds_read_b64_tr_b16 v[198:199], v250 offset:4104\n"
    "ds_read_b64_tr_b16 v[200:201], v251 offset:0\n"
    "ds_read_b64_tr_b16 v[202:203], v251 offset:4096\n"
    "v_mfma_f32_16x16x32_bf16 v[128:131], v[162:165], a[96:99], 0\n"
    "ds_read_b64_tr_b16 v[204:205], v251 offset:8\n"
    "ds_read_b64_tr_b16 v[206:207], v251 offset:4104\n"
    "ds_read_b64_tr_b16 v[208:209], v250 offset:1024\n"
    "v_mfma_f32_16x16x32_bf16 v[128:131], v[166:169], a[96:99], v[128:131]\n"
    "ds_read_b64_tr_b16 v[210:211], v250 offset:5120\n"
    "ds_read_b64_tr_b16 v[212:213], v250 offset:1032\n"
    "ds_read_b64_tr_b16 v[214:215], v250 offset:5128\n"
    "v_mfma_f32_16x16x32_bf16 v[132:135], v[160:163], a[96:99], 0\n"
    "v_exp_f32 v224, v224\n"
    "v_xor_b32 v250, 32768, v250\n"
    "v_mfma_f32_16x16x32_bf16 v[132:135], v[164:167], a[96:99], v[132:135]\n"
    "v_exp_f32 v225, v225\n"
    "v_add_f32 v245, v245, v224\n"
    "v_mfma_f32_16x16x32_bf16 v[228:231], a[208:211], a[80:83], 0\n"
    "v_cvt_pk_bf16_f32 v128, v128, v129\n"
    "v_cvt_pk_bf16_f32 v130, v130, v131\n"
    "v_accvgpr_write_b32 a96, v128\n"
    "v_mfma_f32_16x16x32_bf16 v[228:231], a[212:215], a[84:87], v[228:231]\n"
    "v_accvgpr_write_b32 a97, v130\n"
    "v_exp_f32 v226, v226\n"
    "v_mfma_f32_16x16x32_bf16 v[228:231], a[216:219], a[88:91], v[228:231]\n"
    "v_cvt_pk_bf16_f32 v132, v132, v133\n"
    "v_cvt_pk_bf16_f32 v134, v134, v135\n"
    "v_accvgpr_write_b32 a98, v132\n"
    "v_mfma_f32_16x16x32_bf16 v[228:231], a[220:223], a[92:95], v[228:231]\n"
    "s_waitcnt lgkmcnt(15)\n"
    "v_accvgpr_write_b32 a99, v134\n"
    "v_exp_f32 v227, v227\n"
    "v_mfma_f32_16x16x32_bf16 v[136:139], v[162:165], a[100:103], 0\n"
    "v_cvt_pk_bf16_f32 v236, v224, v225\n"
    "v_cvt_pk_bf16_f32 v237, v226, v227\n"
    "v_add_f32 v245, v245, v225\n"
    "v_mfma_f32_16x16x32_bf16 v[136:139], v[166:169], a[100:103], v[136:139]\n"
    "v_add_f32 v245, v245, v226\n"
    "v_add_f32 v245, v245, v227\n"
    "v_mfma_f32_16x16x32_bf16 v[140:143], v[160:163], a[100:103], 0\n"
    "v_exp_f32 v228, v228\n"
    "v_mfma_f32_16x16x32_bf16 v[140:143], v[164:167], a[100:103], v[140:143]\n"
    "v_exp_f32 v229, v229\n"
    "v_add_f32 v245, v245, v228\n"
    "v_mfma_f32_16x16x32_bf16 v[128:131], v[162:165], a[104:107], 0\n"
    "v_cvt_pk_bf16_f32 v136, v136, v137\n"
    "v_cvt_pk_bf16_f32 v138, v138, v139\n"
    "v_accvgpr_write_b32 a100, v136\n"
    "v_mfma_f32_16x16x32_bf16 v[128:131], v[166:169], a[104:107], v[128:131]\n"
    "v_accvgpr_write_b32 a101, v138\n"
    "v_exp_f32 v230, v230\n"
    "v_mfma_f32_16x16x32_bf16 v[132:135], v[160:163], a[104:107], 0\n"
    "v_cvt_pk_bf16_f32 v140, v140, v141\n"
    "v_cvt_pk_bf16_f32 v142, v142, v143\n"
    "v_accvgpr_write_b32 a102, v140\n"
    "v_mfma_f32_16x16x32_bf16 v[132:135], v[164:167], a[104:107], v[132:135]\n"
    "v_accvgpr_write_b32 a103, v142\n"
    "v_exp_f32 v231, v231\n"
    "v_mfma_f32_16x16x32_bf16 v[64:67], a[224:227], v[232:235], v[64:67]\n"
    "v_cvt_pk_bf16_f32 v128, v128, v129\n"
    "v_cvt_pk_bf16_f32 v130, v130, v131\n"
    "v_accvgpr_write_b32 a104, v128\n"
    "v_mfma_f32_16x16x32_bf16 v[68:71], a[228:231], v[232:235], v[68:71]\n"
    "v_accvgpr_write_b32 a105, v130\n"
    "v_cvt_pk_bf16_f32 v238, v228, v229\n"
    "v_cvt_pk_bf16_f32 v239, v230, v231\n"
    "v_mfma_f32_16x16x32_bf16 v[72:75], a[232:235], v[232:235], v[72:75]\n"
    "v_cvt_pk_bf16_f32 v132, v132, v133\n"
    "v_cvt_pk_bf16_f32 v134, v134, v135\n"
    "v_accvgpr_write_b32 a106, v132\n"
    "v_mfma_f32_16x16x32_bf16 v[76:79], a[236:239], v[232:235], v[76:79]\n"
    "s_waitcnt lgkmcnt(14)\n"
    "v_accvgpr_write_b32 a107, v134\n"
    "v_add_f32 v245, v245, v229\n"
    "v_add_f32 v245, v245, v230\n"
    "v_mfma_f32_16x16x32_bf16 v[136:139], v[162:165], a[108:111], 0\n"
    "v_add_f32 v245, v245, v231\n"
    "v_mfma_f32_16x16x32_bf16 v[136:139], v[166:169], a[108:111], v[136:139]\n"
    "v_mfma_f32_16x16x32_bf16 v[140:143], v[160:163], a[108:111], 0\n"
    "v_mfma_f32_16x16x32_bf16 v[140:143], v[164:167], a[108:111], v[140:143]\n"
    "v_mfma_f32_16x16x32_bf16 v[80:83], a[240:243], v[232:235], v[80:83]\n"
    "v_cvt_pk_bf16_f32 v136, v136, v137\n"
    "v_cvt_pk_bf16_f32 v138, v138, v139\n"
    "v_accvgpr_write_b32 a108, v136\n"
    "v_mfma_f32_16x16x32_bf16 v[84:87], a[244:247], v[232:235], v[84:87]\n"
    "v_accvgpr_write_b32 a109, v138\n"
    "v_mfma_f32_16x16x32_bf16 v[88:91], a[248:251], v[232:235], v[88:91]\n"
    "s_waitcnt lgkmcnt(12)\n"
    "v_cvt_pk_bf16_f32 v140, v140, v141\n"
    "v_cvt_pk_bf16_f32 v142, v142, v143\n"
    "v_accvgpr_write_b32 a110, v140\n"
    "v_mfma_f32_16x16x32_bf16 v[92:95], a[252:255], v[232:235], v[92:95]\n"
    "s_waitcnt vmcnt(4)\n"
    "s_add_u32 s68, s66, 69632\n"
    "v_add_u32 v253, s68, v248\n"
    "s_add_u32 s68, s66, 69632\n"
    "v_add_u32 v254, s68, v249\n"
    "ds_read_b128 a[112:115], v253 offset:0\n"
    "ds_read_b128 a[116:119], v254 offset:0\n"
    "ds_read_b128 a[120:123], v253 offset:1024\n"
    "ds_read_b128 a[124:127], v254 offset:1024\n"
    "s_add_u32 m0, s64, 49152\n"
    "s_add_u32 s69, s65, 24576\n"
    "buffer_load_dwordx4 v252, s[40:43], s69 offen offset:0 nt lds\n"
    "buffer_load_dwordx4 v252, s[40:43], s69 offen offset:2048 nt lds\n"
    "s_add_u32 m0, s64, 57344\n"
    "s_add_u32 s69, s65, 24576\n"
    "buffer_load_dwordx4 v252, s[44:47], s69 offen offset:0 nt lds\n"
    "buffer_load_dwordx4 v252, s[44:47], s69 offen offset:2048 nt lds\n"
    "v_mfma_f32_16x16x32_bf16 v[216:219], a[192:195], a[96:99], 0\n"
    "v_accvgpr_write_b32 a111, v142\n"
    "v_mfma_f32_16x16x32_bf16 v[216:219], a[196:199], a[100:103], v[216:219]\n"
    "v_mfma_f32_16x16x32_bf16 v[216:219], a[200:203], a[104:107], v[216:219]\n"
    "v_mfma_f32_16x16x32_bf16 v[216:219], a[204:207], a[108:111], v[216:219]\n"
    "s_waitcnt lgkmcnt(3)\n"
    "v_mfma_f32_16x16x32_bf16 v[176:179], v[162:165], a[112:115], 0\n"
    "v_mfma_f32_16x16x32_bf16 v[176:179], v[166:169], a[112:115], v[176:179]\n"
    "v_mfma_f32_16x16x32_bf16 v[180:183], v[160:163], a[112:115], 0\n"
    "v_exp_f32 v216, v216\n"
    "v_mfma_f32_16x16x32_bf16 v[180:183], v[164:167], a[112:115], v[180:183]\n"
    "v_exp_f32 v217, v217\n"
    "v_add_f32 v246, v246, v216\n"
    "v_mfma_f32_16x16x32_bf16 v[220:223], a[208:211], a[96:99], 0\n"
    "v_cvt_pk_bf16_f32 v176, v176, v177\n"
    "v_cvt_pk_bf16_f32 v178, v178, v179\n"
    "v_accvgpr_write_b32 a112, v176\n"
    "v_mfma_f32_16x16x32_bf16 v[220:223], a[212:215], a[100:103], v[220:223]\n"
    "v_accvgpr_write_b32 a113, v178\n"
    "v_exp_f32 v218, v218\n"
    "v_mfma_f32_16x16x32_bf16 v[220:223], a[216:219], a[104:107], v[220:223]\n"
    "v_cvt_pk_bf16_f32 v180, v180, v181\n"
    "v_cvt_pk_bf16_f32 v182, v182, v183\n"
    "v_accvgpr_write_b32 a114, v180\n"
    "v_mfma_f32_16x16x32_bf16 v[220:223], a[220:223], a[108:111], v[220:223]\n"
    "s_waitcnt lgkmcnt(2)\n"
    "v_accvgpr_write_b32 a115, v182\n"
    "v_exp_f32 v219, v219\n"
    "v_mfma_f32_16x16x32_bf16 v[184:187], v[162:165], a[116:119], 0\n"
    "v_cvt_pk_bf16_f32 v232, v216, v217\n"
    "v_cvt_pk_bf16_f32 v233, v218, v219\n"
    "v_add_f32 v246, v246, v217\n"
    "v_mfma_f32_16x16x32_bf16 v[184:187], v[166:169], a[116:119], v[184:187]\n"
    "v_add_f32 v246, v246, v218\n"
    "v_add_f32 v246, v246, v219\n"
    "v_mfma_f32_16x16x32_bf16 v[188:191], v[160:163], a[116:119], 0\n"
    "v_exp_f32 v220, v220\n"
    "v_mfma_f32_16x16x32_bf16 v[188:191], v[164:167], a[116:119], v[188:191]\n"
    "s_waitcnt lgkmcnt(1)\n"
    "v_exp_f32 v221, v221\n"
    "v_add_f32 v246, v246, v220\n"
    "v_mfma_f32_16x16x32_bf16 v[176:179], v[162:165], a[120:123], 0\n"
    "v_cvt_pk_bf16_f32 v184, v184, v185\n"
    "v_cvt_pk_bf16_f32 v186, v186, v187\n"
    "v_accvgpr_write_b32 a116, v184\n"
    "v_mfma_f32_16x16x32_bf16 v[176:179], v[166:169], a[120:123], v[176:179]\n"
    "v_accvgpr_write_b32 a117, v186\n"
    "v_exp_f32 v222, v222\n"
    "v_mfma_f32_16x16x32_bf16 v[180:183], v[160:163], a[120:123], 0\n"
    "v_cvt_pk_bf16_f32 v188, v188, v189\n"
    "v_cvt_pk_bf16_f32 v190, v190, v191\n"
    "v_accvgpr_write_b32 a118, v188\n"
    "v_mfma_f32_16x16x32_bf16 v[180:183], v[164:167], a[120:123], v[180:183]\n"
    "v_accvgpr_write_b32 a119, v190\n"
    "v_exp_f32 v223, v223\n"
    "v_mfma_f32_16x16x32_bf16 v[96:99], a[224:227], v[236:239], v[96:99]\n"
    "v_cvt_pk_bf16_f32 v176, v176, v177\n"
    "v_cvt_pk_bf16_f32 v178, v178, v179\n"
    "v_accvgpr_write_b32 a120, v176\n"
    "v_mfma_f32_16x16x32_bf16 v[100:103], a[228:231], v[236:239], v[100:103]\n"
    "v_accvgpr_write_b32 a121, v178\n"
    "v_cvt_pk_bf16_f32 v234, v220, v221\n"
    "v_cvt_pk_bf16_f32 v235, v222, v223\n"
    "v_mfma_f32_16x16x32_bf16 v[104:107], a[232:235], v[236:239], v[104:107]\n"
    "v_cvt_pk_bf16_f32 v180, v180, v181\n"
    "v_cvt_pk_bf16_f32 v182, v182, v183\n"
    "v_accvgpr_write_b32 a122, v180\n"
    "v_mfma_f32_16x16x32_bf16 v[108:111], a[236:239], v[236:239], v[108:111]\n"
    "s_waitcnt lgkmcnt(0)\n"
    "v_accvgpr_write_b32 a123, v182\n"
    "v_add_f32 v246, v246, v221\n"
    "v_add_f32 v246, v246, v222\n"
    "v_mfma_f32_16x16x32_bf16 v[184:187], v[162:165], a[124:127], 0\n"
    "v_add_f32 v246, v246, v223\n"
    "v_mfma_f32_16x16x32_bf16 v[184:187], v[166:169], a[124:127], v[184:187]\n"
    "v_mfma_f32_16x16x32_bf16 v[188:191], v[160:163], a[124:127], 0\n"
    "v_mfma_f32_16x16x32_bf16 v[188:191], v[164:167], a[124:127], v[188:191]\n"
    "v_mfma_f32_16x16x32_bf16 v[112:115], a[240:243], v[236:239], v[112:115]\n"
    "v_cvt_pk_bf16_f32 v184, v184, v185\n"
    "v_cvt_pk_bf16_f32 v186, v186, v187\n"
    "v_accvgpr_write_b32 a124, v184\n"
    "v_mfma_f32_16x16x32_bf16 v[116:119], a[244:247], v[236:239], v[116:119]\n"
    "v_accvgpr_write_b32 a125, v186\n"
    "v_mfma_f32_16x16x32_bf16 v[120:123], a[248:251], v[236:239], v[120:123]\n"
    "v_cvt_pk_bf16_f32 v188, v188, v189\n"
    "v_cvt_pk_bf16_f32 v190, v190, v191\n"
    "v_accvgpr_write_b32 a126, v188\n"
    "v_mfma_f32_16x16x32_bf16 v[124:127], a[252:255], v[236:239], v[124:127]\n"
    "v_accvgpr_write_b32 a127, v190\n"
    "v_mfma_f32_16x16x32_bf16 v[224:227], a[192:195], a[112:115], 0\n"
    "ds_read_b128 a[192:195], v248 offset:0\n"
    "v_mfma_f32_16x16x32_bf16 v[224:227], a[196:199], a[116:119], v[224:227]\n"
    "ds_read_b128 a[196:199], v249 offset:0\n"
    "v_mfma_f32_16x16x32_bf16 v[224:227], a[200:203], a[120:123], v[224:227]\n"
    "ds_read_b128 a[200:203], v248 offset:1024\n"
    "v_mfma_f32_16x16x32_bf16 v[224:227], a[204:207], a[124:127], v[224:227]\n"
    "ds_read_b128 a[204:207], v249 offset:1024\n"
    "v_mfma_f32_16x16x32_bf16 v[228:231], a[208:211], a[112:115], 0\n"
    "ds_read_b128 a[208:211], v248 offset:4096\n"
    "v_mfma_f32_16x16x32_bf16 v[228:231], a[212:215], a[116:119], v[228:231]\n"
    "ds_read_b128 a[212:215], v249 offset:4096\n"
    "v_mfma_f32_16x16x32_bf16 v[228:231], a[216:219], a[120:123], v[228:231]\n"
    "ds_read_b128 a[216:219], v248 offset:5120\n"
    "v_exp_f32 v224, v224\n"
    "v_mfma_f32_16x16x32_bf16 v[228:231], a[220:223], a[124:127], v[228:231]\n"
    "ds_read_b128 a[220:223], v249 offset:5120\n"
    "v_exp_f32 v225, v225\n"
    "v_mfma_f32_16x16x32_bf16 v[128:131], a[224:227], v[232:235], 0\n"
    "v_exp_f32 v226, v226\n"
    "v_cvt_pk_bf16_f32 v236, v224, v225\n"
    "v_mfma_f32_16x16x32_bf16 v[132:135], a[228:231], v[232:235], 0\n"
    "v_exp_f32 v227, v227\n"
    "v_add_f32 v247, v247, v224\n"
    "v_mfma_f32_16x16x32_bf16 v[136:139], a[232:235], v[232:235], 0\n"
    "v_exp_f32 v228, v228\n"
    "v_cvt_pk_bf16_f32 v237, v226, v227\n"
    "v_mfma_f32_16x16x32_bf16 v[140:143], a[236:239], v[232:235], 0\n"
    "v_exp_f32 v229, v229\n"
    "v_add_f32 v247, v247, v225\n"
    "v_mfma_f32_16x16x32_bf16 v[144:147], a[240:243], v[232:235], 0\n"
    "v_exp_f32 v230, v230\n"
    "v_cvt_pk_bf16_f32 v238, v228, v229\n"
    "v_mfma_f32_16x16x32_bf16 v[148:151], a[244:247], v[232:235], 0\n"
    "v_exp_f32 v231, v231\n"
    "v_add_f32 v247, v247, v226\n"
    "v_mfma_f32_16x16x32_bf16 v[152:155], a[248:251], v[232:235], 0\n"
    "v_cvt_pk_bf16_f32 v239, v230, v231\n"
    "v_add_f32 v247, v247, v227\n"
    "v_add_f32 v247, v247, v228\n"
    "v_mfma_f32_16x16x32_bf16 v[156:159], a[252:255], v[232:235], 0\n"
    "s_waitcnt lgkmcnt(7)\n"
    "v_add_f32 v247, v247, v229\n"
    "v_add_f32 v247, v247, v230\n"
    "v_add_f32 v247, v247, v231\n"
    "v_mfma_f32_16x16x32_bf16 v[216:219], a[192:195], a[96:99], 0\n"
    "s_waitcnt lgkmcnt(6)\n"
    "v_xor_b32 v248, 32768, v248\n"
    "v_xor_b32 v249, 32768, v249\n"
    "v_mfma_f32_16x16x32_bf16 v[216:219], a[196:199], a[100:103], v[216:219]\n"
    "s_waitcnt lgkmcnt(5)\n"
    "v_mfma_f32_16x16x32_bf16 v[216:219], a[200:203], a[104:107], v[216:219]\n"
    "s_waitcnt lgkmcnt(4)\n"
    "v_mfma_f32_16x16x32_bf16 v[216:219], a[204:207], a[108:111], v[216:219]\n"
    "s_waitcnt lgkmcnt(3)\n"
    "v_mfma_f32_16x16x32_bf16 v[220:223], a[208:211], a[96:99], 0\n"
    "s_waitcnt lgkmcnt(2)\n"
    "v_mfma_f32_16x16x32_bf16 v[220:223], a[212:215], a[100:103], v[220:223]\n"
    "s_waitcnt lgkmcnt(1)\n"
    "v_mfma_f32_16x16x32_bf16 v[220:223], a[216:219], a[104:107], v[220:223]\n"
    "s_waitcnt lgkmcnt(0)\n"
    "v_exp_f32 v216, v216\n"
    "v_mfma_f32_16x16x32_bf16 v[220:223], a[220:223], a[108:111], v[220:223]\n"
    "v_exp_f32 v217, v217\n"
    "v_add_f32 v246, v246, v216\n"
    "v_mfma_f32_16x16x32_bf16 v[160:163], a[224:227], v[236:239], 0\n"
    "v_exp_f32 v218, v218\n"
    "v_cvt_pk_bf16_f32 v232, v216, v217\n"
    "v_mfma_f32_16x16x32_bf16 v[164:167], a[228:231], v[236:239], 0\n"
    "v_exp_f32 v219, v219\n"
    "v_add_f32 v246, v246, v217\n"
    "v_mfma_f32_16x16x32_bf16 v[168:171], a[232:235], v[236:239], 0\n"
    "v_exp_f32 v220, v220\n"
    "v_cvt_pk_bf16_f32 v233, v218, v219\n"
    "v_mfma_f32_16x16x32_bf16 v[172:175], a[236:239], v[236:239], 0\n"
    "v_exp_f32 v221, v221\n"
    "v_add_f32 v246, v246, v218\n"
    "v_mfma_f32_16x16x32_bf16 v[176:179], a[240:243], v[236:239], 0\n"
    "v_exp_f32 v222, v222\n"
    "v_cvt_pk_bf16_f32 v234, v220, v221\n"
    "v_mfma_f32_16x16x32_bf16 v[180:183], a[244:247], v[236:239], 0\n"
    "v_exp_f32 v223, v223\n"
    "v_add_f32 v246, v246, v219\n"
    "v_mfma_f32_16x16x32_bf16 v[184:187], a[248:251], v[236:239], 0\n"
    "ds_read_b64_tr_b16 a[248:249], v251 offset:1024\n"
    "ds_read_b64_tr_b16 a[250:251], v251 offset:5120\n"
    "v_cvt_pk_bf16_f32 v235, v222, v223\n"
    "v_mfma_f32_16x16x32_bf16 v[188:191], a[252:255], v[236:239], 0\n"
    "ds_read_b64_tr_b16 a[252:253], v251 offset:1032\n"
    "ds_read_b64_tr_b16 a[254:255], v251 offset:5128\n"
    "v_add_f32 v246, v246, v220\n"
    "v_mfma_f32_16x16x32_bf16 v[224:227], a[192:195], a[112:115], 0\n"
    "s_waitcnt vmcnt(6)\n"
    "s_barrier\n"
    "ds_read_b128 a[192:195], v248 offset:0\n"
    "v_add_f32 v246, v246, v221\n"
    "v_add_f32 v246, v246, v222\n"
    "v_mfma_f32_16x16x32_bf16 v[224:227], a[196:199], a[116:119], v[224:227]\n"
    "ds_read_b128 a[196:199], v249 offset:0\n"
    "v_add_f32 v246, v246, v223\n"
    "v_xor_b32 v251, 32768, v251\n"
    "v_mfma_f32_16x16x32_bf16 v[224:227], a[200:203], a[120:123], v[224:227]\n"
    "ds_read_b128 a[200:203], v248 offset:1024\n"
    "v_mfma_f32_16x16x32_bf16 v[224:227], a[204:207], a[124:127], v[224:227]\n"
    "ds_read_b128 a[204:207], v249 offset:1024\n"
    "v_mfma_f32_16x16x32_bf16 v[228:231], a[208:211], a[112:115], 0\n"
    "ds_read_b128 a[208:211], v248 offset:4096\n"
    "v_mfma_f32_16x16x32_bf16 v[228:231], a[212:215], a[116:119], v[228:231]\n"
    "ds_read_b128 a[212:215], v249 offset:4096\n"
    "v_mfma_f32_16x16x32_bf16 v[228:231], a[216:219], a[120:123], v[228:231]\n"
    "ds_read_b128 a[216:219], v248 offset:5120\n"
    "v_exp_f32 v224, v224\n"
    "v_mfma_f32_16x16x32_bf16 v[228:231], a[220:223], a[124:127], v[228:231]\n"
    "ds_read_b128 a[220:223], v249 offset:5120\n"
    "v_exp_f32 v225, v225\n"
    "v_mfma_f32_16x16x32_bf16 v[128:131], v[192:195], v[232:235], v[128:131]\n"
    "v_exp_f32 v226, v226\n"
    "v_cvt_pk_bf16_f32 v236, v224, v225\n"
    "v_mfma_f32_16x16x32_bf16 v[132:135], v[196:199], v[232:235], v[132:135]\n"
    "v_exp_f32 v227, v227\n"
    "v_add_f32 v247, v247, v224\n"
    "v_mfma_f32_16x16x32_bf16 v[136:139], v[200:203], v[232:235], v[136:139]\n"
    "v_exp_f32 v228, v228\n"
    "v_cvt_pk_bf16_f32 v237, v226, v227\n"
    "v_mfma_f32_16x16x32_bf16 v[140:143], v[204:207], v[232:235], v[140:143]\n"
    "v_exp_f32 v229, v229\n"
    "v_add_f32 v247, v247, v225\n"
    "v_mfma_f32_16x16x32_bf16 v[144:147], v[208:211], v[232:235], v[144:147]\n"
    "v_exp_f32 v230, v230\n"
    "v_cvt_pk_bf16_f32 v238, v228, v229\n"
    "v_mfma_f32_16x16x32_bf16 v[148:151], v[212:215], v[232:235], v[148:151]\n"
    "s_waitcnt lgkmcnt(10)\n"
    "v_exp_f32 v231, v231\n"
    "v_add_f32 v247, v247, v226\n"
    "v_mfma_f32_16x16x32_bf16 v[152:155], a[248:251], v[232:235], v[152:155]\n"
    "s_waitcnt lgkmcnt(8)\n"
    "v_cvt_pk_bf16_f32 v239, v230, v231\n"
    "v_add_f32 v247, v247, v227\n"
    "v_add_f32 v247, v247, v228\n"
    "v_mfma_f32_16x16x32_bf16 v[156:159], a[252:255], v[232:235], v[156:159]\n"
    "s_waitcnt lgkmcnt(7)\n"
    "v_add_f32 v247, v247, v229\n"
    "v_add_f32 v247, v247, v230\n"
    "v_add_f32 v247, v247, v231\n"
    "v_mfma_f32_16x16x32_bf16 v[216:219], a[192:195], a[0:3], 0\n"
    "s_waitcnt lgkmcnt(6)\n"
    "v_mfma_f32_16x16x32_bf16 v[216:219], a[196:199], a[4:7], v[216:219]\n"
    "s_waitcnt lgkmcnt(5)\n"
    "v_mfma_f32_16x16x32_bf16 v[216:219], a[200:203], a[8:11], v[216:219]\n"
    "s_waitcnt lgkmcnt(4)\n"
    "s_waitcnt vmcnt(4)\n"
    "s_barrier\n"
    "ds_read_b64_tr_b16 a[224:225], v250 offset:0\n"
    "ds_read_b64_tr_b16 a[226:227], v250 offset:4096\n"
    "v_mfma_f32_16x16x32_bf16 v[216:219], a[204:207], a[12:15], v[216:219]\n"
    "s_waitcnt lgkmcnt(5)\n"
    "ds_read_b64_tr_b16 a[228:229], v250 offset:8\n"
    "ds_read_b64_tr_b16 a[230:231], v250 offset:4104\n"
    "v_mfma_f32_16x16x32_bf16 v[220:223], a[208:211], a[0:3], 0\n"
    "s_waitcnt lgkmcnt(6)\n"
    "ds_read_b64_tr_b16 a[232:233], v251 offset:0\n"
    "ds_read_b64_tr_b16 a[234:235], v251 offset:4096\n"
    "v_mfma_f32_16x16x32_bf16 v[220:223], a[212:215], a[4:7], v[220:223]\n"
    "s_waitcnt lgkmcnt(7)\n"
    "ds_read_b64_tr_b16 a[236:237], v251 offset:8\n"
    "ds_read_b64_tr_b16 a[238:239], v251 offset:4104\n"
    "v_mfma_f32_16x16x32_bf16 v[220:223], a[216:219], a[8:11], v[220:223]\n"
    "s_waitcnt lgkmcnt(8)\n"
    "v_exp_f32 v216, v216\n"
    "v_mfma_f32_16x16x32_bf16 v[220:223], a[220:223], a[12:15], v[220:223]\n"
    "v_exp_f32 v217, v217\n"
    "v_mfma_f32_16x16x32_bf16 v[160:163], v[192:195], v[236:239], v[160:163]\n"
    "v_exp_f32 v218, v218\n"
    "v_mfma_f32_16x16x32_bf16 v[164:167], v[196:199], v[236:239], v[164:167]\n"
    "v_exp_f32 v219, v219\n"
    "v_mfma_f32_16x16x32_bf16 v[168:171], v[200:203], v[236:239], v[168:171]\n"
    "v_exp_f32 v220, v220\n"
    "v_mfma_f32_16x16x32_bf16 v[172:175], v[204:207], v[236:239], v[172:175]\n"
    "v_exp_f32 v221, v221\n"
    "v_mfma_f32_16x16x32_bf16 v[176:179], v[208:211], v[236:239], v[176:179]\n"
    "v_exp_f32 v222, v222\n"
    "v_mfma_f32_16x16x32_bf16 v[180:183], v[212:215], v[236:239], v[180:183]\n"
    "v_exp_f32 v223, v223\n"
    "v_mfma_f32_16x16x32_bf16 v[184:187], a[248:251], v[236:239], v[184:187]\n"
    "v_cvt_pk_bf16_f32 v232, v216, v217\n"
    "v_cvt_pk_bf16_f32 v233, v218, v219\n"
    "v_mfma_f32_16x16x32_bf16 v[188:191], a[252:255], v[236:239], v[188:191]\n"
    "v_cvt_pk_bf16_f32 v234, v220, v221\n"
    "v_cvt_pk_bf16_f32 v235, v222, v223\n"
    "v_mfma_f32_16x16x32_bf16 v[224:227], a[192:195], a[16:19], 0\n"
    "ds_read_b64_tr_b16 a[240:241], v250 offset:1024\n"
    "ds_read_b64_tr_b16 a[242:243], v250 offset:5120\n"
    "v_mfma_f32_16x16x32_bf16 v[224:227], a[196:199], a[20:23], v[224:227]\n"
    "ds_read_b64_tr_b16 a[244:245], v250 offset:1032\n"
    "ds_read_b64_tr_b16 a[246:247], v250 offset:5128\n"
    "v_mfma_f32_16x16x32_bf16 v[224:227], a[200:203], a[24:27], v[224:227]\n"
    "ds_read_b64_tr_b16 a[248:249], v251 offset:1024\n"
    "ds_read_b64_tr_b16 a[250:251], v251 offset:5120\n"
    "v_mfma_f32_16x16x32_bf16 v[224:227], a[204:207], a[28:31], v[224:227]\n"
    "ds_read_b64_tr_b16 a[252:253], v251 offset:1032\n"
    "ds_read_b64_tr_b16 a[254:255], v251 offset:5128\n"
    "v_mfma_f32_16x16x32_bf16 v[228:231], a[208:211], a[16:19], 0\n"
    "v_add_f32 v240, v240, v216\n"
    "v_add_f32 v240, v240, v217\n"
    "v_mfma_f32_16x16x32_bf16 v[228:231], a[212:215], a[20:23], v[228:231]\n"
    "v_add_f32 v240, v240, v218\n"
    "v_add_f32 v240, v240, v219\n"
    "v_mfma_f32_16x16x32_bf16 v[228:231], a[216:219], a[24:27], v[228:231]\n"
    "v_add_f32 v240, v240, v220\n"
    "v_add_f32 v240, v240, v221\n"
    "v_mfma_f32_16x16x32_bf16 v[228:231], a[220:223], a[28:31], v[228:231]\n"
    "s_waitcnt lgkmcnt(14)\n"
    "v_add_f32 v240, v240, v222\n"
    "v_add_f32 v240, v240, v223\n"
    "v_mfma_f32_16x16x32_bf16 a[128:131], a[224:227], v[232:235], a[128:131]\n"
    "s_waitcnt lgkmcnt(12)\n"
    "v_exp_f32 v224, v224\n"
    "v_mfma_f32_16x16x32_bf16 a[132:135], a[228:231], v[232:235], a[132:135]\n"
    "s_waitcnt lgkmcnt(10)\n"
    "v_exp_f32 v225, v225\n"
    "v_mfma_f32_16x16x32_bf16 a[136:139], a[232:235], v[232:235], a[136:139]\n"
    "s_waitcnt lgkmcnt(8)\n"
    "v_exp_f32 v226, v226\n"
    "v_mfma_f32_16x16x32_bf16 a[140:143], a[236:239], v[232:235], a[140:143]\n"
    "s_waitcnt lgkmcnt(6)\n"
    "v_exp_f32 v227, v227\n"
    "v_mfma_f32_16x16x32_bf16 a[144:147], a[240:243], v[232:235], a[144:147]\n"
    "s_waitcnt lgkmcnt(4)\n"
    "v_exp_f32 v228, v228\n"
    "v_mfma_f32_16x16x32_bf16 a[148:151], a[244:247], v[232:235], a[148:151]\n"
    "s_waitcnt lgkmcnt(2)\n"
    "v_exp_f32 v229, v229\n"
    "v_mfma_f32_16x16x32_bf16 a[152:155], a[248:251], v[232:235], a[152:155]\n"
    "s_waitcnt lgkmcnt(0)\n"
    "v_exp_f32 v230, v230\n"
    "v_mfma_f32_16x16x32_bf16 a[156:159], a[252:255], v[232:235], a[156:159]\n"
    "v_exp_f32 v231, v231\n"
    "v_mfma_f32_16x16x32_bf16 v[216:219], a[192:195], a[32:35], 0\n"
    "v_cvt_pk_bf16_f32 v236, v224, v225\n"
    "v_cvt_pk_bf16_f32 v237, v226, v227\n"
    "v_mfma_f32_16x16x32_bf16 v[216:219], a[196:199], a[36:39], v[216:219]\n"
    "v_cvt_pk_bf16_f32 v238, v228, v229\n"
    "v_cvt_pk_bf16_f32 v239, v230, v231\n"
    "v_mfma_f32_16x16x32_bf16 v[216:219], a[200:203], a[40:43], v[216:219]\n"
    "v_add_f32 v241, v241, v224\n"
    "v_add_f32 v241, v241, v225\n"
    "v_mfma_f32_16x16x32_bf16 v[216:219], a[204:207], a[44:47], v[216:219]\n"
    "v_add_f32 v241, v241, v226\n"
    "v_add_f32 v241, v241, v227\n"
    "v_mfma_f32_16x16x32_bf16 v[220:223], a[208:211], a[32:35], 0\n"
    "v_add_f32 v241, v241, v228\n"
    "v_add_f32 v241, v241, v229\n"
    "v_mfma_f32_16x16x32_bf16 v[220:223], a[212:215], a[36:39], v[220:223]\n"
    "v_add_f32 v241, v241, v230\n"
    "v_add_f32 v241, v241, v231\n"
    "v_mfma_f32_16x16x32_bf16 v[220:223], a[216:219], a[40:43], v[220:223]\n"
    "v_exp_f32 v216, v216\n"
    "v_mfma_f32_16x16x32_bf16 v[220:223], a[220:223], a[44:47], v[220:223]\n"
    "v_exp_f32 v217, v217\n"
    "v_mfma_f32_16x16x32_bf16 a[160:163], a[224:227], v[236:239], a[160:163]\n"
    "v_exp_f32 v218, v218\n"
    "v_mfma_f32_16x16x32_bf16 a[164:167], a[228:231], v[236:239], a[164:167]\n"
    "v_exp_f32 v219, v219\n"
    "v_mfma_f32_16x16x32_bf16 a[168:171], a[232:235], v[236:239], a[168:171]\n"
    "v_exp_f32 v220, v220\n"
    "v_mfma_f32_16x16x32_bf16 a[172:175], a[236:239], v[236:239], a[172:175]\n"
    "v_exp_f32 v221, v221\n"
    "v_mfma_f32_16x16x32_bf16 a[176:179], a[240:243], v[236:239], a[176:179]\n"
    "v_exp_f32 v222, v222\n"
    "v_mfma_f32_16x16x32_bf16 a[180:183], a[244:247], v[236:239], a[180:183]\n"
    "v_exp_f32 v223, v223\n"
    "v_mfma_f32_16x16x32_bf16 a[184:187], a[248:251], v[236:239], a[184:187]\n"
    "v_cvt_pk_bf16_f32 v232, v216, v217\n"
    "v_cvt_pk_bf16_f32 v233, v218, v219\n"
    "v_mfma_f32_16x16x32_bf16 a[188:191], a[252:255], v[236:239], a[188:191]\n"
    "v_cvt_pk_bf16_f32 v234, v220, v221\n"
    "v_cvt_pk_bf16_f32 v235, v222, v223\n"
    "v_mfma_f32_16x16x32_bf16 v[224:227], a[192:195], a[48:51], 0\n"
    "v_add_f32 v242, v242, v216\n"
    "v_add_f32 v242, v242, v217\n"
    "v_mfma_f32_16x16x32_bf16 v[224:227], a[196:199], a[52:55], v[224:227]\n"
    "v_add_f32 v242, v242, v218\n"
    "v_add_f32 v242, v242, v219\n"
    "v_mfma_f32_16x16x32_bf16 v[224:227], a[200:203], a[56:59], v[224:227]\n"
    "v_add_f32 v242, v242, v220\n"
    "v_add_f32 v242, v242, v221\n"
    "v_mfma_f32_16x16x32_bf16 v[224:227], a[204:207], a[60:63], v[224:227]\n"
    "v_add_f32 v242, v242, v222\n"
    "v_add_f32 v242, v242, v223\n"
    "v_mfma_f32_16x16x32_bf16 v[228:231], a[208:211], a[48:51], 0\n"
    "v_mfma_f32_16x16x32_bf16 v[228:231], a[212:215], a[52:55], v[228:231]\n"
    "v_mfma_f32_16x16x32_bf16 v[228:231], a[216:219], a[56:59], v[228:231]\n"
    "v_exp_f32 v224, v224\n"
    "v_mfma_f32_16x16x32_bf16 v[228:231], a[220:223], a[60:63], v[228:231]\n"
    "v_exp_f32 v225, v225\n"
    "v_mfma_f32_16x16x32_bf16 v[0:3], a[224:227], v[232:235], v[0:3]\n"
    "v_exp_f32 v226, v226\n"
    "v_mfma_f32_16x16x32_bf16 v[4:7], a[228:231], v[232:235], v[4:7]\n"
    "v_exp_f32 v227, v227\n"
    "v_mfma_f32_16x16x32_bf16 v[8:11], a[232:235], v[232:235], v[8:11]\n"
    "v_exp_f32 v228, v228\n"
    "v_mfma_f32_16x16x32_bf16 v[12:15], a[236:239], v[232:235], v[12:15]\n"
    "v_exp_f32 v229, v229\n"
    "v_mfma_f32_16x16x32_bf16 v[16:19], a[240:243], v[232:235], v[16:19]\n"
    "v_exp_f32 v230, v230\n"
    "v_mfma_f32_16x16x32_bf16 v[20:23], a[244:247], v[232:235], v[20:23]\n"
    "v_exp_f32 v231, v231\n"
    "v_mfma_f32_16x16x32_bf16 v[24:27], a[248:251], v[232:235], v[24:27]\n"
    "v_cvt_pk_bf16_f32 v236, v224, v225\n"
    "v_cvt_pk_bf16_f32 v237, v226, v227\n"
    "v_mfma_f32_16x16x32_bf16 v[28:31], a[252:255], v[232:235], v[28:31]\n"
    "v_cvt_pk_bf16_f32 v238, v228, v229\n"
    "v_cvt_pk_bf16_f32 v239, v230, v231\n"
    "v_mfma_f32_16x16x32_bf16 v[216:219], a[192:195], a[64:67], 0\n"
    "v_add_f32 v243, v243, v224\n"
    "v_add_f32 v243, v243, v225\n"
    "v_mfma_f32_16x16x32_bf16 v[216:219], a[196:199], a[68:71], v[216:219]\n"
    "v_add_f32 v243, v243, v226\n"
    "v_add_f32 v243, v243, v227\n"
    "v_mfma_f32_16x16x32_bf16 v[216:219], a[200:203], a[72:75], v[216:219]\n"
    "v_add_f32 v243, v243, v228\n"
    "v_add_f32 v243, v243, v229\n"
    "v_mfma_f32_16x16x32_bf16 v[216:219], a[204:207], a[76:79], v[216:219]\n"
    "v_add_f32 v243, v243, v230\n"
    "v_add_f32 v243, v243, v231\n"
    "v_mfma_f32_16x16x32_bf16 v[220:223], a[208:211], a[64:67], 0\n"
    "v_mfma_f32_16x16x32_bf16 v[220:223], a[212:215], a[68:71], v[220:223]\n"
    "v_mfma_f32_16x16x32_bf16 v[220:223], a[216:219], a[72:75], v[220:223]\n"
    "v_exp_f32 v216, v216\n"
    "v_mfma_f32_16x16x32_bf16 v[220:223], a[220:223], a[76:79], v[220:223]\n"
    "v_exp_f32 v217, v217\n"
    "v_mfma_f32_16x16x32_bf16 v[32:35], a[224:227], v[236:239], v[32:35]\n"
    "v_exp_f32 v218, v218\n"
    "v_mfma_f32_16x16x32_bf16 v[36:39], a[228:231], v[236:239], v[36:39]\n"
    "v_exp_f32 v219, v219\n"
    "v_mfma_f32_16x16x32_bf16 v[40:43], a[232:235], v[236:239], v[40:43]\n"
    "v_exp_f32 v220, v220\n"
    "v_mfma_f32_16x16x32_bf16 v[44:47], a[236:239], v[236:239], v[44:47]\n"
    "v_exp_f32 v221, v221\n"
    "v_mfma_f32_16x16x32_bf16 v[48:51], a[240:243], v[236:239], v[48:51]\n"
    "v_exp_f32 v222, v222\n"
    "v_mfma_f32_16x16x32_bf16 v[52:55], a[244:247], v[236:239], v[52:55]\n"
    "v_exp_f32 v223, v223\n"
    "v_mfma_f32_16x16x32_bf16 v[56:59], a[248:251], v[236:239], v[56:59]\n"
    "v_cvt_pk_bf16_f32 v232, v216, v217\n"
    "v_cvt_pk_bf16_f32 v233, v218, v219\n"
    "v_mfma_f32_16x16x32_bf16 v[60:63], a[252:255], v[236:239], v[60:63]\n"
    "v_cvt_pk_bf16_f32 v234, v220, v221\n"
    "v_cvt_pk_bf16_f32 v235, v222, v223\n"
    "v_mfma_f32_16x16x32_bf16 v[224:227], a[192:195], a[80:83], 0\n"
    "v_add_f32 v244, v244, v216\n"
    "v_add_f32 v244, v244, v217\n"
    "v_mfma_f32_16x16x32_bf16 v[224:227], a[196:199], a[84:87], v[224:227]\n"
    "v_add_f32 v244, v244, v218\n"
    "v_add_f32 v244, v244, v219\n"
    "v_mfma_f32_16x16x32_bf16 v[224:227], a[200:203], a[88:91], v[224:227]\n"
    "v_add_f32 v244, v244, v220\n"
    "v_add_f32 v244, v244, v221\n"
    "v_mfma_f32_16x16x32_bf16 v[224:227], a[204:207], a[92:95], v[224:227]\n"
    "v_add_f32 v244, v244, v222\n"
    "v_add_f32 v244, v244, v223\n"
    "v_mfma_f32_16x16x32_bf16 v[228:231], a[208:211], a[80:83], 0\n"
    "v_mfma_f32_16x16x32_bf16 v[228:231], a[212:215], a[84:87], v[228:231]\n"
    "v_mfma_f32_16x16x32_bf16 v[228:231], a[216:219], a[88:91], v[228:231]\n"
    "v_exp_f32 v224, v224\n"
    "v_mfma_f32_16x16x32_bf16 v[228:231], a[220:223], a[92:95], v[228:231]\n"
    "v_exp_f32 v225, v225\n"
    "v_mfma_f32_16x16x32_bf16 v[64:67], a[224:227], v[232:235], v[64:67]\n"
    "v_exp_f32 v226, v226\n"
    "v_mfma_f32_16x16x32_bf16 v[68:71], a[228:231], v[232:235], v[68:71]\n"
    "v_exp_f32 v227, v227\n"
    "v_mfma_f32_16x16x32_bf16 v[72:75], a[232:235], v[232:235], v[72:75]\n"
    "v_exp_f32 v228, v228\n"
    "v_mfma_f32_16x16x32_bf16 v[76:79], a[236:239], v[232:235], v[76:79]\n"
    "v_exp_f32 v229, v229\n"
    "v_mfma_f32_16x16x32_bf16 v[80:83], a[240:243], v[232:235], v[80:83]\n"
    "v_exp_f32 v230, v230\n"
    "v_mfma_f32_16x16x32_bf16 v[84:87], a[244:247], v[232:235], v[84:87]\n"
    "v_exp_f32 v231, v231\n"
    "v_mfma_f32_16x16x32_bf16 v[88:91], a[248:251], v[232:235], v[88:91]\n"
    "v_cvt_pk_bf16_f32 v236, v224, v225\n"
    "v_cvt_pk_bf16_f32 v237, v226, v227\n"
    "v_mfma_f32_16x16x32_bf16 v[92:95], a[252:255], v[232:235], v[92:95]\n"
    "v_cvt_pk_bf16_f32 v238, v228, v229\n"
    "v_cvt_pk_bf16_f32 v239, v230, v231\n"
    "v_mfma_f32_16x16x32_bf16 v[216:219], a[192:195], a[96:99], 0\n"
    "v_add_f32 v245, v245, v224\n"
    "v_add_f32 v245, v245, v225\n"
    "v_mfma_f32_16x16x32_bf16 v[216:219], a[196:199], a[100:103], v[216:219]\n"
    "v_add_f32 v245, v245, v226\n"
    "v_add_f32 v245, v245, v227\n"
    "v_mfma_f32_16x16x32_bf16 v[216:219], a[200:203], a[104:107], v[216:219]\n"
    "v_add_f32 v245, v245, v228\n"
    "v_add_f32 v245, v245, v229\n"
    "v_mfma_f32_16x16x32_bf16 v[216:219], a[204:207], a[108:111], v[216:219]\n"
    "v_add_f32 v245, v245, v230\n"
    "v_add_f32 v245, v245, v231\n"
    "v_mfma_f32_16x16x32_bf16 v[220:223], a[208:211], a[96:99], 0\n"
    "v_mfma_f32_16x16x32_bf16 v[220:223], a[212:215], a[100:103], v[220:223]\n"
    "v_mfma_f32_16x16x32_bf16 v[220:223], a[216:219], a[104:107], v[220:223]\n"
    "v_exp_f32 v216, v216\n"
    "v_mfma_f32_16x16x32_bf16 v[220:223], a[220:223], a[108:111], v[220:223]\n"
    "v_exp_f32 v217, v217\n"
    "v_mfma_f32_16x16x32_bf16 v[96:99], a[224:227], v[236:239], v[96:99]\n"
    "s_waitcnt vmcnt(0)\n"
    "s_barrier\n"
    "v_exp_f32 v218, v218\n"
    "v_mfma_f32_16x16x32_bf16 v[100:103], a[228:231], v[236:239], v[100:103]\n"
    "v_exp_f32 v219, v219\n"
    "v_mfma_f32_16x16x32_bf16 v[104:107], a[232:235], v[236:239], v[104:107]\n"
    "v_exp_f32 v220, v220\n"
    "v_mfma_f32_16x16x32_bf16 v[108:111], a[236:239], v[236:239], v[108:111]\n"
    "v_exp_f32 v221, v221\n"
    "v_mfma_f32_16x16x32_bf16 v[112:115], a[240:243], v[236:239], v[112:115]\n"
    "v_exp_f32 v222, v222\n"
    "v_mfma_f32_16x16x32_bf16 v[116:119], a[244:247], v[236:239], v[116:119]\n"
    "v_exp_f32 v223, v223\n"
    "v_mfma_f32_16x16x32_bf16 v[120:123], a[248:251], v[236:239], v[120:123]\n"
    "v_cvt_pk_bf16_f32 v232, v216, v217\n"
    "v_cvt_pk_bf16_f32 v233, v218, v219\n"
    "v_mfma_f32_16x16x32_bf16 v[124:127], a[252:255], v[236:239], v[124:127]\n"
    "v_cvt_pk_bf16_f32 v234, v220, v221\n"
    "v_cvt_pk_bf16_f32 v235, v222, v223\n"
    "v_mfma_f32_16x16x32_bf16 v[224:227], a[192:195], a[112:115], 0\n"
    "ds_read_b128 a[192:195], v248 offset:16384\n"
    "v_add_f32 v246, v246, v216\n"
    "v_mfma_f32_16x16x32_bf16 v[224:227], a[196:199], a[116:119], v[224:227]\n"
    "ds_read_b128 a[196:199], v249 offset:16384\n"
    "v_add_f32 v246, v246, v217\n"
    "v_mfma_f32_16x16x32_bf16 v[224:227], a[200:203], a[120:123], v[224:227]\n"
    "ds_read_b128 a[200:203], v248 offset:17408\n"
    "v_add_f32 v246, v246, v218\n"
    "v_mfma_f32_16x16x32_bf16 v[224:227], a[204:207], a[124:127], v[224:227]\n"
    "ds_read_b128 a[204:207], v249 offset:17408\n"
    "v_add_f32 v246, v246, v219\n"
    "v_mfma_f32_16x16x32_bf16 v[228:231], a[208:211], a[112:115], 0\n"
    "ds_read_b128 a[208:211], v248 offset:20480\n"
    "v_add_f32 v246, v246, v220\n"
    "v_mfma_f32_16x16x32_bf16 v[228:231], a[212:215], a[116:119], v[228:231]\n"
    "ds_read_b128 a[212:215], v249 offset:20480\n"
    "v_add_f32 v246, v246, v221\n"
    "v_mfma_f32_16x16x32_bf16 v[228:231], a[216:219], a[120:123], v[228:231]\n"
    "ds_read_b128 a[216:219], v248 offset:21504\n"
    "v_add_f32 v246, v246, v222\n"
    "v_mfma_f32_16x16x32_bf16 v[228:231], a[220:223], a[124:127], v[228:231]\n"
    "ds_read_b128 a[220:223], v249 offset:21504\n"
    "v_add_f32 v246, v246, v223\n"
    "v_mfma_f32_16x16x32_bf16 v[128:131], a[224:227], v[232:235], v[128:131]\n"
    "v_exp_f32 v224, v224\n"
    "v_mfma_f32_16x16x32_bf16 v[132:135], a[228:231], v[232:235], v[132:135]\n"
    "v_exp_f32 v225, v225\n"
    "v_mfma_f32_16x16x32_bf16 v[136:139], a[232:235], v[232:235], v[136:139]\n"
    "v_exp_f32 v226, v226\n"
    "v_mfma_f32_16x16x32_bf16 v[140:143], a[236:239], v[232:235], v[140:143]\n"
    "v_exp_f32 v227, v227\n"
    "v_mfma_f32_16x16x32_bf16 v[144:147], a[240:243], v[232:235], v[144:147]\n"
    "v_exp_f32 v228, v228\n"
    "v_mfma_f32_16x16x32_bf16 v[148:151], a[244:247], v[232:235], v[148:151]\n"
    "v_exp_f32 v229, v229\n"
    "v_mfma_f32_16x16x32_bf16 v[152:155], a[248:251], v[232:235], v[152:155]\n"
    "v_exp_f32 v230, v230\n"
    "v_mfma_f32_16x16x32_bf16 v[156:159], a[252:255], v[232:235], v[156:159]\n"
    "s_waitcnt lgkmcnt(7)\n"
    "v_exp_f32 v231, v231\n"
    "v_mfma_f32_16x16x32_bf16 v[216:219], a[192:195], a[0:3], 0\n"
    "s_waitcnt lgkmcnt(6)\n"
    "v_cvt_pk_bf16_f32 v236, v224, v225\n"
    "v_cvt_pk_bf16_f32 v237, v226, v227\n"
    "v_mfma_f32_16x16x32_bf16 v[216:219], a[196:199], a[4:7], v[216:219]\n"
    "s_waitcnt lgkmcnt(5)\n"
    "v_cvt_pk_bf16_f32 v238, v228, v229\n"
    "v_cvt_pk_bf16_f32 v239, v230, v231\n"
    "v_mfma_f32_16x16x32_bf16 v[216:219], a[200:203], a[8:11], v[216:219]\n"
    "s_waitcnt lgkmcnt(4)\n"
    "v_add_f32 v247, v247, v224\n"
    "v_add_f32 v247, v247, v225\n"
    "v_mfma_f32_16x16x32_bf16 v[216:219], a[204:207], a[12:15], v[216:219]\n"
    "s_waitcnt lgkmcnt(3)\n"
    "v_add_f32 v247, v247, v226\n"
    "v_add_f32 v247, v247, v227\n"
    "v_mfma_f32_16x16x32_bf16 v[220:223], a[208:211], a[0:3], 0\n"
    "s_waitcnt lgkmcnt(2)\n"
    "ds_read_b64_tr_b16 v[192:193], v250 offset:16384\n"
    "ds_read_b64_tr_b16 v[194:195], v250 offset:20480\n"
    "v_mfma_f32_16x16x32_bf16 v[220:223], a[212:215], a[4:7], v[220:223]\n"
    "s_waitcnt lgkmcnt(3)\n"
    "ds_read_b64_tr_b16 v[196:197], v250 offset:16392\n"
    "ds_read_b64_tr_b16 v[198:199], v250 offset:20488\n"
    "v_mfma_f32_16x16x32_bf16 v[220:223], a[216:219], a[8:11], v[220:223]\n"
    "s_waitcnt lgkmcnt(4)\n"
    "ds_read_b64_tr_b16 v[200:201], v251 offset:16384\n"
    "ds_read_b64_tr_b16 v[202:203], v251 offset:20480\n"
    "v_mfma_f32_16x16x32_bf16 v[220:223], a[220:223], a[12:15], v[220:223]\n"
    "v_add_f32 v247, v247, v228\n"
    "v_add_f32 v247, v247, v229\n"
    "v_mfma_f32_16x16x32_bf16 v[160:163], a[224:227], v[236:239], v[160:163]\n"
    "v_add_f32 v247, v247, v230\n"
    "v_add_f32 v247, v247, v231\n"
    "v_mfma_f32_16x16x32_bf16 v[164:167], a[228:231], v[236:239], v[164:167]\n"
    "ds_read_b64_tr_b16 v[204:205], v251 offset:16392\n"
    "ds_read_b64_tr_b16 v[206:207], v251 offset:20488\n"
    "v_mfma_f32_16x16x32_bf16 v[168:171], a[232:235], v[236:239], v[168:171]\n"
    "ds_read_b64_tr_b16 v[208:209], v250 offset:17408\n"
    "ds_read_b64_tr_b16 v[210:211], v250 offset:21504\n"
    "v_mfma_f32_16x16x32_bf16 v[172:175], a[236:239], v[236:239], v[172:175]\n"
    "v_exp_f32 v216, v216\n"
    "v_mfma_f32_16x16x32_bf16 v[176:179], a[240:243], v[236:239], v[176:179]\n"
    "v_exp_f32 v217, v217\n"
    "v_mfma_f32_16x16x32_bf16 v[180:183], a[244:247], v[236:239], v[180:183]\n"
    "v_exp_f32 v218, v218\n"
    "v_mfma_f32_16x16x32_bf16 v[184:187], a[248:251], v[236:239], v[184:187]\n"
    "v_exp_f32 v219, v219\n"
    "v_mfma_f32_16x16x32_bf16 v[188:191], a[252:255], v[236:239], v[188:191]\n"
    "v_exp_f32 v220, v220\n"
    "v_mfma_f32_16x16x32_bf16 v[224:227], a[192:195], a[16:19], 0\n"
    "v_exp_f32 v221, v221\n"
    "v_mfma_f32_16x16x32_bf16 v[224:227], a[196:199], a[20:23], v[224:227]\n"
    "v_exp_f32 v222, v222\n"
    "v_mfma_f32_16x16x32_bf16 v[224:227], a[200:203], a[24:27], v[224:227]\n"
    "v_exp_f32 v223, v223\n"
    "v_mfma_f32_16x16x32_bf16 v[224:227], a[204:207], a[28:31], v[224:227]\n"
    "ds_read_b64_tr_b16 v[212:213], v250 offset:17416\n"
    "ds_read_b64_tr_b16 v[214:215], v250 offset:21512\n"
    "v_mfma_f32_16x16x32_bf16 v[228:231], a[208:211], a[16:19], 0\n"
    "v_cvt_pk_bf16_f32 v232, v216, v217\n"
    "v_cvt_pk_bf16_f32 v233, v218, v219\n"
    "v_mfma_f32_16x16x32_bf16 v[228:231], a[212:215], a[20:23], v[228:231]\n"
    "v_cvt_pk_bf16_f32 v234, v220, v221\n"
    "v_cvt_pk_bf16_f32 v235, v222, v223\n"
    "v_mfma_f32_16x16x32_bf16 v[228:231], a[216:219], a[24:27], v[228:231]\n"
    "ds_read_b64_tr_b16 a[248:249], v251 offset:17408\n"
    "ds_read_b64_tr_b16 a[250:251], v251 offset:21504\n"
    "v_mfma_f32_16x16x32_bf16 v[228:231], a[220:223], a[28:31], v[228:231]\n"
    "s_waitcnt lgkmcnt(12)\n"
    "ds_read_b64_tr_b16 a[252:253], v251 offset:17416\n"
    "ds_read_b64_tr_b16 a[254:255], v251 offset:21512\n"
    "v_mfma_f32_16x16x32_bf16 a[128:131], v[192:195], v[232:235], a[128:131]\n"
    "s_waitcnt lgkmcnt(12)\n"
    "v_add_f32 v240, v240, v216\n"
    "v_add_f32 v240, v240, v217\n"
    "v_mfma_f32_16x16x32_bf16 a[132:135], v[196:199], v[232:235], a[132:135]\n"
    "s_waitcnt lgkmcnt(10)\n"
    "v_add_f32 v240, v240, v218\n"
    "v_add_f32 v240, v240, v219\n"
    "v_mfma_f32_16x16x32_bf16 a[136:139], v[200:203], v[232:235], a[136:139]\n"
    "s_waitcnt lgkmcnt(8)\n"
    "v_add_f32 v240, v240, v220\n"
    "v_add_f32 v240, v240, v221\n"
    "v_mfma_f32_16x16x32_bf16 a[140:143], v[204:207], v[232:235], a[140:143]\n"
    "s_waitcnt lgkmcnt(6)\n"
    "v_add_f32 v240, v240, v222\n"
    "v_add_f32 v240, v240, v223\n"
    "v_mfma_f32_16x16x32_bf16 a[144:147], v[208:211], v[232:235], a[144:147]\n"
    "s_waitcnt lgkmcnt(4)\n"
    "v_exp_f32 v224, v224\n"
    "v_mfma_f32_16x16x32_bf16 a[148:151], v[212:215], v[232:235], a[148:151]\n"
    "s_waitcnt lgkmcnt(2)\n"
    "v_exp_f32 v225, v225\n"
    "v_mfma_f32_16x16x32_bf16 a[152:155], a[248:251], v[232:235], a[152:155]\n"
    "s_waitcnt lgkmcnt(0)\n"
    "v_exp_f32 v226, v226\n"
    "v_mfma_f32_16x16x32_bf16 a[156:159], a[252:255], v[232:235], a[156:159]\n"
    "v_exp_f32 v227, v227\n"
    "v_mfma_f32_16x16x32_bf16 v[216:219], a[192:195], a[32:35], 0\n"
    "v_exp_f32 v228, v228\n"
    "v_mfma_f32_16x16x32_bf16 v[216:219], a[196:199], a[36:39], v[216:219]\n"
    "v_exp_f32 v229, v229\n"
    "v_mfma_f32_16x16x32_bf16 v[216:219], a[200:203], a[40:43], v[216:219]\n"
    "v_exp_f32 v230, v230\n"
    "v_mfma_f32_16x16x32_bf16 v[216:219], a[204:207], a[44:47], v[216:219]\n"
    "v_exp_f32 v231, v231\n"
    "v_mfma_f32_16x16x32_bf16 v[220:223], a[208:211], a[32:35], 0\n"
    "v_cvt_pk_bf16_f32 v236, v224, v225\n"
    "v_cvt_pk_bf16_f32 v237, v226, v227\n"
    "v_mfma_f32_16x16x32_bf16 v[220:223], a[212:215], a[36:39], v[220:223]\n"
    "v_cvt_pk_bf16_f32 v238, v228, v229\n"
    "v_cvt_pk_bf16_f32 v239, v230, v231\n"
    "v_mfma_f32_16x16x32_bf16 v[220:223], a[216:219], a[40:43], v[220:223]\n"
    "v_add_f32 v241, v241, v224\n"
    "v_add_f32 v241, v241, v225\n"
    "v_mfma_f32_16x16x32_bf16 v[220:223], a[220:223], a[44:47], v[220:223]\n"
    "v_add_f32 v241, v241, v226\n"
    "v_add_f32 v241, v241, v227\n"
    "v_mfma_f32_16x16x32_bf16 a[160:163], v[192:195], v[236:239], a[160:163]\n"
    "v_add_f32 v241, v241, v228\n"
    "v_add_f32 v241, v241, v229\n"
    "v_mfma_f32_16x16x32_bf16 a[164:167], v[196:199], v[236:239], a[164:167]\n"
    "v_add_f32 v241, v241, v230\n"
    "v_add_f32 v241, v241, v231\n"
    "v_mfma_f32_16x16x32_bf16 a[168:171], v[200:203], v[236:239], a[168:171]\n"
    "v_exp_f32 v216, v216\n"
    "v_mfma_f32_16x16x32_bf16 a[172:175], v[204:207], v[236:239], a[172:175]\n"
    "v_exp_f32 v217, v217\n"
    "v_mfma_f32_16x16x32_bf16 a[176:179], v[208:211], v[236:239], a[176:179]\n"
    "v_exp_f32 v218, v218\n"
    "v_mfma_f32_16x16x32_bf16 a[180:183], v[212:215], v[236:239], a[180:183]\n"
    "v_exp_f32 v219, v219\n"
    "v_mfma_f32_16x16x32_bf16 a[184:187], a[248:251], v[236:239], a[184:187]\n"
    "v_exp_f32 v220, v220\n"
    "v_mfma_f32_16x16x32_bf16 a[188:191], a[252:255], v[236:239], a[188:191]\n"
    "v_exp_f32 v221, v221\n"
    "v_mfma_f32_16x16x32_bf16 v[224:227], a[192:195], a[48:51], 0\n"
    "v_exp_f32 v222, v222\n"
    "v_mfma_f32_16x16x32_bf16 v[224:227], a[196:199], a[52:55], v[224:227]\n"
    "v_exp_f32 v223, v223\n"
    "v_mfma_f32_16x16x32_bf16 v[224:227], a[200:203], a[56:59], v[224:227]\n"
    "v_cvt_pk_bf16_f32 v232, v216, v217\n"
    "v_cvt_pk_bf16_f32 v233, v218, v219\n"
    "v_mfma_f32_16x16x32_bf16 v[224:227], a[204:207], a[60:63], v[224:227]\n"
    "v_cvt_pk_bf16_f32 v234, v220, v221\n"
    "v_cvt_pk_bf16_f32 v235, v222, v223\n"
    "v_mfma_f32_16x16x32_bf16 v[228:231], a[208:211], a[48:51], 0\n"
    "v_add_f32 v242, v242, v216\n"
    "v_add_f32 v242, v242, v217\n"
    "v_mfma_f32_16x16x32_bf16 v[228:231], a[212:215], a[52:55], v[228:231]\n"
    "v_add_f32 v242, v242, v218\n"
    "v_add_f32 v242, v242, v219\n"
    "v_mfma_f32_16x16x32_bf16 v[228:231], a[216:219], a[56:59], v[228:231]\n"
    "v_add_f32 v242, v242, v220\n"
    "v_add_f32 v242, v242, v221\n"
    "v_mfma_f32_16x16x32_bf16 v[228:231], a[220:223], a[60:63], v[228:231]\n"
    "v_add_f32 v242, v242, v222\n"
    "v_add_f32 v242, v242, v223\n"
    "v_mfma_f32_16x16x32_bf16 v[0:3], v[192:195], v[232:235], v[0:3]\n"
    "v_exp_f32 v224, v224\n"
    "v_mfma_f32_16x16x32_bf16 v[4:7], v[196:199], v[232:235], v[4:7]\n"
    "v_exp_f32 v225, v225\n"
    "v_mfma_f32_16x16x32_bf16 v[8:11], v[200:203], v[232:235], v[8:11]\n"
    "v_exp_f32 v226, v226\n"
    "v_mfma_f32_16x16x32_bf16 v[12:15], v[204:207], v[232:235], v[12:15]\n"
    "v_exp_f32 v227, v227\n"
    "v_mfma_f32_16x16x32_bf16 v[16:19], v[208:211], v[232:235], v[16:19]\n"
    "v_exp_f32 v228, v228\n"
    "v_mfma_f32_16x16x32_bf16 v[20:23], v[212:215], v[232:235], v[20:23]\n"
    "v_exp_f32 v229, v229\n"
    "v_mfma_f32_16x16x32_bf16 v[24:27], a[248:251], v[232:235], v[24:27]\n"
    "v_exp_f32 v230, v230\n"
    "v_mfma_f32_16x16x32_bf16 v[28:31], a[252:255], v[232:235], v[28:31]\n"
    "v_exp_f32 v231, v231\n"
    "v_mfma_f32_16x16x32_bf16 v[216:219], a[192:195], a[64:67], 0\n"
    "v_cvt_pk_bf16_f32 v236, v224, v225\n"
    "v_cvt_pk_bf16_f32 v237, v226, v227\n"
    "v_mfma_f32_16x16x32_bf16 v[216:219], a[196:199], a[68:71], v[216:219]\n"
    "v_cvt_pk_bf16_f32 v238, v228, v229\n"
    "v_cvt_pk_bf16_f32 v239, v230, v231\n"
    "v_mfma_f32_16x16x32_bf16 v[216:219], a[200:203], a[72:75], v[216:219]\n"
    "v_add_f32 v243, v243, v224\n"
    "v_add_f32 v243, v243, v225\n"
    "v_mfma_f32_16x16x32_bf16 v[216:219], a[204:207], a[76:79], v[216:219]\n"
    "v_add_f32 v243, v243, v226\n"
    "v_add_f32 v243, v243, v227\n"
    "v_mfma_f32_16x16x32_bf16 v[220:223], a[208:211], a[64:67], 0\n"
    "v_add_f32 v243, v243, v228\n"
    "v_add_f32 v243, v243, v229\n"
    "v_mfma_f32_16x16x32_bf16 v[220:223], a[212:215], a[68:71], v[220:223]\n"
    "v_add_f32 v243, v243, v230\n"
    "v_add_f32 v243, v243, v231\n"
    "v_mfma_f32_16x16x32_bf16 v[220:223], a[216:219], a[72:75], v[220:223]\n"
    "v_exp_f32 v216, v216\n"
    "v_mfma_f32_16x16x32_bf16 v[220:223], a[220:223], a[76:79], v[220:223]\n"
    "v_exp_f32 v217, v217\n"
    "v_mfma_f32_16x16x32_bf16 v[32:35], v[192:195], v[236:239], v[32:35]\n"
    "s_waitcnt vmcnt(63)\n"
    "s_barrier\n"
    "v_exp_f32 v218, v218\n"
    "v_mfma_f32_16x16x32_bf16 v[36:39], v[196:199], v[236:239], v[36:39]\n"
    "v_exp_f32 v219, v219\n"
    "v_mfma_f32_16x16x32_bf16 v[40:43], v[200:203], v[236:239], v[40:43]\n"
    "v_exp_f32 v220, v220\n"
    "v_mfma_f32_16x16x32_bf16 v[44:47], v[204:207], v[236:239], v[44:47]\n"
    "v_exp_f32 v221, v221\n"
    "v_mfma_f32_16x16x32_bf16 v[48:51], v[208:211], v[236:239], v[48:51]\n"
    "v_exp_f32 v222, v222\n"
    "v_mfma_f32_16x16x32_bf16 v[52:55], v[212:215], v[236:239], v[52:55]\n"
    "v_exp_f32 v223, v223\n"
    "v_mfma_f32_16x16x32_bf16 v[56:59], a[248:251], v[236:239], v[56:59]\n"
    "v_cvt_pk_bf16_f32 v232, v216, v217\n"
    "v_cvt_pk_bf16_f32 v233, v218, v219\n"
    "v_mfma_f32_16x16x32_bf16 v[60:63], a[252:255], v[236:239], v[60:63]\n"
    "v_cvt_pk_bf16_f32 v234, v220, v221\n"
    "v_cvt_pk_bf16_f32 v235, v222, v223\n"
    "v_mfma_f32_16x16x32_bf16 v[224:227], a[192:195], a[80:83], 0\n"
    "v_add_f32 v244, v244, v216\n"
    "v_add_f32 v244, v244, v217\n"
    "v_mfma_f32_16x16x32_bf16 v[224:227], a[196:199], a[84:87], v[224:227]\n"
    "v_add_f32 v244, v244, v218\n"
    "v_add_f32 v244, v244, v219\n"
    "v_mfma_f32_16x16x32_bf16 v[224:227], a[200:203], a[88:91], v[224:227]\n"
    "v_add_f32 v244, v244, v220\n"
    "v_add_f32 v244, v244, v221\n"
    "v_mfma_f32_16x16x32_bf16 v[224:227], a[204:207], a[92:95], v[224:227]\n"
    "v_add_f32 v244, v244, v222\n"
    "v_add_f32 v244, v244, v223\n"
    "v_mfma_f32_16x16x32_bf16 v[228:231], a[208:211], a[80:83], 0\n"
    "v_xor_b32 v248, 65536, v248\n"
    "v_xor_b32 v249, 65536, v249\n"
    "v_mfma_f32_16x16x32_bf16 v[228:231], a[212:215], a[84:87], v[228:231]\n"
    "v_xor_b32 v250, 65536, v250\n"
    "v_xor_b32 v251, 65536, v251\n"
    "v_mfma_f32_16x16x32_bf16 v[228:231], a[216:219], a[88:91], v[228:231]\n"
    "v_exp_f32 v224, v224\n"
    "v_mfma_f32_16x16x32_bf16 v[228:231], a[220:223], a[92:95], v[228:231]\n"
    "v_exp_f32 v225, v225\n"
    "v_mfma_f32_16x16x32_bf16 v[64:67], v[192:195], v[232:235], v[64:67]\n"
    "v_exp_f32 v226, v226\n"
    "v_mfma_f32_16x16x32_bf16 v[68:71], v[196:199], v[232:235], v[68:71]\n"
    "v_exp_f32 v227, v227\n"
    "v_mfma_f32_16x16x32_bf16 v[72:75], v[200:203], v[232:235], v[72:75]\n"
    "v_exp_f32 v228, v228\n"
    "v_mfma_f32_16x16x32_bf16 v[76:79], v[204:207], v[232:235], v[76:79]\n"
    "v_exp_f32 v229, v229\n"
    "v_mfma_f32_16x16x32_bf16 v[80:83], v[208:211], v[232:235], v[80:83]\n"
    "v_exp_f32 v230, v230\n"
    "v_mfma_f32_16x16x32_bf16 v[84:87], v[212:215], v[232:235], v[84:87]\n"
    "v_exp_f32 v231, v231\n"
    "v_mfma_f32_16x16x32_bf16 v[88:91], a[248:251], v[232:235], v[88:91]\n"
    "v_cvt_pk_bf16_f32 v236, v224, v225\n"
    "v_cvt_pk_bf16_f32 v237, v226, v227\n"
    "v_mfma_f32_16x16x32_bf16 v[92:95], a[252:255], v[232:235], v[92:95]\n"
    "v_cvt_pk_bf16_f32 v238, v228, v229\n"
    "v_cvt_pk_bf16_f32 v239, v230, v231\n"
    "v_mfma_f32_16x16x32_bf16 v[216:219], a[192:195], a[96:99], 0\n"
    "v_add_f32 v245, v245, v224\n"
    "v_add_f32 v245, v245, v225\n"
    "v_mfma_f32_16x16x32_bf16 v[216:219], a[196:199], a[100:103], v[216:219]\n"
    "v_add_f32 v245, v245, v226\n"
    "v_add_f32 v245, v245, v227\n"
    "v_mfma_f32_16x16x32_bf16 v[216:219], a[200:203], a[104:107], v[216:219]\n"
    "v_add_f32 v245, v245, v228\n"
    "v_add_f32 v245, v245, v229\n"
    "v_mfma_f32_16x16x32_bf16 v[216:219], a[204:207], a[108:111], v[216:219]\n"
    "v_add_f32 v245, v245, v230\n"
    "v_add_f32 v245, v245, v231\n"
    "v_mfma_f32_16x16x32_bf16 v[220:223], a[208:211], a[96:99], 0\n"
    "ds_read_b64_tr_b16 a[224:225], v250 offset:0\n"
    "ds_read_b64_tr_b16 a[226:227], v250 offset:4096\n"
    "v_mfma_f32_16x16x32_bf16 v[220:223], a[212:215], a[100:103], v[220:223]\n"
    "ds_read_b64_tr_b16 a[228:229], v250 offset:8\n"
    "ds_read_b64_tr_b16 a[230:231], v250 offset:4104\n"
    "v_mfma_f32_16x16x32_bf16 v[220:223], a[216:219], a[104:107], v[220:223]\n"
    "v_exp_f32 v216, v216\n"
    "v_mfma_f32_16x16x32_bf16 v[220:223], a[220:223], a[108:111], v[220:223]\n"
    "v_exp_f32 v217, v217\n"
    "v_mfma_f32_16x16x32_bf16 v[96:99], v[192:195], v[236:239], v[96:99]\n"
    "v_exp_f32 v218, v218\n"
    "v_mfma_f32_16x16x32_bf16 v[100:103], v[196:199], v[236:239], v[100:103]\n"
    "v_exp_f32 v219, v219\n"
    "v_mfma_f32_16x16x32_bf16 v[104:107], v[200:203], v[236:239], v[104:107]\n"
    "v_exp_f32 v220, v220\n"
    "v_mfma_f32_16x16x32_bf16 v[108:111], v[204:207], v[236:239], v[108:111]\n"
    "v_exp_f32 v221, v221\n"
    "v_mfma_f32_16x16x32_bf16 v[112:115], v[208:211], v[236:239], v[112:115]\n"
    "v_exp_f32 v222, v222\n"
    "v_mfma_f32_16x16x32_bf16 v[116:119], v[212:215], v[236:239], v[116:119]\n"
    "v_exp_f32 v223, v223\n"
    "v_mfma_f32_16x16x32_bf16 v[120:123], a[248:251], v[236:239], v[120:123]\n"
    "v_cvt_pk_bf16_f32 v232, v216, v217\n"
    "v_cvt_pk_bf16_f32 v233, v218, v219\n"
    "v_mfma_f32_16x16x32_bf16 v[124:127], a[252:255], v[236:239], v[124:127]\n"
    "v_cvt_pk_bf16_f32 v234, v220, v221\n"
    "v_cvt_pk_bf16_f32 v235, v222, v223\n"
    "v_mfma_f32_16x16x32_bf16 v[224:227], a[192:195], a[112:115], 0\n"
    "ds_read_b128 a[192:195], v248 offset:0\n"
    "ds_read_b64_tr_b16 a[232:233], v251 offset:0\n"
    "v_mfma_f32_16x16x32_bf16 v[224:227], a[196:199], a[116:119], v[224:227]\n"
    "ds_read_b128 a[196:199], v249 offset:0\n"
    "ds_read_b64_tr_b16 a[234:235], v251 offset:4096\n"
    "v_mfma_f32_16x16x32_bf16 v[224:227], a[200:203], a[120:123], v[224:227]\n"
    "ds_read_b128 a[200:203], v248 offset:1024\n"
    "ds_read_b64_tr_b16 a[236:237], v251 offset:8\n"
    "v_mfma_f32_16x16x32_bf16 v[224:227], a[204:207], a[124:127], v[224:227]\n"
    "ds_read_b128 a[204:207], v249 offset:1024\n"
    "ds_read_b64_tr_b16 a[238:239], v251 offset:4104\n"
    "v_mfma_f32_16x16x32_bf16 v[228:231], a[208:211], a[112:115], 0\n"
    "ds_read_b128 a[208:211], v248 offset:4096\n"
    "ds_read_b64_tr_b16 a[240:241], v250 offset:1024\n"
    "v_mfma_f32_16x16x32_bf16 v[228:231], a[212:215], a[116:119], v[228:231]\n"
    "ds_read_b128 a[212:215], v249 offset:4096\n"
    "ds_read_b64_tr_b16 a[242:243], v250 offset:5120\n"
    "v_mfma_f32_16x16x32_bf16 v[228:231], a[216:219], a[120:123], v[228:231]\n"
    "v_add_f32 v246, v246, v216\n"
    "v_add_f32 v246, v246, v217\n"
    "v_mfma_f32_16x16x32_bf16 v[228:231], a[220:223], a[124:127], v[228:231]\n"
    "v_add_f32 v246, v246, v218\n"
    "v_add_f32 v246, v246, v219\n"
    "v_mfma_f32_16x16x32_bf16 v[128:131], v[192:195], v[232:235], v[128:131]\n"
    "v_add_f32 v246, v246, v220\n"
    "v_add_f32 v246, v246, v221\n"
    "v_mfma_f32_16x16x32_bf16 v[132:135], v[196:199], v[232:235], v[132:135]\n"
    "v_add_f32 v246, v246, v222\n"
    "v_add_f32 v246, v246, v223\n"
    "v_mfma_f32_16x16x32_bf16 v[136:139], v[200:203], v[232:235], v[136:139]\n"
    "ds_read_b128 a[216:219], v248 offset:5120\n"
    "ds_read_b128 a[220:223], v249 offset:5120\n"
    "v_mfma_f32_16x16x32_bf16 v[140:143], v[204:207], v[232:235], v[140:143]\n"
    "v_exp_f32 v224, v224\n"
    "v_mfma_f32_16x16x32_bf16 v[144:147], v[208:211], v[232:235], v[144:147]\n"
    "v_exp_f32 v225, v225\n"
    "v_mfma_f32_16x16x32_bf16 v[148:151], v[212:215], v[232:235], v[148:151]\n"
    "v_exp_f32 v226, v226\n"
    "v_mfma_f32_16x16x32_bf16 v[152:155], a[248:251], v[232:235], v[152:155]\n"
    "v_exp_f32 v227, v227\n"
    "v_mfma_f32_16x16x32_bf16 v[156:159], a[252:255], v[232:235], v[156:159]\n"
    "s_waitcnt lgkmcnt(13)\n"
    "v_exp_f32 v228, v228\n"
    "v_exp_f32 v229, v229\n"
    "v_exp_f32 v230, v230\n"
    "s_mov_b32 s54, 12\n"
    "s_branch LAL_%=\n"
    "LSTUB_%=:\n"
    "s_setpc_b64 s[30:31]\n"
    "LW3_%=:\n"
    "v_mov_b32 v7, 0x3f803f80\n"
    "v_accvgpr_write_b32 a112, v7\n"
    "v_accvgpr_write_b32 a113, v7\n"
    "v_accvgpr_write_b32 a114, v7\n"
    "v_accvgpr_write_b32 a115, v7\n"
    "s_add_u32 s69, s67, 0\n"
    "s_add_u32 m0, s66, 131072\n"
    "s_nop 0\n"
    "buffer_load_dwordx4 v252, s[36:39], s69 offen offset:0 nt lds\n"
    "buffer_load_dwordx4 v252, s[36:39], s69 offen offset:2048 nt lds\n"
    "s_add_u32 m0, s66, 131968\n"
    "s_nop 0\n"
    "buffer_load_dwordx4 v252, s[36:39], s69 offen offset:128 nt lds\n"
    "buffer_load_dwordx4 v252, s[36:39], s69 offen offset:2176 nt lds\n"
    "s_add_u32 m0, s64, 0\n"
    "s_add_u32 s69, s65, 0\n"
    "buffer_load_dwordx4 v252, s[40:43], s69 offen offset:0 nt lds\n"
    "buffer_load_dwordx4 v252, s[40:43], s69 offen offset:2048 nt lds\n"
    "s_sleep 12\n"
    "s_add_u32 m0, s64, 8192\n"
    "s_add_u32 s69, s65, 0\n"
    "buffer_load_dwordx4 v252, s[44:47], s69 offen offset:0 nt lds\n"
    "buffer_load_dwordx4 v252, s[44:47], s69 offen offset:2048 nt lds\n"
    "s_add_u32 s69, s67, 16384\n"
    "s_add_u32 m0, s66, 135168\n"
    "s_nop 0\n"
    "buffer_load_dwordx4 v252, s[36:39], s69 offen offset:0 nt lds\n"
    "buffer_load_dwordx4 v252, s[36:39], s69 offen offset:2048 nt lds\n"
    "s_add_u32 m0, s66, 136064\n"
    "s_nop 0\n"
    "buffer_load_dwordx4 v252, s[36:39], s69 offen offset:128 nt lds\n"
    "buffer_load_dwordx4 v252, s[36:39], s69 offen offset:2176 nt lds\n"
    "s_nop 0\n"
    "s_waitcnt vmcnt(6)\n"
    "s_barrier\n"
    "s_waitcnt vmcnt(63)\n"
    "s_add_u32 s68, s66, 131072\n"
    "v_add_u32 v253, s68, v248\n"
    "s_add_u32 s68, s66, 131072\n"
    "v_add_u32 v254, s68, v249\n"
    "ds_read_b128 a[0:3], v253 offset:0\n"
    "ds_read_b128 a[4:7], v254 offset:0\n"
    "ds_read_b128 a[8:11], v253 offset:1024\n"
    "ds_read_b128 a[12:15], v254 offset:1024\n"
    "s_add_u32 s69, s67, 32768\n"
    "s_add_u32 m0, s66, 65536\n"
    "s_nop 0\n"
    "buffer_load_dwordx4 v252, s[36:39], s69 offen offset:0 nt lds\n"
    "buffer_load_dwordx4 v252, s[36:39], s69 offen offset:2048 nt lds\n"
    "s_add_u32 m0, s66, 66432\n"
    "s_nop 0\n"
    "buffer_load_dwordx4 v252, s[36:39], s69 offen offset:128 nt lds\n"
    "buffer_load_dwordx4 v252, s[36:39], s69 offen offset:2176 nt lds\n"
    "s_add_u32 m0, s64, 16384\n"
    "s_add_u32 s69, s65, 8192\n"
    "buffer_load_dwordx4 v252, s[40:43], s69 offen offset:0 nt lds\n"
    "buffer_load_dwordx4 v252, s[40:43], s69 offen offset:2048 nt lds\n"
    "s_add_u32 m0, s64, 24576\n"
    "s_add_u32 s69, s65, 8192\n"
    "buffer_load_dwordx4 v252, s[44:47], s69 offen offset:0 nt lds\n"
    "buffer_load_dwordx4 v252, s[44:47], s69 offen offset:2048 nt lds\n"
    "ds_read_b128 a[192:195], v248 offset:0\n"
    "ds_read_b128 a[196:199], v249 offset:0\n"
    "ds_read_b128 a[200:203], v248 offset:1024\n"
    "ds_read_b128 a[204:207], v249 offset:1024\n"
    "ds_read_b128 a[208:211], v248 offset:4096\n"
    "ds_read_b128 a[212:215], v249 offset:4096\n"
    "ds_read_b128 a[216:219], v248 offset:5120\n"
    "ds_read_b128 a[220:223], v249 offset:5120\n"
    "s_waitcnt lgkmcnt(11)\n"
    "v_mfma_f32_16x16x32_bf16 v[176:179], v[162:165], a[0:3], 0\n"
    "s_mov_b32 s56, 0\n"
    "v_mfma_f32_16x16x32_bf16 v[176:179], v[166:169], a[0:3], v[176:179]\n"
    "s_mov_b32 s57, 32768\n"
    "v_mfma_f32_16x16x32_bf16 v[180:183], v[160:163], a[0:3], 0\n"
    "s_mov_b32 s58, 65536\n"
    "v_mfma_f32_16x16x32_bf16 v[180:183], v[164:167], a[0:3], v[180:183]\n"
    "s_waitcnt lgkmcnt(10)\n"
    "s_mov_b32 s55, 32768\n"
    "v_mfma_f32_16x16x32_bf16 v[184:187], v[162:165], a[4:7], 0\n"
    "v_cvt_pk_bf16_f32 v176, v176, v177\n"
    "v_cvt_pk_bf16_f32 v178, v178, v179\n"
    "v_accvgpr_write_b32 a0, v176\n"
    "v_mfma_f32_16x16x32_bf16 v[184:187], v[166:169], a[4:7], v[184:187]\n"
    "v_accvgpr_write_b32 a1, v178\n"
    "v_mfma_f32_16x16x32_bf16 v[188:191], v[160:163], a[4:7], 0\n"
    "v_cvt_pk_bf16_f32 v180, v180, v181\n"
    "v_cvt_pk_bf16_f32 v182, v182, v183\n"
    "v_accvgpr_write_b32 a2, v180\n"
    "v_mfma_f32_16x16x32_bf16 v[188:191], v[164:167], a[4:7], v[188:191]\n"
    "s_waitcnt lgkmcnt(9)\n"
    "v_accvgpr_write_b32 a3, v182\n"
    "v_mfma_f32_16x16x32_bf16 v[176:179], v[162:165], a[8:11], 0\n"
    "v_cvt_pk_bf16_f32 v184, v184, v185\n"
    "v_cvt_pk_bf16_f32 v186, v186, v187\n"
    "v_accvgpr_write_b32 a4, v184\n"
    "v_mfma_f32_16x16x32_bf16 v[176:179], v[166:169], a[8:11], v[176:179]\n"
    "v_accvgpr_write_b32 a5, v186\n"
    "v_mfma_f32_16x16x32_bf16 v[180:183], v[160:163], a[8:11], 0\n"
    "v_cvt_pk_bf16_f32 v188, v188, v189\n"
    "v_cvt_pk_bf16_f32 v190, v190, v191\n"
    "v_accvgpr_write_b32 a6, v188\n"
    "v_mfma_f32_16x16x32_bf16 v[180:183], v[164:167], a[8:11], v[180:183]\n"
    "s_waitcnt lgkmcnt(8)\n"
    "v_accvgpr_write_b32 a7, v190\n"
    "v_mfma_f32_16x16x32_bf16 v[184:187], v[162:165], a[12:15], 0\n"
    "v_cvt_pk_bf16_f32 v176, v176, v177\n"
    "v_cvt_pk_bf16_f32 v178, v178, v179\n"
    "v_accvgpr_write_b32 a8, v176\n"
    "v_mfma_f32_16x16x32_bf16 v[184:187], v[166:169], a[12:15], v[184:187]\n"
    "v_accvgpr_write_b32 a9, v178\n"
    "v_mfma_f32_16x16x32_bf16 v[188:191], v[160:163], a[12:15], 0\n"
    "v_cvt_pk_bf16_f32 v180, v180, v181\n"
    "v_cvt_pk_bf16_f32 v182, v182, v183\n"
    "v_accvgpr_write_b32 a10, v180\n"
    "v_mfma_f32_16x16x32_bf16 v[188:191], v[164:167], a[12:15], v[188:191]\n"
    "s_waitcnt lgkmcnt(7)\n"
    "s_waitcnt vmcnt(8)\n"
    "s_add_u32 s68, s66, 135168\n"
    "v_add_u32 v253, s68, v248\n"
    "s_add_u32 s68, s66, 135168\n"
    "v_add_u32 v254, s68, v249\n"
    "ds_read_b128 a[16:19], v253 offset:0\n"
    "ds_read_b128 a[20:23], v254 offset:0\n"
    "ds_read_b128 a[24:27], v253 offset:1024\n"
    "ds_read_b128 a[28:31], v254 offset:1024\n"
    "s_add_u32 s69, s67, 49152\n"
    "s_add_u32 m0, s66, 69632\n"
    "s_nop 0\n"
    "buffer_load_dwordx4 v252, s[36:39], s69 offen offset:0 nt lds\n"
    "buffer_load_dwordx4 v252, s[36:39], s69 offen offset:2048 nt lds\n"
    "s_add_u32 m0, s66, 70528\n"
    "s_nop 0\n"
    "buffer_load_dwordx4 v252, s[36:39], s69 offen offset:128 nt lds\n"
    "buffer_load_dwordx4 v252, s[36:39], s69 offen offset:2176 nt lds\n"
    "v_mfma_f32_16x16x32_bf16 v[216:219], a[192:195], a[0:3], 0\n"
    "s_waitcnt lgkmcnt(10)\n"
    "v_accvgpr_write_b32 a11, v182\n"
    "v_cvt_pk_bf16_f32 v184, v184, v185\n"
    "v_cvt_pk_bf16_f32 v186, v186, v187\n"
    "v_mfma_f32_16x16x32_bf16 v[216:219], a[196:199], a[4:7], v[216:219]\n"
    "s_waitcnt lgkmcnt(9)\n"
    "v_accvgpr_write_b32 a12, v184\n"
    "v_accvgpr_write_b32 a13, v186\n"
    "v_mfma_f32_16x16x32_bf16 v[216:219], a[200:203], a[8:11], v[216:219]\n"
    "s_waitcnt lgkmcnt(8)\n"
    "v_cvt_pk_bf16_f32 v188, v188, v189\n"
    "v_cvt_pk_bf16_f32 v190, v190, v191\n"
    "v_accvgpr_write_b32 a14, v188\n"
    "v_accvgpr_write_b32 a15, v190\n"
    "s_nop 1\n"
    "v_mfma_f32_16x16x32_bf16 v[216:219], a[204:207], a[12:15], v[216:219]\n"
    "s_waitcnt lgkmcnt(3)\n"
    "s_waitcnt vmcnt(63)\n"
    "s_barrier\n"
    "ds_read_b64_tr_b16 v[192:193], v250 offset:0\n"
    "ds_read_b64_tr_b16 v[194:195], v250 offset:4096\n"
    "ds_read_b64_tr_b16 v[196:197], v250 offset:8\n"
    "ds_read_b64_tr_b16 v[198:199], v250 offset:4104\n"
    "ds_read_b64_tr_b16 v[200:201], v251 offset:0\n"
    "ds_read_b64_tr_b16 v[202:203], v251 offset:4096\n"
    "ds_read_b64_tr_b16 v[204:205], v251 offset:8\n"
    "ds_read_b64_tr_b16 v[206:207], v251 offset:4104\n"
    "ds_read_b64_tr_b16 v[208:209], v250 offset:1024\n"
    "ds_read_b64_tr_b16 v[210:211], v250 offset:5120\n"
    "ds_read_b64_tr_b16 v[212:213], v250 offset:1032\n"
    "ds_read_b64_tr_b16 v[214:215], v250 offset:5128\n"
    "ds_read_b64_tr_b16 a[248:249], v251 offset:1024\n"
    "ds_read_b64_tr_b16 a[250:251], v251 offset:5120\n"
    "ds_read_b64_tr_b16 a[252:253], v251 offset:1032\n"
    "ds_read_b64_tr_b16 a[254:255], v251 offset:5128\n"
    "v_mfma_f32_16x16x32_bf16 v[176:179], v[162:165], a[16:19], 0\n"
    "v_mfma_f32_16x16x32_bf16 v[176:179], v[166:169], a[16:19], v[176:179]\n"
    "v_mfma_f32_16x16x32_bf16 v[180:183], v[160:163], a[16:19], 0\n"
    "v_exp_f32 v216, v216\n"
    "v_mfma_f32_16x16x32_bf16 v[180:183], v[164:167], a[16:19], v[180:183]\n"
    "v_exp_f32 v217, v217\n"
    "v_add_f32 v240, v240, v216\n"
    "v_mfma_f32_16x16x32_bf16 v[220:223], a[208:211], a[0:3], 0\n"
    "v_cvt_pk_bf16_f32 v176, v176, v177\n"
    "v_cvt_pk_bf16_f32 v178, v178, v179\n"
    "v_accvgpr_write_b32 a16, v176\n"
    "v_mfma_f32_16x16x32_bf16 v[220:223], a[212:215], a[4:7], v[220:223]\n"
    "v_accvgpr_write_b32 a17, v178\n"
    "v_exp_f32 v218, v218\n"
    "v_mfma_f32_16x16x32_bf16 v[220:223], a[216:219], a[8:11], v[220:223]\n"
    "v_cvt_pk_bf16_f32 v180, v180, v181\n"
    "v_cvt_pk_bf16_f32 v182, v182, v183\n"
    "v_accvgpr_write_b32 a18, v180\n"
    "v_mfma_f32_16x16x32_bf16 v[220:223], a[220:223], a[12:15], v[220:223]\n"
    "s_waitcnt lgkmcnt(15)\n"
    "v_accvgpr_write_b32 a19, v182\n"
    "v_exp_f32 v219, v219\n"
    "v_mfma_f32_16x16x32_bf16 v[184:187], v[162:165], a[20:23], 0\n"
    "v_cvt_pk_bf16_f32 v232, v216, v217\n"
    "v_cvt_pk_bf16_f32 v233, v218, v219\n"
    "v_add_f32 v240, v240, v217\n"
    "v_mfma_f32_16x16x32_bf16 v[184:187], v[166:169], a[20:23], v[184:187]\n"
    "v_add_f32 v240, v240, v218\n"
    "v_add_f32 v240, v240, v219\n"
    "v_mfma_f32_16x16x32_bf16 v[188:191], v[160:163], a[20:23], 0\n"
    "v_exp_f32 v220, v220\n"
    "v_mfma_f32_16x16x32_bf16 v[188:191], v[164:167], a[20:23], v[188:191]\n"
    "v_exp_f32 v221, v221\n"
    "v_add_f32 v240, v240, v220\n"
    "v_mfma_f32_16x16x32_bf16 v[176:179], v[162:165], a[24:27], 0\n"
    "v_cvt_pk_bf16_f32 v184, v184, v185\n"
    "v_cvt_pk_bf16_f32 v186, v186, v187\n"
    "v_accvgpr_write_b32 a20, v184\n"
    "v_mfma_f32_16x16x32_bf16 v[176:179], v[166:169], a[24:27], v[176:179]\n"
    "v_accvgpr_write_b32 a21, v186\n"
    "v_exp_f32 v222, v222\n"
    "v_mfma_f32_16x16x32_bf16 v[180:183], v[160:163], a[24:27], 0\n"
    "v_cvt_pk_bf16_f32 v188, v188, v189\n"
    "v_cvt_pk_bf16_f32 v190, v190, v191\n"
    "v_accvgpr_write_b32 a22, v188\n"
    "v_mfma_f32_16x16x32_bf16 v[180:183], v[164:167], a[24:27], v[180:183]\n"
    "v_accvgpr_write_b32 a23, v190\n"
    "v_exp_f32 v223, v223\n"
    "v_mfma_f32_16x16x32_bf16 v[184:187], v[162:165], a[28:31], 0\n"
    "v_cvt_pk_bf16_f32 v176, v176, v177\n"
    "v_cvt_pk_bf16_f32 v178, v178, v179\n"
    "v_accvgpr_write_b32 a24, v176\n"
    "v_mfma_f32_16x16x32_bf16 v[184:187], v[166:169], a[28:31], v[184:187]\n"
    "v_accvgpr_write_b32 a25, v178\n"
    "v_cvt_pk_bf16_f32 v234, v220, v221\n"
    "v_cvt_pk_bf16_f32 v235, v222, v223\n"
    "v_mfma_f32_16x16x32_bf16 v[188:191], v[160:163], a[28:31], 0\n"
    "v_cvt_pk_bf16_f32 v180, v180, v181\n"
    "v_cvt_pk_bf16_f32 v182, v182, v183\n"
    "v_accvgpr_write_b32 a26, v180\n"
    "v_mfma_f32_16x16x32_bf16 v[188:191], v[164:167], a[28:31], v[188:191]\n"
    "v_accvgpr_write_b32 a27, v182\n"
    "v_add_f32 v240, v240, v221\n"
    "v_add_f32 v240, v240, v222\n"
    "v_mfma_f32_16x16x32_bf16 v[224:227], a[192:195], a[16:19], 0\n"
    "v_add_f32 v240, v240, v223\n"
    "v_cvt_pk_bf16_f32 v184, v184, v185\n"
    "v_cvt_pk_bf16_f32 v186, v186, v187\n"
    "v_accvgpr_write_b32 a28, v184\n"
    "v_accvgpr_write_b32 a29, v186\n"
    "s_waitcnt vmcnt(4)\n"
    "s_barrier\n"
    "ds_read_b64_tr_b16 a[224:225], v250 offset:16384\n"
    "ds_read_b64_tr_b16 a[226:227], v250 offset:20480\n"
    "ds_read_b64_tr_b16 a[228:229], v250 offset:16392\n"
    "ds_read_b64_tr_b16 a[230:231], v250 offset:20488\n"
    "ds_read_b64_tr_b16 a[232:233], v251 offset:16384\n"
    "ds_read_b64_tr_b16 a[234:235], v251 offset:20480\n"
    "ds_read_b64_tr_b16 a[236:237], v251 offset:16392\n"
    "ds_read_b64_tr_b16 a[238:239], v251 offset:20488\n"
    "ds_read_b64_tr_b16 a[240:241], v250 offset:17408\n"
    "ds_read_b64_tr_b16 a[242:243], v250 offset:21504\n"
    "ds_read_b64_tr_b16 a[244:245], v250 offset:17416\n"
    "ds_read_b64_tr_b16 a[246:247], v250 offset:21512\n"
    "ds_read_b128 a[192:195], v248 offset:16384\n"
    "v_mfma_f32_16x16x32_bf16 v[224:227], a[196:199], a[20:23], v[224:227]\n"
    "ds_read_b128 a[196:199], v249 offset:16384\n"
    "v_mfma_f32_16x16x32_bf16 v[224:227], a[200:203], a[24:27], v[224:227]\n"
    "v_cvt_pk_bf16_f32 v188, v188, v189\n"
    "v_cvt_pk_bf16_f32 v190, v190, v191\n"
    "v_accvgpr_write_b32 a30, v188\n"
    "v_accvgpr_write_b32 a31, v190\n"
    "s_nop 1\n"
    "v_mfma_f32_16x16x32_bf16 v[224:227], a[204:207], a[28:31], v[224:227]\n"
    "ds_read_b128 a[200:203], v248 offset:17408\n"
    "ds_read_b128 a[204:207], v249 offset:17408\n"
    "v_mfma_f32_16x16x32_bf16 v[228:231], a[208:211], a[16:19], 0\n"
    "ds_read_b128 a[208:211], v248 offset:20480\n"
    "v_mfma_f32_16x16x32_bf16 v[228:231], a[212:215], a[20:23], v[228:231]\n"
    "ds_read_b128 a[212:215], v249 offset:20480\n"
    "v_mfma_f32_16x16x32_bf16 v[228:231], a[216:219], a[24:27], v[228:231]\n"
    "ds_read_b128 a[216:219], v248 offset:21504\n"
    "v_exp_f32 v224, v224\n"
    "v_mfma_f32_16x16x32_bf16 v[228:231], a[220:223], a[28:31], v[228:231]\n"
    "s_waitcnt lgkmcnt(15)\n"
    "ds_read_b128 a[220:223], v249 offset:21504\n"
    "v_exp_f32 v225, v225\n"
    "v_mfma_f32_16x16x32_bf16 a[128:131], v[192:195], v[232:235], 0\n"
    "v_exp_f32 v226, v226\n"
    "v_cvt_pk_bf16_f32 v236, v224, v225\n"
    "v_mfma_f32_16x16x32_bf16 a[132:135], v[196:199], v[232:235], 0\n"
    "v_exp_f32 v227, v227\n"
    "v_add_f32 v241, v241, v224\n"
    "v_mfma_f32_16x16x32_bf16 a[136:139], v[200:203], v[232:235], 0\n"
    "v_exp_f32 v228, v228\n"
    "v_cvt_pk_bf16_f32 v237, v226, v227\n"
    "v_mfma_f32_16x16x32_bf16 a[140:143], v[204:207], v[232:235], 0\n"
    "v_exp_f32 v229, v229\n"
    "v_add_f32 v241, v241, v225\n"
    "v_mfma_f32_16x16x32_bf16 a[144:147], v[208:211], v[232:235], 0\n"
    "v_exp_f32 v230, v230\n"
    "v_cvt_pk_bf16_f32 v238, v228, v229\n"
    "v_mfma_f32_16x16x32_bf16 a[148:151], v[212:215], v[232:235], 0\n"
    "v_exp_f32 v231, v231\n"
    "v_add_f32 v241, v241, v226\n"
    "v_mfma_f32_16x16x32_bf16 a[152:155], a[248:251], v[232:235], 0\n"
    "v_cvt_pk_bf16_f32 v239, v230, v231\n"
    "v_add_f32 v241, v241, v227\n"
    "v_add_f32 v241, v241, v228\n"
    "v_mfma_f32_16x16x32_bf16 a[156:159], a[252:255], v[232:235], 0\n"
    "s_waitcnt lgkmcnt(7)\n"
    "v_add_f32 v241, v241, v229\n"
    "v_add_f32 v241, v241, v230\n"
    "v_add_f32 v241, v241, v231\n"
    "v_mfma_f32_16x16x32_bf16 v[216:219], a[192:195], a[0:3], 0\n"
    "s_waitcnt lgkmcnt(6)\n"
    "v_mfma_f32_16x16x32_bf16 v[216:219], a[196:199], a[4:7], v[216:219]\n"
    "s_waitcnt lgkmcnt(5)\n"
    "v_mfma_f32_16x16x32_bf16 v[216:219], a[200:203], a[8:11], v[216:219]\n"
    "s_waitcnt lgkmcnt(4)\n"
    "v_mfma_f32_16x16x32_bf16 v[216:219], a[204:207], a[12:15], v[216:219]\n"
    "s_waitcnt lgkmcnt(3)\n"
    "v_mfma_f32_16x16x32_bf16 v[220:223], a[208:211], a[0:3], 0\n"
    "s_waitcnt lgkmcnt(2)\n"
    "v_mfma_f32_16x16x32_bf16 v[220:223], a[212:215], a[4:7], v[220:223]\n"
    "s_waitcnt lgkmcnt(1)\n"
    "v_mfma_f32_16x16x32_bf16 v[220:223], a[216:219], a[8:11], v[220:223]\n"
    "s_waitcnt lgkmcnt(0)\n"
    "v_exp_f32 v216, v216\n"
    "v_mfma_f32_16x16x32_bf16 v[220:223], a[220:223], a[12:15], v[220:223]\n"
    "v_exp_f32 v217, v217\n"
    "v_add_f32 v240, v240, v216\n"
    "v_mfma_f32_16x16x32_bf16 a[160:163], v[192:195], v[236:239], 0\n"
    "v_exp_f32 v218, v218\n"
    "v_cvt_pk_bf16_f32 v232, v216, v217\n"
    "v_mfma_f32_16x16x32_bf16 a[164:167], v[196:199], v[236:239], 0\n"
    "v_exp_f32 v219, v219\n"
    "v_add_f32 v240, v240, v217\n"
    "v_mfma_f32_16x16x32_bf16 a[168:171], v[200:203], v[236:239], 0\n"
    "v_exp_f32 v220, v220\n"
    "v_cvt_pk_bf16_f32 v233, v218, v219\n"
    "v_mfma_f32_16x16x32_bf16 a[172:175], v[204:207], v[236:239], 0\n"
    "v_exp_f32 v221, v221\n"
    "v_add_f32 v240, v240, v218\n"
    "v_mfma_f32_16x16x32_bf16 a[176:179], v[208:211], v[236:239], 0\n"
    "v_exp_f32 v222, v222\n"
    "v_cvt_pk_bf16_f32 v234, v220, v221\n"
    "v_mfma_f32_16x16x32_bf16 a[180:183], v[212:215], v[236:239], 0\n"
    "v_exp_f32 v223, v223\n"
    "v_add_f32 v240, v240, v219\n"
    "v_mfma_f32_16x16x32_bf16 a[184:187], a[248:251], v[236:239], 0\n"
    "ds_read_b64_tr_b16 a[248:249], v251 offset:17408\n"
    "ds_read_b64_tr_b16 a[250:251], v251 offset:21504\n"
    "v_cvt_pk_bf16_f32 v235, v222, v223\n"
    "v_mfma_f32_16x16x32_bf16 a[188:191], a[252:255], v[236:239], 0\n"
    "s_waitcnt vmcnt(63)\n"
    "s_add_u32 s68, s66, 65536\n"
    "v_add_u32 v253, s68, v248\n"
    "s_add_u32 s68, s66, 65536\n"
    "v_add_u32 v254, s68, v249\n"
    "ds_read_b128 a[32:35], v253 offset:0\n"
    "ds_read_b128 a[36:39], v254 offset:0\n"
    "ds_read_b128 a[40:43], v253 offset:1024\n"
    "ds_read_b128 a[44:47], v254 offset:1024\n"
    "s_add_u32 s69, s67, 65536\n"
    "s_add_u32 m0, s66, 131072\n"
    "s_nop 0\n"
    "buffer_load_dwordx4 v252, s[36:39], s69 offen offset:0 nt lds\n"
    "buffer_load_dwordx4 v252, s[36:39], s69 offen offset:2048 nt lds\n"
    "s_add_u32 m0, s66, 131968\n"
    "s_nop 0\n"
    "buffer_load_dwordx4 v252, s[36:39], s69 offen offset:128 nt lds\n"
    "buffer_load_dwordx4 v252, s[36:39], s69 offen offset:2176 nt lds\n"
    "v_mfma_f32_16x16x32_bf16 v[224:227], a[192:195], a[16:19], 0\n"
    "ds_read_b64_tr_b16 a[252:253], v251 offset:17416\n"
    "ds_read_b64_tr_b16 a[254:255], v251 offset:21512\n"
    "v_add_f32 v240, v240, v220\n"
    "v_mfma_f32_16x16x32_bf16 v[224:227], a[196:199], a[20:23], v[224:227]\n"
    "v_add_f32 v240, v240, v221\n"
    "v_add_f32 v240, v240, v222\n"
    "v_add_f32 v240, v240, v223\n"
    "v_mfma_f32_16x16x32_bf16 v[224:227], a[200:203], a[24:27], v[224:227]\n"
    "ds_read_b64_tr_b16 v[192:193], v250 offset:0\n"
    "ds_read_b64_tr_b16 v[194:195], v250 offset:4096\n"
    "ds_read_b64_tr_b16 v[196:197], v250 offset:8\n"
    "v_mfma_f32_16x16x32_bf16 v[224:227], a[204:207], a[28:31], v[224:227]\n"
    "s_waitcnt lgkmcnt(8)\n"
    "ds_read_b64_tr_b16 v[198:199], v250 offset:4104\n"
    "ds_read_b64_tr_b16 v[200:201], v251 offset:0\n"
    "ds_read_b64_tr_b16 v[202:203], v251 offset:4096\n"
    "v_mfma_f32_16x16x32_bf16 v[0:3], v[162:165], a[32:35], 0\n"
    "ds_read_b64_tr_b16 v[204:205], v251 offset:8\n"
    "ds_read_b64_tr_b16 v[206:207], v251 offset:4104\n"
    "ds_read_b64_tr_b16 v[208:209], v250 offset:1024\n"
    "v_mfma_f32_16x16x32_bf16 v[0:3], v[166:169], a[32:35], v[0:3]\n"
    "ds_read_b64_tr_b16 v[210:211], v250 offset:5120\n"
    "ds_read_b64_tr_b16 v[212:213], v250 offset:1032\n"
    "ds_read_b64_tr_b16 v[214:215], v250 offset:5128\n"
    "v_mfma_f32_16x16x32_bf16 v[4:7], v[160:163], a[32:35], 0\n"
    "v_exp_f32 v224, v224\n"
    "v_mfma_f32_16x16x32_bf16 v[4:7], v[164:167], a[32:35], v[4:7]\n"
    "v_exp_f32 v225, v225\n"
    "v_add_f32 v241, v241, v224\n"
    "v_mfma_f32_16x16x32_bf16 v[228:231], a[208:211], a[16:19], 0\n"
    "v_cvt_pk_bf16_f32 v0, v0, v1\n"
    "v_cvt_pk_bf16_f32 v2, v2, v3\n"
    "v_accvgpr_write_b32 a32, v0\n"
    "v_mfma_f32_16x16x32_bf16 v[228:231], a[212:215], a[20:23], v[228:231]\n"
    "v_accvgpr_write_b32 a33, v2\n"
    "v_exp_f32 v226, v226\n"
    "v_mfma_f32_16x16x32_bf16 v[228:231], a[216:219], a[24:27], v[228:231]\n"
    "v_cvt_pk_bf16_f32 v4, v4, v5\n"
    "v_cvt_pk_bf16_f32 v6, v6, v7\n"
    "v_accvgpr_write_b32 a34, v4\n"
    "v_mfma_f32_16x16x32_bf16 v[228:231], a[220:223], a[28:31], v[228:231]\n"
    "s_waitcnt lgkmcnt(15)\n"
    "v_accvgpr_write_b32 a35, v6\n"
    "v_exp_f32 v227, v227\n"
    "v_mfma_f32_16x16x32_bf16 v[8:11], v[162:165], a[36:39], 0\n"
    "v_cvt_pk_bf16_f32 v236, v224, v225\n"
    "v_cvt_pk_bf16_f32 v237, v226, v227\n"
    "v_add_f32 v241, v241, v225\n"
    "v_mfma_f32_16x16x32_bf16 v[8:11], v[166:169], a[36:39], v[8:11]\n"
    "v_add_f32 v241, v241, v226\n"
    "v_add_f32 v241, v241, v227\n"
    "v_mfma_f32_16x16x32_bf16 v[12:15], v[160:163], a[36:39], 0\n"
    "v_exp_f32 v228, v228\n"
    "v_mfma_f32_16x16x32_bf16 v[12:15], v[164:167], a[36:39], v[12:15]\n"
    "v_exp_f32 v229, v229\n"
    "v_add_f32 v241, v241, v228\n"
    "v_mfma_f32_16x16x32_bf16 v[0:3], v[162:165], a[40:43], 0\n"
    "v_cvt_pk_bf16_f32 v8, v8, v9\n"
    "v_cvt_pk_bf16_f32 v10, v10, v11\n"
    "v_accvgpr_write_b32 a36, v8\n"
    "v_mfma_f32_16x16x32_bf16 v[0:3], v[166:169], a[40:43], v[0:3]\n"
    "v_accvgpr_write_b32 a37, v10\n"
    "v_exp_f32 v230, v230\n"
    "v_mfma_f32_16x16x32_bf16 v[4:7], v[160:163], a[40:43], 0\n"
    "v_cvt_pk_bf16_f32 v12, v12, v13\n"
    "v_cvt_pk_bf16_f32 v14, v14, v15\n"
    "v_accvgpr_write_b32 a38, v12\n"
    "v_mfma_f32_16x16x32_bf16 v[4:7], v[164:167], a[40:43], v[4:7]\n"
    "v_accvgpr_write_b32 a39, v14\n"
    "v_exp_f32 v231, v231\n"
    "v_mfma_f32_16x16x32_bf16 a[128:131], a[224:227], v[232:235], a[128:131]\n"
    "v_cvt_pk_bf16_f32 v0, v0, v1\n"
    "v_cvt_pk_bf16_f32 v2, v2, v3\n"
    "v_accvgpr_write_b32 a40, v0\n"
    "v_mfma_f32_16x16x32_bf16 a[132:135], a[228:231], v[232:235], a[132:135]\n"
    "v_accvgpr_write_b32 a41, v2\n"
    "v_cvt_pk_bf16_f32 v238, v228, v229\n"
    "v_cvt_pk_bf16_f32 v239, v230, v231\n"
    "v_mfma_f32_16x16x32_bf16 a[136:139], a[232:235], v[232:235], a[136:139]\n"
    "v_cvt_pk_bf16_f32 v4, v4, v5\n"
    "v_cvt_pk_bf16_f32 v6, v6, v7\n"
    "v_accvgpr_write_b32 a42, v4\n"
    "v_mfma_f32_16x16x32_bf16 a[140:143], a[236:239], v[232:235], a[140:143]\n"
    "s_waitcnt lgkmcnt(14)\n"
    "v_accvgpr_write_b32 a43, v6\n"
    "v_add_f32 v241, v241, v229\n"
    "v_add_f32 v241, v241, v230\n"
    "v_mfma_f32_16x16x32_bf16 v[8:11], v[162:165], a[44:47], 0\n"
    "v_add_f32 v241, v241, v231\n"
    "v_mfma_f32_16x16x32_bf16 v[8:11], v[166:169], a[44:47], v[8:11]\n"
    "v_mfma_f32_16x16x32_bf16 v[12:15], v[160:163], a[44:47], 0\n"
    "v_mfma_f32_16x16x32_bf16 v[12:15], v[164:167], a[44:47], v[12:15]\n"
    "v_mfma_f32_16x16x32_bf16 a[144:147], a[240:243], v[232:235], a[144:147]\n"
    "v_cvt_pk_bf16_f32 v8, v8, v9\n"
    "v_cvt_pk_bf16_f32 v10, v10, v11\n"
    "v_accvgpr_write_b32 a44, v8\n"
    "v_mfma_f32_16x16x32_bf16 a[148:151], a[244:247], v[232:235], a[148:151]\n"
    "v_accvgpr_write_b32 a45, v10\n"
    "v_mfma_f32_16x16x32_bf16 a[152:155], a[248:251], v[232:235], a[152:155]\n"
    "s_waitcnt lgkmcnt(12)\n"
    "v_cvt_pk_bf16_f32 v12, v12, v13\n"
    "v_cvt_pk_bf16_f32 v14, v14, v15\n"
    "v_accvgpr_write_b32 a46, v12\n"
    "v_mfma_f32_16x16x32_bf16 a[156:159], a[252:255], v[232:235], a[156:159]\n"
    "s_waitcnt vmcnt(4)\n"
    "s_add_u32 s68, s66, 69632\n"
    "v_add_u32 v253, s68, v248\n"
    "s_add_u32 s68, s66, 69632\n"
    "v_add_u32 v254, s68, v249\n"
    "ds_read_b128 a[48:51], v253 offset:0\n"
    "ds_read_b128 a[52:55], v254 offset:0\n"
    "ds_read_b128 a[56:59], v253 offset:1024\n"
    "ds_read_b128 a[60:63], v254 offset:1024\n"
    "s_add_u32 s69, s67, 81920\n"
    "s_add_u32 m0, s66, 135168\n"
    "s_nop 0\n"
    "buffer_load_dwordx4 v252, s[36:39], s69 offen offset:0 nt lds\n"
    "buffer_load_dwordx4 v252, s[36:39], s69 offen offset:2048 nt lds\n"
    "s_add_u32 m0, s66, 136064\n"
    "s_nop 0\n"
    "buffer_load_dwordx4 v252, s[36:39], s69 offen offset:128 nt lds\n"
    "buffer_load_dwordx4 v252, s[36:39], s69 offen offset:2176 nt lds\n"
    "v_mfma_f32_16x16x32_bf16 v[216:219], a[192:195], a[32:35], 0\n"
    "v_accvgpr_write_b32 a47, v14\n"
    "v_mfma_f32_16x16x32_bf16 v[216:219], a[196:199], a[36:39], v[216:219]\n"
    "v_mfma_f32_16x16x32_bf16 v[216:219], a[200:203], a[40:43], v[216:219]\n"
    "v_mfma_f32_16x16x32_bf16 v[216:219], a[204:207], a[44:47], v[216:219]\n"
    "s_waitcnt lgkmcnt(3)\n"
    "v_mfma_f32_16x16x32_bf16 v[32:35], v[162:165], a[48:51], 0\n"
    "v_mfma_f32_16x16x32_bf16 v[32:35], v[166:169], a[48:51], v[32:35]\n"
    "v_mfma_f32_16x16x32_bf16 v[36:39], v[160:163], a[48:51], 0\n"
    "v_exp_f32 v216, v216\n"
    "v_mfma_f32_16x16x32_bf16 v[36:39], v[164:167], a[48:51], v[36:39]\n"
    "v_exp_f32 v217, v217\n"
    "v_mfma_f32_16x16x32_bf16 v[220:223], a[208:211], a[32:35], 0\n"
    "v_cvt_pk_bf16_f32 v32, v32, v33\n"
    "v_cvt_pk_bf16_f32 v34, v34, v35\n"
    "v_accvgpr_write_b32 a48, v32\n"
    "v_mfma_f32_16x16x32_bf16 v[220:223], a[212:215], a[36:39], v[220:223]\n"
    "v_accvgpr_write_b32 a49, v34\n"
    "v_exp_f32 v218, v218\n"
    "v_mfma_f32_16x16x32_bf16 v[220:223], a[216:219], a[40:43], v[220:223]\n"
    "v_cvt_pk_bf16_f32 v36, v36, v37\n"
    "v_cvt_pk_bf16_f32 v38, v38, v39\n"
    "v_accvgpr_write_b32 a50, v36\n"
    "v_mfma_f32_16x16x32_bf16 v[220:223], a[220:223], a[44:47], v[220:223]\n"
    "s_waitcnt lgkmcnt(2)\n"
    "v_accvgpr_write_b32 a51, v38\n"
    "v_exp_f32 v219, v219\n"
    "v_mfma_f32_16x16x32_bf16 v[40:43], v[162:165], a[52:55], 0\n"
    "v_cvt_pk_bf16_f32 v232, v216, v217\n"
    "v_cvt_pk_bf16_f32 v233, v218, v219\n"
    "v_mfma_f32_16x16x32_bf16 v[40:43], v[166:169], a[52:55], v[40:43]\n"
    "v_mfma_f32_16x16x32_bf16 v[44:47], v[160:163], a[52:55], 0\n"
    "v_exp_f32 v220, v220\n"
    "v_mfma_f32_16x16x32_bf16 v[44:47], v[164:167], a[52:55], v[44:47]\n"
    "s_waitcnt lgkmcnt(1)\n"
    "v_exp_f32 v221, v221\n"
    "v_mfma_f32_16x16x32_bf16 v[32:35], v[162:165], a[56:59], 0\n"
    "v_cvt_pk_bf16_f32 v40, v40, v41\n"
    "v_cvt_pk_bf16_f32 v42, v42, v43\n"
    "v_accvgpr_write_b32 a52, v40\n"
    "v_mfma_f32_16x16x32_bf16 v[32:35], v[166:169], a[56:59], v[32:35]\n"
    "v_accvgpr_write_b32 a53, v42\n"
    "v_exp_f32 v222, v222\n"
    "v_mfma_f32_16x16x32_bf16 v[36:39], v[160:163], a[56:59], 0\n"
    "v_cvt_pk_bf16_f32 v44, v44, v45\n"
    "v_cvt_pk_bf16_f32 v46, v46, v47\n"
    "v_accvgpr_write_b32 a54, v44\n"
    "v_mfma_f32_16x16x32_bf16 v[36:39], v[164:167], a[56:59], v[36:39]\n"
    "v_accvgpr_write_b32 a55, v46\n"
    "v_exp_f32 v223, v223\n"
    "v_mfma_f32_16x16x32_bf16 a[160:163], a[224:227], v[236:239], a[160:163]\n"
    "v_cvt_pk_bf16_f32 v32, v32, v33\n"
    "v_cvt_pk_bf16_f32 v34, v34, v35\n"
    "v_accvgpr_write_b32 a56, v32\n"
    "v_mfma_f32_16x16x32_bf16 a[164:167], a[228:231], v[236:239], a[164:167]\n"
    "v_accvgpr_write_b32 a57, v34\n"
    "v_cvt_pk_bf16_f32 v234, v220, v221\n"
    "v_cvt_pk_bf16_f32 v235, v222, v223\n"
    "v_mfma_f32_16x16x32_bf16 a[168:171], a[232:235], v[236:239], a[168:171]\n"
    "v_cvt_pk_bf16_f32 v36, v36, v37\n"
    "v_cvt_pk_bf16_f32 v38, v38, v39\n"
    "v_accvgpr_write_b32 a58, v36\n"
    "v_mfma_f32_16x16x32_bf16 a[172:175], a[236:239], v[236:239], a[172:175]\n"
    "s_waitcnt lgkmcnt(0)\n"
    "v_accvgpr_write_b32 a59, v38\n"
    "v_mfma_f32_16x16x32_bf16 v[40:43], v[162:165], a[60:63], 0\n"
    "v_mfma_f32_16x16x32_bf16 v[40:43], v[166:169], a[60:63], v[40:43]\n"
    "v_mfma_f32_16x16x32_bf16 v[44:47], v[160:163], a[60:63], 0\n"
    "v_mfma_f32_16x16x32_bf16 v[44:47], v[164:167], a[60:63], v[44:47]\n"
    "v_mfma_f32_16x16x32_bf16 a[176:179], a[240:243], v[236:239], a[176:179]\n"
    "v_cvt_pk_bf16_f32 v40, v40, v41\n"
    "v_cvt_pk_bf16_f32 v42, v42, v43\n"
    "v_accvgpr_write_b32 a60, v40\n"
    "v_mfma_f32_16x16x32_bf16 a[180:183], a[244:247], v[236:239], a[180:183]\n"
    "v_accvgpr_write_b32 a61, v42\n"
    "v_mfma_f32_16x16x32_bf16 a[184:187], a[248:251], v[236:239], a[184:187]\n"
    "v_cvt_pk_bf16_f32 v44, v44, v45\n"
    "v_cvt_pk_bf16_f32 v46, v46, v47\n"
    "v_accvgpr_write_b32 a62, v44\n"
    "v_mfma_f32_16x16x32_bf16 a[188:191], a[252:255], v[236:239], a[188:191]\n"
    "v_accvgpr_write_b32 a63, v46\n"
    "v_mfma_f32_16x16x32_bf16 v[224:227], a[192:195], a[48:51], 0\n"
    "ds_read_b128 a[192:195], v248 offset:0\n"
    "v_mfma_f32_16x16x32_bf16 v[224:227], a[196:199], a[52:55], v[224:227]\n"
    "ds_read_b128 a[196:199], v249 offset:0\n"
    "v_mfma_f32_16x16x32_bf16 v[224:227], a[200:203], a[56:59], v[224:227]\n"
    "ds_read_b128 a[200:203], v248 offset:1024\n"
    "v_mfma_f32_16x16x32_bf16 v[224:227], a[204:207], a[60:63], v[224:227]\n"
    "ds_read_b128 a[204:207], v249 offset:1024\n"
    "v_mfma_f32_16x16x32_bf16 v[228:231], a[208:211], a[48:51], 0\n"
    "ds_read_b128 a[208:211], v248 offset:4096\n"
    "v_mfma_f32_16x16x32_bf16 v[228:231], a[212:215], a[52:55], v[228:231]\n"
    "ds_read_b128 a[212:215], v249 offset:4096\n"
    "v_mfma_f32_16x16x32_bf16 v[228:231], a[216:219], a[56:59], v[228:231]\n"
    "ds_read_b128 a[216:219], v248 offset:5120\n"
    "v_exp_f32 v224, v224\n"
    "v_mfma_f32_16x16x32_bf16 v[228:231], a[220:223], a[60:63], v[228:231]\n"
    "ds_read_b128 a[220:223], v249 offset:5120\n"
    "v_exp_f32 v225, v225\n"
    "v_mfma_f32_16x16x32_bf16 v[0:3], a[224:227], v[232:235], 0\n"
    "v_exp_f32 v226, v226\n"
    "v_cvt_pk_bf16_f32 v236, v224, v225\n"
    "v_mfma_f32_16x16x32_bf16 v[4:7], a[228:231], v[232:235], 0\n"
    "v_exp_f32 v227, v227\n"
    "v_add_f32 v243, v243, v224\n"
    "v_mfma_f32_16x16x32_bf16 v[8:11], a[232:235], v[232:235], 0\n"
    "v_exp_f32 v228, v228\n"
    "v_cvt_pk_bf16_f32 v237, v226, v227\n"
    "v_mfma_f32_16x16x32_bf16 v[12:15], a[236:239], v[232:235], 0\n"
    "v_exp_f32 v229, v229\n"
    "v_add_f32 v243, v243, v225\n"
    "v_mfma_f32_16x16x32_bf16 v[16:19], a[240:243], v[232:235], 0\n"
    "v_exp_f32 v230, v230\n"
    "v_cvt_pk_bf16_f32 v238, v228, v229\n"
    "v_mfma_f32_16x16x32_bf16 v[20:23], a[244:247], v[232:235], 0\n"
    "v_exp_f32 v231, v231\n"
    "v_add_f32 v243, v243, v226\n"
    "v_mfma_f32_16x16x32_bf16 v[24:27], a[248:251], v[232:235], 0\n"
    "v_cvt_pk_bf16_f32 v239, v230, v231\n"
    "v_add_f32 v243, v243, v227\n"
    "v_add_f32 v243, v243, v228\n"
    "v_mfma_f32_16x16x32_bf16 v[28:31], a[252:255], v[232:235], 0\n"
    "v_add_f32 v243, v243, v229\n"
    "v_add_f32 v243, v243, v230\n"
    "v_add_f32 v243, v243, v231\n"
    "v_mfma_f32_16x16x32_bf16 v[180:183], a[112:115], v[232:235], 0\n"
    "s_waitcnt lgkmcnt(7)\n"
    "v_mfma_f32_16x16x32_bf16 v[216:219], a[192:195], a[32:35], 0\n"
    "s_waitcnt lgkmcnt(6)\n"
    "v_mfma_f32_16x16x32_bf16 v[216:219], a[196:199], a[36:39], v[216:219]\n"
    "s_waitcnt lgkmcnt(5)\n"
    "v_mfma_f32_16x16x32_bf16 v[216:219], a[200:203], a[40:43], v[216:219]\n"
    "s_waitcnt lgkmcnt(4)\n"
    "v_mfma_f32_16x16x32_bf16 v[216:219], a[204:207], a[44:47], v[216:219]\n"
    "s_waitcnt lgkmcnt(3)\n"
    "v_mfma_f32_16x16x32_bf16 v[220:223], a[208:211], a[32:35], 0\n"
    "s_waitcnt lgkmcnt(2)\n"
    "v_mfma_f32_16x16x32_bf16 v[220:223], a[212:215], a[36:39], v[220:223]\n"
    "s_waitcnt lgkmcnt(1)\n"
    "v_mfma_f32_16x16x32_bf16 v[220:223], a[216:219], a[40:43], v[220:223]\n"
    "s_waitcnt lgkmcnt(0)\n"
    "v_exp_f32 v216, v216\n"
    "v_mfma_f32_16x16x32_bf16 v[220:223], a[220:223], a[44:47], v[220:223]\n"
    "v_exp_f32 v217, v217\n"
    "v_mfma_f32_16x16x32_bf16 v[32:35], a[224:227], v[236:239], 0\n"
    "v_exp_f32 v218, v218\n"
    "v_cvt_pk_bf16_f32 v232, v216, v217\n"
    "v_mfma_f32_16x16x32_bf16 v[36:39], a[228:231], v[236:239], 0\n"
    "ds_read_b64_tr_b16 a[224:225], v250 offset:16384\n"
    "v_exp_f32 v219, v219\n"
    "v_mfma_f32_16x16x32_bf16 v[40:43], a[232:235], v[236:239], 0\n"
    "v_exp_f32 v220, v220\n"
    "v_cvt_pk_bf16_f32 v233, v218, v219\n"
    "v_mfma_f32_16x16x32_bf16 v[44:47], a[236:239], v[236:239], 0\n"
    "ds_read_b64_tr_b16 a[226:227], v250 offset:20480\n"
    "v_exp_f32 v221, v221\n"
    "v_mfma_f32_16x16x32_bf16 v[48:51], a[240:243], v[236:239], 0\n"
    "v_exp_f32 v222, v222\n"
    "v_cvt_pk_bf16_f32 v234, v220, v221\n"
    "v_mfma_f32_16x16x32_bf16 v[52:55], a[244:247], v[236:239], 0\n"
    "ds_read_b64_tr_b16 a[228:229], v250 offset:16392\n"
    "v_exp_f32 v223, v223\n"
    "v_mfma_f32_16x16x32_bf16 v[56:59], a[248:251], v[236:239], 0\n"
    "ds_read_b64_tr_b16 a[248:249], v251 offset:1024\n"
    "ds_read_b64_tr_b16 a[250:251], v251 offset:5120\n"
    "v_cvt_pk_bf16_f32 v235, v222, v223\n"
    "v_mfma_f32_16x16x32_bf16 v[60:63], a[252:255], v[236:239], 0\n"
    "s_waitcnt vmcnt(4)\n"
    "s_add_u32 s68, s66, 131072\n"
    "v_add_u32 v253, s68, v248\n"
    "s_add_u32 s68, s66, 131072\n"
    "v_add_u32 v254, s68, v249\n"
    "ds_read_b128 a[64:67], v253 offset:0\n"
    "ds_read_b128 a[68:71], v254 offset:0\n"
    "ds_read_b128 a[72:75], v253 offset:1024\n"
    "ds_read_b128 a[76:79], v254 offset:1024\n"
    "s_add_u32 s69, s67, 98304\n"
    "s_add_u32 m0, s66, 65536\n"
    "s_nop 0\n"
    "buffer_load_dwordx4 v252, s[36:39], s69 offen offset:0 nt lds\n"
    "buffer_load_dwordx4 v252, s[36:39], s69 offen offset:2048 nt lds\n"
    "s_add_u32 m0, s66, 66432\n"
    "s_nop 0\n"
    "buffer_load_dwordx4 v252, s[36:39], s69 offen offset:128 nt lds\n"
    "buffer_load_dwordx4 v252, s[36:39], s69 offen offset:2176 nt lds\n"
    "v_mfma_f32_16x16x32_bf16 v[224:227], a[192:195], a[48:51], 0\n"
    "ds_read_b64_tr_b16 a[252:253], v251 offset:1032\n"
    "ds_read_b64_tr_b16 a[254:255], v251 offset:5128\n"
    "ds_read_b64_tr_b16 a[230:231], v250 offset:20488\n"
    "v_mfma_f32_16x16x32_bf16 v[224:227], a[196:199], a[52:55], v[224:227]\n"
    "ds_read_b64_tr_b16 a[232:233], v251 offset:16384\n"
    "ds_read_b64_tr_b16 a[234:235], v251 offset:20480\n"
    "ds_read_b64_tr_b16 a[236:237], v251 offset:16392\n"
    "v_mfma_f32_16x16x32_bf16 v[224:227], a[200:203], a[56:59], v[224:227]\n"
    "ds_read_b64_tr_b16 a[238:239], v251 offset:20488\n"
    "ds_read_b64_tr_b16 a[240:241], v250 offset:17408\n"
    "ds_read_b64_tr_b16 a[242:243], v250 offset:21504\n"
    "v_mfma_f32_16x16x32_bf16 v[224:227], a[204:207], a[60:63], v[224:227]\n"
    "s_waitcnt lgkmcnt(12)\n"
    "ds_read_b64_tr_b16 a[244:245], v250 offset:17416\n"
    "ds_read_b64_tr_b16 a[246:247], v250 offset:21512\n"
    "v_mfma_f32_16x16x32_bf16 v[64:67], v[162:165], a[64:67], 0\n"
    "v_mfma_f32_16x16x32_bf16 v[64:67], v[166:169], a[64:67], v[64:67]\n"
    "v_mfma_f32_16x16x32_bf16 v[68:71], v[160:163], a[64:67], 0\n"
    "v_exp_f32 v224, v224\n"
    "v_mfma_f32_16x16x32_bf16 v[68:71], v[164:167], a[64:67], v[68:71]\n"
    "v_exp_f32 v225, v225\n"
    "v_add_f32 v243, v243, v224\n"
    "v_mfma_f32_16x16x32_bf16 v[228:231], a[208:211], a[48:51], 0\n"
    "v_cvt_pk_bf16_f32 v64, v64, v65\n"
    "v_cvt_pk_bf16_f32 v66, v66, v67\n"
    "v_accvgpr_write_b32 a64, v64\n"
    "v_mfma_f32_16x16x32_bf16 v[228:231], a[212:215], a[52:55], v[228:231]\n"
    "v_accvgpr_write_b32 a65, v66\n"
    "v_exp_f32 v226, v226\n"
    "v_mfma_f32_16x16x32_bf16 v[228:231], a[216:219], a[56:59], v[228:231]\n"
    "v_cvt_pk_bf16_f32 v68, v68, v69\n"
    "v_cvt_pk_bf16_f32 v70, v70, v71\n"
    "v_accvgpr_write_b32 a66, v68\n"
    "v_mfma_f32_16x16x32_bf16 v[228:231], a[220:223], a[60:63], v[228:231]\n"
    "s_waitcnt lgkmcnt(13)\n"
    "v_accvgpr_write_b32 a67, v70\n"
    "v_exp_f32 v227, v227\n"
    "v_mfma_f32_16x16x32_bf16 v[72:75], v[162:165], a[68:71], 0\n"
    "v_cvt_pk_bf16_f32 v236, v224, v225\n"
    "v_cvt_pk_bf16_f32 v237, v226, v227\n"
    "v_add_f32 v243, v243, v225\n"
    "v_mfma_f32_16x16x32_bf16 v[72:75], v[166:169], a[68:71], v[72:75]\n"
    "v_add_f32 v243, v243, v226\n"
    "v_add_f32 v243, v243, v227\n"
    "v_mfma_f32_16x16x32_bf16 v[76:79], v[160:163], a[68:71], 0\n"
    "v_exp_f32 v228, v228\n"
    "v_mfma_f32_16x16x32_bf16 v[76:79], v[164:167], a[68:71], v[76:79]\n"
    "s_waitcnt lgkmcnt(12)\n"
    "v_exp_f32 v229, v229\n"
    "v_add_f32 v243, v243, v228\n"
    "v_mfma_f32_16x16x32_bf16 v[64:67], v[162:165], a[72:75], 0\n"
    "v_cvt_pk_bf16_f32 v72, v72, v73\n"
    "v_cvt_pk_bf16_f32 v74, v74, v75\n"
    "v_accvgpr_write_b32 a68, v72\n"
    "v_mfma_f32_16x16x32_bf16 v[64:67], v[166:169], a[72:75], v[64:67]\n"
    "v_accvgpr_write_b32 a69, v74\n"
    "v_exp_f32 v230, v230\n"
    "v_mfma_f32_16x16x32_bf16 v[68:71], v[160:163], a[72:75], 0\n"
    "v_cvt_pk_bf16_f32 v76, v76, v77\n"
    "v_cvt_pk_bf16_f32 v78, v78, v79\n"
    "v_accvgpr_write_b32 a70, v76\n"
    "v_mfma_f32_16x16x32_bf16 v[68:71], v[164:167], a[72:75], v[68:71]\n"
    "v_accvgpr_write_b32 a71, v78\n"
    "v_exp_f32 v231, v231\n"
    "v_mfma_f32_16x16x32_bf16 v[0:3], v[192:195], v[232:235], v[0:3]\n"
    "v_cvt_pk_bf16_f32 v64, v64, v65\n"
    "v_cvt_pk_bf16_f32 v66, v66, v67\n"
    "v_accvgpr_write_b32 a72, v64\n"
    "v_mfma_f32_16x16x32_bf16 v[4:7], v[196:199], v[232:235], v[4:7]\n"
    "v_accvgpr_write_b32 a73, v66\n"
    "v_cvt_pk_bf16_f32 v238, v228, v229\n"
    "v_cvt_pk_bf16_f32 v239, v230, v231\n"
    "v_mfma_f32_16x16x32_bf16 v[8:11], v[200:203], v[232:235], v[8:11]\n"
    "v_cvt_pk_bf16_f32 v68, v68, v69\n"
    "v_cvt_pk_bf16_f32 v70, v70, v71\n"
    "v_accvgpr_write_b32 a74, v68\n"
    "v_mfma_f32_16x16x32_bf16 v[12:15], v[204:207], v[232:235], v[12:15]\n"
    "s_waitcnt lgkmcnt(11)\n"
    "v_accvgpr_write_b32 a75, v70\n"
    "v_add_f32 v243, v243, v229\n"
    "v_add_f32 v243, v243, v230\n"
    "v_mfma_f32_16x16x32_bf16 v[72:75], v[162:165], a[76:79], 0\n"
    "v_add_f32 v243, v243, v231\n"
    "v_mfma_f32_16x16x32_bf16 v[72:75], v[166:169], a[76:79], v[72:75]\n"
    "v_mfma_f32_16x16x32_bf16 v[76:79], v[160:163], a[76:79], 0\n"
    "v_mfma_f32_16x16x32_bf16 v[76:79], v[164:167], a[76:79], v[76:79]\n"
    "v_mfma_f32_16x16x32_bf16 v[16:19], v[208:211], v[232:235], v[16:19]\n"
    "v_cvt_pk_bf16_f32 v72, v72, v73\n"
    "v_cvt_pk_bf16_f32 v74, v74, v75\n"
    "v_accvgpr_write_b32 a76, v72\n"
    "v_mfma_f32_16x16x32_bf16 v[20:23], v[212:215], v[232:235], v[20:23]\n"
    "v_accvgpr_write_b32 a77, v74\n"
    "v_mfma_f32_16x16x32_bf16 v[24:27], a[248:251], v[232:235], v[24:27]\n"
    "s_waitcnt lgkmcnt(9)\n"
    "v_cvt_pk_bf16_f32 v76, v76, v77\n"
    "v_cvt_pk_bf16_f32 v78, v78, v79\n"
    "v_accvgpr_write_b32 a78, v76\n"
    "v_mfma_f32_16x16x32_bf16 v[28:31], a[252:255], v[232:235], v[28:31]\n"
    "v_accvgpr_write_b32 a79, v78\n"
    "v_mfma_f32_16x16x32_bf16 v[180:183], a[112:115], v[232:235], v[180:183]\n"
    "s_waitcnt vmcnt(4)\n"
    "s_add_u32 s68, s66, 135168\n"
    "v_add_u32 v253, s68, v248\n"
    "s_add_u32 s68, s66, 135168\n"
    "v_add_u32 v254, s68, v249\n"
    "ds_read_b128 a[80:83], v253 offset:0\n"
    "ds_read_b128 a[84:87], v254 offset:0\n"
    "ds_read_b128 a[88:91], v253 offset:1024\n"
    "ds_read_b128 a[92:95], v254 offset:1024\n"
    "v_mfma_f32_16x16x32_bf16 v[216:219], a[192:195], a[64:67], 0\n"
    "v_mfma_f32_16x16x32_bf16 v[216:219], a[196:199], a[68:71], v[216:219]\n"
    "v_mfma_f32_16x16x32_bf16 v[216:219], a[200:203], a[72:75], v[216:219]\n"
    "v_mfma_f32_16x16x32_bf16 v[216:219], a[204:207], a[76:79], v[216:219]\n"
    "s_waitcnt lgkmcnt(3)\n"
    "v_mfma_f32_16x16x32_bf16 v[96:99], v[162:165], a[80:83], 0\n"
    "v_mfma_f32_16x16x32_bf16 v[96:99], v[166:169], a[80:83], v[96:99]\n"
    "v_mfma_f32_16x16x32_bf16 v[100:103], v[160:163], a[80:83], 0\n"
    "v_exp_f32 v216, v216\n"
    "v_mfma_f32_16x16x32_bf16 v[100:103], v[164:167], a[80:83], v[100:103]\n"
    "v_exp_f32 v217, v217\n"
    "v_mfma_f32_16x16x32_bf16 v[220:223], a[208:211], a[64:67], 0\n"
    "v_cvt_pk_bf16_f32 v96, v96, v97\n"
    "v_cvt_pk_bf16_f32 v98, v98, v99\n"
    "v_accvgpr_write_b32 a80, v96\n"
    "v_mfma_f32_16x16x32_bf16 v[220:223], a[212:215], a[68:71], v[220:223]\n"
    "v_accvgpr_write_b32 a81, v98\n"
    "v_exp_f32 v218, v218\n"
    "v_mfma_f32_16x16x32_bf16 v[220:223], a[216:219], a[72:75], v[220:223]\n"
    "v_cvt_pk_bf16_f32 v100, v100, v101\n"
    "v_cvt_pk_bf16_f32 v102, v102, v103\n"
    "v_accvgpr_write_b32 a82, v100\n"
    "v_mfma_f32_16x16x32_bf16 v[220:223], a[220:223], a[76:79], v[220:223]\n"
    "s_waitcnt lgkmcnt(2)\n"
    "v_accvgpr_write_b32 a83, v102\n"
    "v_exp_f32 v219, v219\n"
    "v_mfma_f32_16x16x32_bf16 v[104:107], v[162:165], a[84:87], 0\n"
    "v_cvt_pk_bf16_f32 v232, v216, v217\n"
    "v_cvt_pk_bf16_f32 v233, v218, v219\n"
    "v_mfma_f32_16x16x32_bf16 v[104:107], v[166:169], a[84:87], v[104:107]\n"
    "v_mfma_f32_16x16x32_bf16 v[108:111], v[160:163], a[84:87], 0\n"
    "v_exp_f32 v220, v220\n"
    "v_mfma_f32_16x16x32_bf16 v[108:111], v[164:167], a[84:87], v[108:111]\n"
    "s_waitcnt lgkmcnt(1)\n"
    "v_exp_f32 v221, v221\n"
    "v_mfma_f32_16x16x32_bf16 v[96:99], v[162:165], a[88:91], 0\n"
    "v_cvt_pk_bf16_f32 v104, v104, v105\n"
    "v_cvt_pk_bf16_f32 v106, v106, v107\n"
    "v_accvgpr_write_b32 a84, v104\n"
    "v_mfma_f32_16x16x32_bf16 v[96:99], v[166:169], a[88:91], v[96:99]\n"
    "v_accvgpr_write_b32 a85, v106\n"
    "v_exp_f32 v222, v222\n"
    "v_mfma_f32_16x16x32_bf16 v[100:103], v[160:163], a[88:91], 0\n"
    "v_cvt_pk_bf16_f32 v108, v108, v109\n"
    "v_cvt_pk_bf16_f32 v110, v110, v111\n"
    "v_accvgpr_write_b32 a86, v108\n"
    "v_mfma_f32_16x16x32_bf16 v[100:103], v[164:167], a[88:91], v[100:103]\n"
    "v_accvgpr_write_b32 a87, v110\n"
    "v_exp_f32 v223, v223\n"
    "v_mfma_f32_16x16x32_bf16 v[32:35], v[192:195], v[236:239], v[32:35]\n"
    "v_cvt_pk_bf16_f32 v96, v96, v97\n"
    "v_cvt_pk_bf16_f32 v98, v98, v99\n"
    "v_accvgpr_write_b32 a88, v96\n"
    "v_mfma_f32_16x16x32_bf16 v[36:39], v[196:199], v[236:239], v[36:39]\n"
    "v_accvgpr_write_b32 a89, v98\n"
    "v_cvt_pk_bf16_f32 v234, v220, v221\n"
    "v_cvt_pk_bf16_f32 v235, v222, v223\n"
    "v_mfma_f32_16x16x32_bf16 v[40:43], v[200:203], v[236:239], v[40:43]\n"
    "v_cvt_pk_bf16_f32 v100, v100, v101\n"
    "v_cvt_pk_bf16_f32 v102, v102, v103\n"
    "v_accvgpr_write_b32 a90, v100\n"
    "v_mfma_f32_16x16x32_bf16 v[44:47], v[204:207], v[236:239], v[44:47]\n"
    "s_waitcnt lgkmcnt(0)\n"
    "v_accvgpr_write_b32 a91, v102\n"
    "v_mfma_f32_16x16x32_bf16 v[104:107], v[162:165], a[92:95], 0\n"
    "v_mfma_f32_16x16x32_bf16 v[104:107], v[166:169], a[92:95], v[104:107]\n"
    "v_mfma_f32_16x16x32_bf16 v[108:111], v[160:163], a[92:95], 0\n"
    "v_mfma_f32_16x16x32_bf16 v[108:111], v[164:167], a[92:95], v[108:111]\n"
    "v_mfma_f32_16x16x32_bf16 v[48:51], v[208:211], v[236:239], v[48:51]\n"
    "v_cvt_pk_bf16_f32 v104, v104, v105\n"
    "v_cvt_pk_bf16_f32 v106, v106, v107\n"
    "v_accvgpr_write_b32 a92, v104\n"
    "v_mfma_f32_16x16x32_bf16 v[52:55], v[212:215], v[236:239], v[52:55]\n"
    "v_accvgpr_write_b32 a93, v106\n"
    "v_mfma_f32_16x16x32_bf16 v[56:59], a[248:251], v[236:239], v[56:59]\n"
    "v_cvt_pk_bf16_f32 v108, v108, v109\n"
    "v_cvt_pk_bf16_f32 v110, v110, v111\n"
    "v_accvgpr_write_b32 a94, v108\n"
    "v_mfma_f32_16x16x32_bf16 v[60:63], a[252:255], v[236:239], v[60:63]\n"
    "v_accvgpr_write_b32 a95, v110\n"
    "v_mfma_f32_16x16x32_bf16 v[224:227], a[192:195], a[80:83], 0\n"
    "ds_read_b128 a[192:195], v248 offset:16384\n"
    "v_mfma_f32_16x16x32_bf16 v[224:227], a[196:199], a[84:87], v[224:227]\n"
    "ds_read_b128 a[196:199], v249 offset:16384\n"
    "v_mfma_f32_16x16x32_bf16 v[224:227], a[200:203], a[88:91], v[224:227]\n"
    "ds_read_b128 a[200:203], v248 offset:17408\n"
    "v_mfma_f32_16x16x32_bf16 v[224:227], a[204:207], a[92:95], v[224:227]\n"
    "ds_read_b128 a[204:207], v249 offset:17408\n"
    "v_mfma_f32_16x16x32_bf16 v[228:231], a[208:211], a[80:83], 0\n"
    "ds_read_b128 a[208:211], v248 offset:20480\n"
    "v_mfma_f32_16x16x32_bf16 v[228:231], a[212:215], a[84:87], v[228:231]\n"
    "ds_read_b128 a[212:215], v249 offset:20480\n"
    "v_mfma_f32_16x16x32_bf16 v[228:231], a[216:219], a[88:91], v[228:231]\n"
    "ds_read_b128 a[216:219], v248 offset:21504\n"
    "v_exp_f32 v224, v224\n"
    "v_mfma_f32_16x16x32_bf16 v[228:231], a[220:223], a[92:95], v[228:231]\n"
    "ds_read_b128 a[220:223], v249 offset:21504\n"
    "v_exp_f32 v225, v225\n"
    "v_mfma_f32_16x16x32_bf16 v[64:67], v[192:195], v[232:235], 0\n"
    "v_exp_f32 v226, v226\n"
    "v_cvt_pk_bf16_f32 v236, v224, v225\n"
    "v_mfma_f32_16x16x32_bf16 v[68:71], v[196:199], v[232:235], 0\n"
    "v_exp_f32 v227, v227\n"
    "v_add_f32 v245, v245, v224\n"
    "v_mfma_f32_16x16x32_bf16 v[72:75], v[200:203], v[232:235], 0\n"
    "v_exp_f32 v228, v228\n"
    "v_cvt_pk_bf16_f32 v237, v226, v227\n"
    "v_mfma_f32_16x16x32_bf16 v[76:79], v[204:207], v[232:235], 0\n"
    "v_exp_f32 v229, v229\n"
    "v_add_f32 v245, v245, v225\n"
    "v_mfma_f32_16x16x32_bf16 v[80:83], v[208:211], v[232:235], 0\n"
    "v_exp_f32 v230, v230\n"
    "v_cvt_pk_bf16_f32 v238, v228, v229\n"
    "v_mfma_f32_16x16x32_bf16 v[84:87], v[212:215], v[232:235], 0\n"
    "v_exp_f32 v231, v231\n"
    "v_add_f32 v245, v245, v226\n"
    "v_mfma_f32_16x16x32_bf16 v[88:91], a[248:251], v[232:235], 0\n"
    "v_cvt_pk_bf16_f32 v239, v230, v231\n"
    "v_add_f32 v245, v245, v227\n"
    "v_add_f32 v245, v245, v228\n"
    "v_mfma_f32_16x16x32_bf16 v[92:95], a[252:255], v[232:235], 0\n"
    "v_add_f32 v245, v245, v229\n"
    "v_add_f32 v245, v245, v230\n"
    "v_add_f32 v245, v245, v231\n"
    "v_mfma_f32_16x16x32_bf16 v[184:187], a[112:115], v[232:235], 0\n"
    "s_waitcnt lgkmcnt(7)\n"
    "v_mfma_f32_16x16x32_bf16 v[216:219], a[192:195], a[64:67], 0\n"
    "s_waitcnt lgkmcnt(6)\n"
    "v_mfma_f32_16x16x32_bf16 v[216:219], a[196:199], a[68:71], v[216:219]\n"
    "s_waitcnt lgkmcnt(5)\n"
    "v_mfma_f32_16x16x32_bf16 v[216:219], a[200:203], a[72:75], v[216:219]\n"
    "s_waitcnt lgkmcnt(4)\n"
    "v_mfma_f32_16x16x32_bf16 v[216:219], a[204:207], a[76:79], v[216:219]\n"
    "s_waitcnt lgkmcnt(3)\n"
    "v_mfma_f32_16x16x32_bf16 v[220:223], a[208:211], a[64:67], 0\n"
    "s_waitcnt lgkmcnt(2)\n"
    "v_mfma_f32_16x16x32_bf16 v[220:223], a[212:215], a[68:71], v[220:223]\n"
    "s_waitcnt lgkmcnt(1)\n"
    "v_mfma_f32_16x16x32_bf16 v[220:223], a[216:219], a[72:75], v[220:223]\n"
    "s_waitcnt lgkmcnt(0)\n"
    "v_exp_f32 v216, v216\n"
    "v_mfma_f32_16x16x32_bf16 v[220:223], a[220:223], a[76:79], v[220:223]\n"
    "v_exp_f32 v217, v217\n"
    "v_mfma_f32_16x16x32_bf16 v[96:99], v[192:195], v[236:239], 0\n"
    "v_exp_f32 v218, v218\n"
    "v_cvt_pk_bf16_f32 v232, v216, v217\n"
    "v_mfma_f32_16x16x32_bf16 v[100:103], v[196:199], v[236:239], 0\n"
    "ds_read_b64_tr_b16 v[192:193], v250 offset:0\n"
    "v_exp_f32 v219, v219\n"
    "v_mfma_f32_16x16x32_bf16 v[104:107], v[200:203], v[236:239], 0\n"
    "v_exp_f32 v220, v220\n"
    "v_cvt_pk_bf16_f32 v233, v218, v219\n"
    "v_mfma_f32_16x16x32_bf16 v[108:111], v[204:207], v[236:239], 0\n"
    "ds_read_b64_tr_b16 v[194:195], v250 offset:4096\n"
    "v_exp_f32 v221, v221\n"
    "v_mfma_f32_16x16x32_bf16 v[112:115], v[208:211], v[236:239], 0\n"
    "v_exp_f32 v222, v222\n"
    "v_cvt_pk_bf16_f32 v234, v220, v221\n"
    "v_mfma_f32_16x16x32_bf16 v[116:119], v[212:215], v[236:239], 0\n"
    "ds_read_b64_tr_b16 v[196:197], v250 offset:8\n"
    "v_exp_f32 v223, v223\n"
    "v_mfma_f32_16x16x32_bf16 v[120:123], a[248:251], v[236:239], 0\n"
    "ds_read_b64_tr_b16 a[248:249], v251 offset:17408\n"
    "ds_read_b64_tr_b16 a[250:251], v251 offset:21504\n"
    "v_cvt_pk_bf16_f32 v235, v222, v223\n"
    "v_mfma_f32_16x16x32_bf16 v[124:127], a[252:255], v[236:239], 0\n"
    "s_waitcnt vmcnt(0)\n"
    "s_add_u32 s68, s66, 65536\n"
    "v_add_u32 v253, s68, v248\n"
    "s_add_u32 s68, s66, 65536\n"
    "v_add_u32 v254, s68, v249\n"
    "ds_read_b128 a[96:99], v253 offset:0\n"
    "ds_read_b128 a[100:103], v254 offset:0\n"
    "ds_read_b128 a[104:107], v253 offset:1024\n"
    "ds_read_b128 a[108:111], v254 offset:1024\n"
    "s_add_u32 m0, s64, 32768\n"
    "s_add_u32 s69, s65, 16384\n"
    "buffer_load_dwordx4 v252, s[40:43], s69 offen offset:0 nt lds\n"
    "buffer_load_dwordx4 v252, s[40:43], s69 offen offset:2048 nt lds\n"
    "s_add_u32 m0, s64, 40960\n"
    "s_add_u32 s69, s65, 16384\n"
    "buffer_load_dwordx4 v252, s[44:47], s69 offen offset:0 nt lds\n"
    "buffer_load_dwordx4 v252, s[44:47], s69 offen offset:2048 nt lds\n"
    "v_mfma_f32_16x16x32_bf16 v[224:227], a[192:195], a[80:83], 0\n"
    "ds_read_b64_tr_b16 a[252:253], v251 offset:17416\n"
    "ds_read_b64_tr_b16 a[254:255], v251 offset:21512\n"
    "ds_read_b64_tr_b16 v[198:199], v250 offset:4104\n"
    "v_mfma_f32_16x16x32_bf16 v[224:227], a[196:199], a[84:87], v[224:227]\n"
    "ds_read_b64_tr_b16 v[200:201], v251 offset:0\n"
    "ds_read_b64_tr_b16 v[202:203], v251 offset:4096\n"
    "ds_read_b64_tr_b16 v[204:205], v251 offset:8\n"
    "v_mfma_f32_16x16x32_bf16 v[224:227], a[200:203], a[88:91], v[224:227]\n"
    "ds_read_b64_tr_b16 v[206:207], v251 offset:4104\n"
    "ds_read_b64_tr_b16 v[208:209], v250 offset:1024\n"
    "ds_read_b64_tr_b16 v[210:211], v250 offset:5120\n"
    "v_mfma_f32_16x16x32_bf16 v[224:227], a[204:207], a[92:95], v[224:227]\n"
    "s_waitcnt lgkmcnt(12)\n"
    "ds_read_b64_tr_b16 v[212:213], v250 offset:1032\n"
    "ds_read_b64_tr_b16 v[214:215], v250 offset:5128\n"
    "v_xor_b32 v250, 32768, v250\n"
    "v_mfma_f32_16x16x32_bf16 v[128:131], v[162:165], a[96:99], 0\n"
    "v_mfma_f32_16x16x32_bf16 v[128:131], v[166:169], a[96:99], v[128:131]\n"
    "v_mfma_f32_16x16x32_bf16 v[132:135], v[160:163], a[96:99], 0\n"
    "v_exp_f32 v224, v224\n"
    "v_mfma_f32_16x16x32_bf16 v[132:135], v[164:167], a[96:99], v[132:135]\n"
    "v_exp_f32 v225, v225\n"
    "v_add_f32 v245, v245, v224\n"
    "v_mfma_f32_16x16x32_bf16 v[228:231], a[208:211], a[80:83], 0\n"
    "v_cvt_pk_bf16_f32 v128, v128, v129\n"
    "v_cvt_pk_bf16_f32 v130, v130, v131\n"
    "v_accvgpr_write_b32 a96, v128\n"
    "v_mfma_f32_16x16x32_bf16 v[228:231], a[212:215], a[84:87], v[228:231]\n"
    "v_accvgpr_write_b32 a97, v130\n"
    "v_exp_f32 v226, v226\n"
    "v_mfma_f32_16x16x32_bf16 v[228:231], a[216:219], a[88:91], v[228:231]\n"
    "v_cvt_pk_bf16_f32 v132, v132, v133\n"
    "v_cvt_pk_bf16_f32 v134, v134, v135\n"
    "v_accvgpr_write_b32 a98, v132\n"
    "v_mfma_f32_16x16x32_bf16 v[228:231], a[220:223], a[92:95], v[228:231]\n"
    "s_waitcnt lgkmcnt(13)\n"
    "v_accvgpr_write_b32 a99, v134\n"
    "v_exp_f32 v227, v227\n"
    "v_mfma_f32_16x16x32_bf16 v[136:139], v[162:165], a[100:103], 0\n"
    "v_cvt_pk_bf16_f32 v236, v224, v225\n"
    "v_cvt_pk_bf16_f32 v237, v226, v227\n"
    "v_add_f32 v245, v245, v225\n"
    "v_mfma_f32_16x16x32_bf16 v[136:139], v[166:169], a[100:103], v[136:139]\n"
    "v_add_f32 v245, v245, v226\n"
    "v_add_f32 v245, v245, v227\n"
    "v_mfma_f32_16x16x32_bf16 v[140:143], v[160:163], a[100:103], 0\n"
    "v_exp_f32 v228, v228\n"
    "v_mfma_f32_16x16x32_bf16 v[140:143], v[164:167], a[100:103], v[140:143]\n"
    "s_waitcnt lgkmcnt(12)\n"
    "v_exp_f32 v229, v229\n"
    "v_add_f32 v245, v245, v228\n"
    "v_mfma_f32_16x16x32_bf16 v[128:131], v[162:165], a[104:107], 0\n"
    "v_cvt_pk_bf16_f32 v136, v136, v137\n"
    "v_cvt_pk_bf16_f32 v138, v138, v139\n"
    "v_accvgpr_write_b32 a100, v136\n"
    "v_mfma_f32_16x16x32_bf16 v[128:131], v[166:169], a[104:107], v[128:131]\n"
    "v_accvgpr_write_b32 a101, v138\n"
    "v_exp_f32 v230, v230\n"
    "v_mfma_f32_16x16x32_bf16 v[132:135], v[160:163], a[104:107], 0\n"
    "v_cvt_pk_bf16_f32 v140, v140, v141\n"
    "v_cvt_pk_bf16_f32 v142, v142, v143\n"
    "v_accvgpr_write_b32 a102, v140\n"
    "v_mfma_f32_16x16x32_bf16 v[132:135], v[164:167], a[104:107], v[132:135]\n"
    "v_accvgpr_write_b32 a103, v142\n"
    "v_exp_f32 v231, v231\n"
    "v_mfma_f32_16x16x32_bf16 v[64:67], a[224:227], v[232:235], v[64:67]\n"
    "v_cvt_pk_bf16_f32 v128, v128, v129\n"
    "v_cvt_pk_bf16_f32 v130, v130, v131\n"
    "v_accvgpr_write_b32 a104, v128\n"
    "v_mfma_f32_16x16x32_bf16 v[68:71], a[228:231], v[232:235], v[68:71]\n"
    "v_accvgpr_write_b32 a105, v130\n"
    "v_cvt_pk_bf16_f32 v238, v228, v229\n"
    "v_cvt_pk_bf16_f32 v239, v230, v231\n"
    "v_mfma_f32_16x16x32_bf16 v[72:75], a[232:235], v[232:235], v[72:75]\n"
    "v_cvt_pk_bf16_f32 v132, v132, v133\n"
    "v_cvt_pk_bf16_f32 v134, v134, v135\n"
    "v_accvgpr_write_b32 a106, v132\n"
    "v_mfma_f32_16x16x32_bf16 v[76:79], a[236:239], v[232:235], v[76:79]\n"
    "s_waitcnt lgkmcnt(11)\n"
    "v_accvgpr_write_b32 a107, v134\n"
    "v_add_f32 v245, v245, v229\n"
    "v_add_f32 v245, v245, v230\n"
    "v_mfma_f32_16x16x32_bf16 v[136:139], v[162:165], a[108:111], 0\n"
    "v_add_f32 v245, v245, v231\n"
    "v_mfma_f32_16x16x32_bf16 v[136:139], v[166:169], a[108:111], v[136:139]\n"
    "v_mfma_f32_16x16x32_bf16 v[140:143], v[160:163], a[108:111], 0\n"
    "v_mfma_f32_16x16x32_bf16 v[140:143], v[164:167], a[108:111], v[140:143]\n"
    "v_mfma_f32_16x16x32_bf16 v[80:83], a[240:243], v[232:235], v[80:83]\n"
    "v_cvt_pk_bf16_f32 v136, v136, v137\n"
    "v_cvt_pk_bf16_f32 v138, v138, v139\n"
    "v_accvgpr_write_b32 a108, v136\n"
    "v_mfma_f32_16x16x32_bf16 v[84:87], a[244:247], v[232:235], v[84:87]\n"
    "v_accvgpr_write_b32 a109, v138\n"
    "v_mfma_f32_16x16x32_bf16 v[88:91], a[248:251], v[232:235], v[88:91]\n"
    "s_waitcnt lgkmcnt(9)\n"
    "v_cvt_pk_bf16_f32 v140, v140, v141\n"
    "v_cvt_pk_bf16_f32 v142, v142, v143\n"
    "v_accvgpr_write_b32 a110, v140\n"
    "v_mfma_f32_16x16x32_bf16 v[92:95], a[252:255], v[232:235], v[92:95]\n"
    "v_accvgpr_write_b32 a111, v142\n"
    "v_mfma_f32_16x16x32_bf16 v[184:187], a[112:115], v[232:235], v[184:187]\n"
    "v_mfma_f32_16x16x32_bf16 v[216:219], a[192:195], a[96:99], 0\n"
    "s_add_u32 m0, s64, 49152\n"
    "s_add_u32 s69, s65, 24576\n"
    "buffer_load_dwordx4 v252, s[40:43], s69 offen offset:0 nt lds\n"
    "buffer_load_dwordx4 v252, s[40:43], s69 offen offset:2048 nt lds\n"
    "s_add_u32 m0, s64, 57344\n"
    "s_add_u32 s69, s65, 24576\n"
    "buffer_load_dwordx4 v252, s[44:47], s69 offen offset:0 nt lds\n"
    "buffer_load_dwordx4 v252, s[44:47], s69 offen offset:2048 nt lds\n"
    "v_mfma_f32_16x16x32_bf16 v[216:219], a[196:199], a[100:103], v[216:219]\n"
    "ds_read_b128 a[192:195], v248 offset:0\n"
    "ds_read_b128 a[196:199], v249 offset:0\n"
    "v_mfma_f32_16x16x32_bf16 v[216:219], a[200:203], a[104:107], v[216:219]\n"
    "ds_read_b128 a[200:203], v248 offset:1024\n"
    "v_mfma_f32_16x16x32_bf16 v[216:219], a[204:207], a[108:111], v[216:219]\n"
    "ds_read_b128 a[204:207], v249 offset:1024\n"
    "v_mfma_f32_16x16x32_bf16 v[220:223], a[208:211], a[96:99], 0\n"
    "ds_read_b128 a[208:211], v248 offset:4096\n"
    "v_mfma_f32_16x16x32_bf16 v[220:223], a[212:215], a[100:103], v[220:223]\n"
    "ds_read_b128 a[212:215], v249 offset:4096\n"
    "v_mfma_f32_16x16x32_bf16 v[220:223], a[216:219], a[104:107], v[220:223]\n"
    "ds_read_b128 a[216:219], v248 offset:5120\n"
    "v_exp_f32 v216, v216\n"
    "v_mfma_f32_16x16x32_bf16 v[220:223], a[220:223], a[108:111], v[220:223]\n"
    "ds_read_b128 a[220:223], v249 offset:5120\n"
    "v_exp_f32 v217, v217\n"
    "v_mfma_f32_16x16x32_bf16 v[96:99], a[224:227], v[236:239], v[96:99]\n"
    "v_exp_f32 v218, v218\n"
    "v_cvt_pk_bf16_f32 v232, v216, v217\n"
    "v_mfma_f32_16x16x32_bf16 v[100:103], a[228:231], v[236:239], v[100:103]\n"
    "v_exp_f32 v219, v219\n"
    "v_xor_b32 v248, 32768, v248\n"
    "v_mfma_f32_16x16x32_bf16 v[104:107], a[232:235], v[236:239], v[104:107]\n"
    "v_exp_f32 v220, v220\n"
    "v_cvt_pk_bf16_f32 v233, v218, v219\n"
    "v_mfma_f32_16x16x32_bf16 v[108:111], a[236:239], v[236:239], v[108:111]\n"
    "v_exp_f32 v221, v221\n"
    "v_xor_b32 v249, 32768, v249\n"
    "v_mfma_f32_16x16x32_bf16 v[112:115], a[240:243], v[236:239], v[112:115]\n"
    "v_exp_f32 v222, v222\n"
    "v_cvt_pk_bf16_f32 v234, v220, v221\n"
    "v_mfma_f32_16x16x32_bf16 v[116:119], a[244:247], v[236:239], v[116:119]\n"
    "v_exp_f32 v223, v223\n"
    "v_mfma_f32_16x16x32_bf16 v[120:123], a[248:251], v[236:239], v[120:123]\n"
    "v_cvt_pk_bf16_f32 v235, v222, v223\n"
    "v_mfma_f32_16x16x32_bf16 v[124:127], a[252:255], v[236:239], v[124:127]\n"
    "s_waitcnt lgkmcnt(7)\n"
    "v_mfma_f32_16x16x32_bf16 v[224:227], a[192:195], a[96:99], 0\n"
    "s_waitcnt lgkmcnt(6)\n"
    "s_waitcnt vmcnt(6)\n"
    "s_barrier\n"
    "ds_read_b128 a[192:195], v248 offset:0\n"
    "v_mfma_f32_16x16x32_bf16 v[224:227], a[196:199], a[100:103], v[224:227]\n"
    "s_waitcnt lgkmcnt(6)\n"
    "ds_read_b128 a[196:199], v249 offset:0\n"
    "v_mfma_f32_16x16x32_bf16 v[224:227], a[200:203], a[104:107], v[224:227]\n"
    "s_waitcnt lgkmcnt(6)\n"
    "ds_read_b128 a[200:203], v248 offset:1024\n"
    "v_mfma_f32_16x16x32_bf16 v[224:227], a[204:207], a[108:111], v[224:227]\n"
    "s_waitcnt lgkmcnt(6)\n"
    "ds_read_b128 a[204:207], v249 offset:1024\n"
    "v_mfma_f32_16x16x32_bf16 v[228:231], a[208:211], a[96:99], 0\n"
    "s_waitcnt lgkmcnt(6)\n"
    "ds_read_b128 a[208:211], v248 offset:4096\n"
    "v_mfma_f32_16x16x32_bf16 v[228:231], a[212:215], a[100:103], v[228:231]\n"
    "s_waitcnt lgkmcnt(6)\n"
    "ds_read_b128 a[212:215], v249 offset:4096\n"
    "v_mfma_f32_16x16x32_bf16 v[228:231], a[216:219], a[104:107], v[228:231]\n"
    "s_waitcnt lgkmcnt(6)\n"
    "ds_read_b128 a[216:219], v248 offset:5120\n"
    "v_exp_f32 v224, v224\n"
    "v_mfma_f32_16x16x32_bf16 v[228:231], a[220:223], a[108:111], v[228:231]\n"
    "ds_read_b128 a[220:223], v249 offset:5120\n"
    "v_exp_f32 v225, v225\n"
    "v_mfma_f32_16x16x32_bf16 v[128:131], a[224:227], v[232:235], 0\n"
    "v_exp_f32 v226, v226\n"
    "v_cvt_pk_bf16_f32 v236, v224, v225\n"
    "v_mfma_f32_16x16x32_bf16 v[132:135], a[228:231], v[232:235], 0\n"
    "v_exp_f32 v227, v227\n"
    "v_mfma_f32_16x16x32_bf16 v[136:139], a[232:235], v[232:235], 0\n"
    "v_exp_f32 v228, v228\n"
    "v_cvt_pk_bf16_f32 v237, v226, v227\n"
    "v_mfma_f32_16x16x32_bf16 v[140:143], a[236:239], v[232:235], 0\n"
    "v_exp_f32 v229, v229\n"
    "v_mfma_f32_16x16x32_bf16 v[144:147], a[240:243], v[232:235], 0\n"
    "v_exp_f32 v230, v230\n"
    "v_cvt_pk_bf16_f32 v238, v228, v229\n"
    "v_mfma_f32_16x16x32_bf16 v[148:151], a[244:247], v[232:235], 0\n"
    "v_exp_f32 v231, v231\n"
    "v_mfma_f32_16x16x32_bf16 v[152:155], a[248:251], v[232:235], 0\n"
    "ds_read_b64_tr_b16 a[248:249], v251 offset:1024\n"
    "ds_read_b64_tr_b16 a[250:251], v251 offset:5120\n"
    "v_cvt_pk_bf16_f32 v239, v230, v231\n"
    "v_mfma_f32_16x16x32_bf16 v[156:159], a[252:255], v[232:235], 0\n"
    "ds_read_b64_tr_b16 a[252:253], v251 offset:1032\n"
    "ds_read_b64_tr_b16 a[254:255], v251 offset:5128\n"
    "v_xor_b32 v251, 32768, v251\n"
    "v_mfma_f32_16x16x32_bf16 v[188:191], a[112:115], v[232:235], 0\n"
    "s_waitcnt lgkmcnt(11)\n"
    "s_mov_b32 s59, 32768\n"
    "v_mfma_f32_16x16x32_bf16 v[216:219], a[192:195], a[0:3], 0\n"
    "s_waitcnt lgkmcnt(10)\n"
    "s_mov_b32 m0, 106496\n"
    "v_mfma_f32_16x16x32_bf16 v[216:219], a[196:199], a[4:7], v[216:219]\n"
    "s_waitcnt lgkmcnt(9)\n"
    "s_mov_b32 s60, 36864\n"
    "buffer_load_dwordx4 v252, s[44:47], s59 offen offset:0 nt lds\n"
    "v_mfma_f32_16x16x32_bf16 v[216:219], a[200:203], a[8:11], v[216:219]\n"
    "s_waitcnt lgkmcnt(8)\n"
    "s_waitcnt vmcnt(5)\n"
    "s_barrier\n"
    "ds_read_b64_tr_b16 a[224:225], v250 offset:0\n"
    "ds_read_b64_tr_b16 a[226:227], v250 offset:4096\n"
    "v_mfma_f32_16x16x32_bf16 v[216:219], a[204:207], a[12:15], v[216:219]\n"
    "s_waitcnt lgkmcnt(9)\n"
    "buffer_load_dwordx4 v252, s[44:47], s59 offen offset:2048 nt lds\n"
    "v_mfma_f32_16x16x32_bf16 v[220:223], a[208:211], a[0:3], 0\n"
    "s_waitcnt lgkmcnt(8)\n"
    "s_mov_b32 m0, 107392\n"
    "ds_read_b64_tr_b16 a[228:229], v250 offset:8\n"
    "ds_read_b64_tr_b16 a[230:231], v250 offset:4104\n"
    "v_mfma_f32_16x16x32_bf16 v[220:223], a[212:215], a[4:7], v[220:223]\n"
    "s_waitcnt lgkmcnt(9)\n"
    "buffer_load_dwordx4 v252, s[44:47], s59 offen offset:128 nt lds\n"
    "v_mfma_f32_16x16x32_bf16 v[220:223], a[216:219], a[8:11], v[220:223]\n"
    "s_waitcnt lgkmcnt(8)\n"
    "v_exp_f32 v216, v216\n"
    "v_mfma_f32_16x16x32_bf16 v[220:223], a[220:223], a[12:15], v[220:223]\n"
    "buffer_load_dwordx4 v252, s[44:47], s59 offen offset:2176 nt lds\n"
    "v_mfma_f32_16x16x32_bf16 v[128:131], v[192:195], v[236:239], v[128:131]\n"
    "s_mov_b32 m0, 110592\n"
    "v_exp_f32 v217, v217\n"
    "v_mfma_f32_16x16x32_bf16 v[132:135], v[196:199], v[236:239], v[132:135]\n"
    "s_mov_b32 s59, 32768\n"
    "buffer_load_dwordx4 v252, s[44:47], s60 offen offset:0 nt lds\n"
    "v_mfma_f32_16x16x32_bf16 v[136:139], v[200:203], v[236:239], v[136:139]\n"
    "v_exp_f32 v218, v218\n"
    "v_mfma_f32_16x16x32_bf16 v[140:143], v[204:207], v[236:239], v[140:143]\n"
    "buffer_load_dwordx4 v252, s[44:47], s60 offen offset:2048 nt lds\n"
    "v_mfma_f32_16x16x32_bf16 v[144:147], v[208:211], v[236:239], v[144:147]\n"
    "s_mov_b32 m0, 111488\n"
    "v_exp_f32 v219, v219\n"
    "v_mfma_f32_16x16x32_bf16 v[148:151], v[212:215], v[236:239], v[148:151]\n"
    "s_waitcnt lgkmcnt(6)\n"
    "buffer_load_dwordx4 v252, s[44:47], s60 offen offset:128 nt lds\n"
    "v_mfma_f32_16x16x32_bf16 v[152:155], a[248:251], v[236:239], v[152:155]\n"
    "s_waitcnt lgkmcnt(4)\n"
    "v_exp_f32 v220, v220\n"
    "v_mfma_f32_16x16x32_bf16 v[156:159], a[252:255], v[236:239], v[156:159]\n"
    "buffer_load_dwordx4 v252, s[44:47], s60 offen offset:2176 nt lds\n"
    "v_mfma_f32_16x16x32_bf16 v[188:191], a[112:115], v[236:239], v[188:191]\n"
    "s_mov_b32 m0, 98304\n"
    "v_exp_f32 v221, v221\n"
    "v_mfma_f32_16x16x32_bf16 v[224:227], a[192:195], a[16:19], 0\n"
    "s_mov_b32 s60, 36864\n"
    "v_exp_f32 v222, v222\n"
    "v_mfma_f32_16x16x32_bf16 v[224:227], a[196:199], a[20:23], v[224:227]\n"
    "v_exp_f32 v223, v223\n"
    "v_mfma_f32_16x16x32_bf16 v[224:227], a[200:203], a[24:27], v[224:227]\n"
    "buffer_load_dwordx4 v252, s[40:43], s59 offen offset:0 nt lds\n"
    "v_mfma_f32_16x16x32_bf16 v[224:227], a[204:207], a[28:31], v[224:227]\n"
    "v_cvt_pk_bf16_f32 v232, v216, v217\n"
    "v_cvt_pk_bf16_f32 v233, v218, v219\n"
    "v_mfma_f32_16x16x32_bf16 v[228:231], a[208:211], a[16:19], 0\n"
    "v_cvt_pk_bf16_f32 v234, v220, v221\n"
    "v_cvt_pk_bf16_f32 v235, v222, v223\n"
    "v_mfma_f32_16x16x32_bf16 v[228:231], a[212:215], a[20:23], v[228:231]\n"
    "ds_read_b64_tr_b16 a[232:233], v251 offset:0\n"
    "ds_read_b64_tr_b16 a[234:235], v251 offset:4096\n"
    "v_mfma_f32_16x16x32_bf16 v[228:231], a[216:219], a[24:27], v[228:231]\n"
    "ds_read_b64_tr_b16 a[236:237], v251 offset:8\n"
    "ds_read_b64_tr_b16 a[238:239], v251 offset:4104\n"
    "v_mfma_f32_16x16x32_bf16 v[228:231], a[220:223], a[28:31], v[228:231]\n"
    "s_waitcnt lgkmcnt(6)\n"
    "buffer_load_dwordx4 v252, s[40:43], s59 offen offset:2048 nt lds\n"
    "v_mfma_f32_16x16x32_bf16 a[128:131], a[224:227], v[232:235], a[128:131]\n"
    "s_waitcnt lgkmcnt(4)\n"
    "s_mov_b32 m0, 99200\n"
    "ds_read_b64_tr_b16 a[240:241], v250 offset:1024\n"
    "ds_read_b64_tr_b16 a[242:243], v250 offset:5120\n"
    "v_mfma_f32_16x16x32_bf16 a[132:135], a[228:231], v[232:235], a[132:135]\n"
    "s_waitcnt lgkmcnt(4)\n"
    "ds_read_b64_tr_b16 a[244:245], v250 offset:1032\n"
    "ds_read_b64_tr_b16 a[246:247], v250 offset:5128\n"
    "v_mfma_f32_16x16x32_bf16 a[136:139], a[232:235], v[232:235], a[136:139]\n"
    "s_waitcnt lgkmcnt(4)\n"
    "buffer_load_dwordx4 v252, s[40:43], s59 offen offset:128 nt lds\n"
    "v_mfma_f32_16x16x32_bf16 a[140:143], a[236:239], v[232:235], a[140:143]\n"
    "s_waitcnt lgkmcnt(2)\n"
    "ds_read_b64_tr_b16 a[248:249], v251 offset:1024\n"
    "ds_read_b64_tr_b16 a[250:251], v251 offset:5120\n"
    "v_mfma_f32_16x16x32_bf16 a[144:147], a[240:243], v[232:235], a[144:147]\n"
    "s_waitcnt lgkmcnt(2)\n"
    "buffer_load_dwordx4 v252, s[40:43], s59 offen offset:2176 nt lds\n"
    "ds_read_b64_tr_b16 a[252:253], v251 offset:1032\n"
    "ds_read_b64_tr_b16 a[254:255], v251 offset:5128\n"
    "v_mfma_f32_16x16x32_bf16 a[148:151], a[244:247], v[232:235], a[148:151]\n"
    "s_waitcnt lgkmcnt(2)\n"
    "s_mov_b32 m0, 102400\n"
    "v_add_f32 v240, v240, v216\n"
    "v_add_f32 v240, v240, v217\n"
    "v_mfma_f32_16x16x32_bf16 a[152:155], a[248:251], v[232:235], a[152:155]\n"
    "s_waitcnt lgkmcnt(0)\n"
    "buffer_load_dwordx4 v252, s[40:43], s60 offen offset:0 nt lds\n"
    "v_mfma_f32_16x16x32_bf16 a[156:159], a[252:255], v[232:235], a[156:159]\n"
    "v_add_f32 v240, v240, v218\n"
    "v_add_f32 v240, v240, v219\n"
    "v_mfma_f32_16x16x32_bf16 v[216:219], a[192:195], a[32:35], 0\n"
    "buffer_load_dwordx4 v252, s[40:43], s60 offen offset:2048 nt lds\n"
    "v_mfma_f32_16x16x32_bf16 v[216:219], a[196:199], a[36:39], v[216:219]\n"
    "s_mov_b32 m0, 103296\n"
    "v_add_f32 v240, v240, v220\n"
    "v_add_f32 v240, v240, v221\n"
    "v_mfma_f32_16x16x32_bf16 v[216:219], a[200:203], a[40:43], v[216:219]\n"
    "buffer_load_dwordx4 v252, s[40:43], s60 offen offset:128 nt lds\n"
    "v_mfma_f32_16x16x32_bf16 v[216:219], a[204:207], a[44:47], v[216:219]\n"
    "v_add_f32 v240, v240, v222\n"
    "v_add_f32 v240, v240, v223\n"
    "v_mfma_f32_16x16x32_bf16 v[220:223], a[208:211], a[32:35], 0\n"
    "buffer_load_dwordx4 v252, s[40:43], s60 offen offset:2176 nt lds\n"
    "v_mfma_f32_16x16x32_bf16 v[220:223], a[212:215], a[36:39], v[220:223]\n"
    "v_exp_f32 v224, v224\n"
    "v_exp_f32 v225, v225\n"
    "v_exp_f32 v226, v226\n"
    "v_exp_f32 v227, v227\n"
    "v_exp_f32 v228, v228\n"
    "v_exp_f32 v229, v229\n"
    "v_exp_f32 v230, v230\n"
    "v_exp_f32 v231, v231\n"
    "v_mfma_f32_16x16x32_bf16 v[220:223], a[216:219], a[40:43], v[220:223]\n"
    "v_cvt_pk_bf16_f32 v236, v224, v225\n"
    "v_cvt_pk_bf16_f32 v237, v226, v227\n"
    "v_cvt_pk_bf16_f32 v238, v228, v229\n"
    "v_cvt_pk_bf16_f32 v239, v230, v231\n"
    "v_mfma_f32_16x16x32_bf16 v[220:223], a[220:223], a[44:47], v[220:223]\n"
    "v_add_f32 v241, v241, v224\n"
    "v_add_f32 v241, v241, v225\n"
    "v_mfma_f32_16x16x32_bf16 a[160:163], a[224:227], v[236:239], a[160:163]\n"
    "v_add_f32 v241, v241, v226\n"
    "v_add_f32 v241, v241, v227\n"
    "v_mfma_f32_16x16x32_bf16 a[164:167], a[228:231], v[236:239], a[164:167]\n"
    "v_add_f32 v241, v241, v228\n"
    "v_add_f32 v241, v241, v229\n"
    "v_mfma_f32_16x16x32_bf16 a[168:171], a[232:235], v[236:239], a[168:171]\n"
    "v_add_f32 v241, v241, v230\n"
    "v_add_f32 v241, v241, v231\n"
    "v_mfma_f32_16x16x32_bf16 a[172:175], a[236:239], v[236:239], a[172:175]\n"
    "v_exp_f32 v216, v216\n"
    "v_mfma_f32_16x16x32_bf16 a[176:179], a[240:243], v[236:239], a[176:179]\n"
    "v_exp_f32 v217, v217\n"
    "v_mfma_f32_16x16x32_bf16 a[180:183], a[244:247], v[236:239], a[180:183]\n"
    "v_exp_f32 v218, v218\n"
    "v_mfma_f32_16x16x32_bf16 a[184:187], a[248:251], v[236:239], a[184:187]\n"
    "v_exp_f32 v219, v219\n"
    "v_mfma_f32_16x16x32_bf16 a[188:191], a[252:255], v[236:239], a[188:191]\n"
    "s_mov_b32 s59, 40960\n"
    "v_exp_f32 v220, v220\n"
    "v_mfma_f32_16x16x32_bf16 v[224:227], a[192:195], a[48:51], 0\n"
    "s_mov_b32 m0, 122880\n"
    "v_exp_f32 v221, v221\n"
    "v_mfma_f32_16x16x32_bf16 v[224:227], a[196:199], a[52:55], v[224:227]\n"
    "s_mov_b32 s60, 45056\n"
    "v_exp_f32 v222, v222\n"
    "v_mfma_f32_16x16x32_bf16 v[224:227], a[200:203], a[56:59], v[224:227]\n"
    "v_exp_f32 v223, v223\n"
    "v_mfma_f32_16x16x32_bf16 v[224:227], a[204:207], a[60:63], v[224:227]\n"
    "v_cvt_pk_bf16_f32 v232, v216, v217\n"
    "v_cvt_pk_bf16_f32 v233, v218, v219\n"
    "v_mfma_f32_16x16x32_bf16 v[228:231], a[208:211], a[48:51], 0\n"
    "v_cvt_pk_bf16_f32 v234, v220, v221\n"
    "v_cvt_pk_bf16_f32 v235, v222, v223\n"
    "v_mfma_f32_16x16x32_bf16 v[228:231], a[212:215], a[52:55], v[228:231]\n"
    "buffer_load_dwordx4 v252, s[44:47], s59 offen offset:0 nt lds\n"
    "v_mfma_f32_16x16x32_bf16 v[228:231], a[216:219], a[56:59], v[228:231]\n"
    "buffer_load_dwordx4 v252, s[44:47], s59 offen offset:2048 nt lds\n"
    "v_mfma_f32_16x16x32_bf16 v[228:231], a[220:223], a[60:63], v[228:231]\n"
    "s_mov_b32 m0, 123776\n"
    "v_exp_f32 v224, v224\n"
    "v_mfma_f32_16x16x32_bf16 v[0:3], a[224:227], v[232:235], v[0:3]\n"
    "buffer_load_dwordx4 v252, s[44:47], s59 offen offset:128 nt lds\n"
    "v_mfma_f32_16x16x32_bf16 v[4:7], a[228:231], v[232:235], v[4:7]\n"
    "buffer_load_dwordx4 v252, s[44:47], s59 offen offset:2176 nt lds\n"
    "v_mfma_f32_16x16x32_bf16 v[8:11], a[232:235], v[232:235], v[8:11]\n"
    "s_mov_b32 m0, 126976\n"
    "v_exp_f32 v225, v225\n"
    "v_mfma_f32_16x16x32_bf16 v[12:15], a[236:239], v[232:235], v[12:15]\n"
    "s_mov_b32 s59, 40960\n"
    "buffer_load_dwordx4 v252, s[44:47], s60 offen offset:0 nt lds\n"
    "v_mfma_f32_16x16x32_bf16 v[16:19], a[240:243], v[232:235], v[16:19]\n"
    "buffer_load_dwordx4 v252, s[44:47], s60 offen offset:2048 nt lds\n"
    "v_mfma_f32_16x16x32_bf16 v[20:23], a[244:247], v[232:235], v[20:23]\n"
    "s_mov_b32 m0, 127872\n"
    "v_exp_f32 v226, v226\n"
    "v_mfma_f32_16x16x32_bf16 v[24:27], a[248:251], v[232:235], v[24:27]\n"
    "buffer_load_dwordx4 v252, s[44:47], s60 offen offset:128 nt lds\n"
    "v_mfma_f32_16x16x32_bf16 v[28:31], a[252:255], v[232:235], v[28:31]\n"
    "buffer_load_dwordx4 v252, s[44:47], s60 offen offset:2176 nt lds\n"
    "v_mfma_f32_16x16x32_bf16 v[180:183], a[112:115], v[232:235], v[180:183]\n"
    "s_mov_b32 m0, 114688\n"
    "v_exp_f32 v227, v227\n"
    "v_mfma_f32_16x16x32_bf16 v[216:219], a[192:195], a[64:67], 0\n"
    "s_mov_b32 s60, 45056\n"
    "v_exp_f32 v228, v228\n"
    "v_mfma_f32_16x16x32_bf16 v[216:219], a[196:199], a[68:71], v[216:219]\n"
    "v_exp_f32 v229, v229\n"
    "v_mfma_f32_16x16x32_bf16 v[216:219], a[200:203], a[72:75], v[216:219]\n"
    "v_exp_f32 v230, v230\n"
    "v_mfma_f32_16x16x32_bf16 v[216:219], a[204:207], a[76:79], v[216:219]\n"
    "v_exp_f32 v231, v231\n"
    "v_mfma_f32_16x16x32_bf16 v[220:223], a[208:211], a[64:67], 0\n"
    "buffer_load_dwordx4 v252, s[40:43], s59 offen offset:0 nt lds\n"
    "v_mfma_f32_16x16x32_bf16 v[220:223], a[212:215], a[68:71], v[220:223]\n"
    "v_cvt_pk_bf16_f32 v236, v224, v225\n"
    "v_cvt_pk_bf16_f32 v237, v226, v227\n"
    "v_mfma_f32_16x16x32_bf16 v[220:223], a[216:219], a[72:75], v[220:223]\n"
    "v_cvt_pk_bf16_f32 v238, v228, v229\n"
    "v_cvt_pk_bf16_f32 v239, v230, v231\n"
    "v_mfma_f32_16x16x32_bf16 v[220:223], a[220:223], a[76:79], v[220:223]\n"
    "buffer_load_dwordx4 v252, s[40:43], s59 offen offset:2048 nt lds\n"
    "v_mfma_f32_16x16x32_bf16 v[32:35], a[224:227], v[236:239], v[32:35]\n"
    "s_mov_b32 m0, 115584\n"
    "v_add_f32 v243, v243, v224\n"
    "v_add_f32 v243, v243, v225\n"
    "v_mfma_f32_16x16x32_bf16 v[36:39], a[228:231], v[236:239], v[36:39]\n"
    "buffer_load_dwordx4 v252, s[40:43], s59 offen offset:128 nt lds\n"
    "v_mfma_f32_16x16x32_bf16 v[40:43], a[232:235], v[236:239], v[40:43]\n"
    "buffer_load_dwordx4 v252, s[40:43], s59 offen offset:2176 nt lds\n"
    "v_mfma_f32_16x16x32_bf16 v[44:47], a[236:239], v[236:239], v[44:47]\n"
    "s_mov_b32 m0, 118784\n"
    "v_add_f32 v243, v243, v226\n"
    "v_add_f32 v243, v243, v227\n"
    "v_mfma_f32_16x16x32_bf16 v[48:51], a[240:243], v[236:239], v[48:51]\n"
    "buffer_load_dwordx4 v252, s[40:43], s60 offen offset:0 nt lds\n"
    "v_mfma_f32_16x16x32_bf16 v[52:55], a[244:247], v[236:239], v[52:55]\n"
    "buffer_load_dwordx4 v252, s[40:43], s60 offen offset:2048 nt lds\n"
    "v_mfma_f32_16x16x32_bf16 v[56:59], a[248:251], v[236:239], v[56:59]\n"
    "s_mov_b32 m0, 119680\n"
    "v_add_f32 v243, v243, v228\n"
    "v_add_f32 v243, v243, v229\n"
    "v_mfma_f32_16x16x32_bf16 v[60:63], a[252:255], v[236:239], v[60:63]\n"
    "buffer_load_dwordx4 v252, s[40:43], s60 offen offset:128 nt lds\n"
    "v_mfma_f32_16x16x32_bf16 v[224:227], a[192:195], a[80:83], 0\n"
    "v_add_f32 v243, v243, v230\n"
    "v_add_f32 v243, v243, v231\n"
    "v_mfma_f32_16x16x32_bf16 v[224:227], a[196:199], a[84:87], v[224:227]\n"
    "buffer_load_dwordx4 v252, s[40:43], s60 offen offset:2176 nt lds\n"
    "v_mfma_f32_16x16x32_bf16 v[224:227], a[200:203], a[88:91], v[224:227]\n"
    "v_exp_f32 v216, v216\n"
    "v_mfma_f32_16x16x32_bf16 v[224:227], a[204:207], a[92:95], v[224:227]\n"
    "v_exp_f32 v217, v217\n"
    "v_mfma_f32_16x16x32_bf16 v[228:231], a[208:211], a[80:83], 0\n"
    "v_exp_f32 v218, v218\n"
    "v_mfma_f32_16x16x32_bf16 v[228:231], a[212:215], a[84:87], v[228:231]\n"
    "v_exp_f32 v219, v219\n"
    "v_exp_f32 v220, v220\n"
    "v_exp_f32 v221, v221\n"
    "v_exp_f32 v222, v222\n"
    "v_exp_f32 v223, v223\n"
    "v_mfma_f32_16x16x32_bf16 v[228:231], a[216:219], a[88:91], v[228:231]\n"
    "v_cvt_pk_bf16_f32 v232, v216, v217\n"
    "v_cvt_pk_bf16_f32 v233, v218, v219\n"
    "v_cvt_pk_bf16_f32 v234, v220, v221\n"
    "v_cvt_pk_bf16_f32 v235, v222, v223\n"
    "v_mfma_f32_16x16x32_bf16 v[228:231], a[220:223], a[92:95], v[228:231]\n"
    "v_exp_f32 v224, v224\n"
    "v_mfma_f32_16x16x32_bf16 v[64:67], a[224:227], v[232:235], v[64:67]\n"
    "s_waitcnt vmcnt(32)\n"
    "s_barrier\n"
    "v_exp_f32 v225, v225\n"
    "v_mfma_f32_16x16x32_bf16 v[68:71], a[228:231], v[232:235], v[68:71]\n"
    "v_exp_f32 v226, v226\n"
    "v_mfma_f32_16x16x32_bf16 v[72:75], a[232:235], v[232:235], v[72:75]\n"
    "v_exp_f32 v227, v227\n"
    "v_mfma_f32_16x16x32_bf16 v[76:79], a[236:239], v[232:235], v[76:79]\n"
    "v_exp_f32 v228, v228\n"
    "v_mfma_f32_16x16x32_bf16 v[80:83], a[240:243], v[232:235], v[80:83]\n"
    "v_exp_f32 v229, v229\n"
    "v_mfma_f32_16x16x32_bf16 v[84:87], a[244:247], v[232:235], v[84:87]\n"
    "v_exp_f32 v230, v230\n"
    "v_mfma_f32_16x16x32_bf16 v[88:91], a[248:251], v[232:235], v[88:91]\n"
    "v_exp_f32 v231, v231\n"
    "v_mfma_f32_16x16x32_bf16 v[92:95], a[252:255], v[232:235], v[92:95]\n"
    "v_cvt_pk_bf16_f32 v236, v224, v225\n"
    "v_cvt_pk_bf16_f32 v237, v226, v227\n"
    "v_mfma_f32_16x16x32_bf16 v[184:187], a[112:115], v[232:235], v[184:187]\n"
    "v_cvt_pk_bf16_f32 v238, v228, v229\n"
    "v_cvt_pk_bf16_f32 v239, v230, v231\n"
    "v_mfma_f32_16x16x32_bf16 v[216:219], a[192:195], a[96:99], 0\n"
    "ds_read_b128 a[192:195], v248 offset:16384\n"
    "v_add_f32 v245, v245, v224\n"
    "v_mfma_f32_16x16x32_bf16 v[216:219], a[196:199], a[100:103], v[216:219]\n"
    "ds_read_b128 a[196:199], v249 offset:16384\n"
    "v_add_f32 v245, v245, v225\n"
    "v_mfma_f32_16x16x32_bf16 v[216:219], a[200:203], a[104:107], v[216:219]\n"
    "ds_read_b128 a[200:203], v248 offset:17408\n"
    "v_add_f32 v245, v245, v226\n"
    "v_mfma_f32_16x16x32_bf16 v[216:219], a[204:207], a[108:111], v[216:219]\n"
    "ds_read_b128 a[204:207], v249 offset:17408\n"
    "v_add_f32 v245, v245, v227\n"
    "v_mfma_f32_16x16x32_bf16 v[220:223], a[208:211], a[96:99], 0\n"
    "ds_read_b128 a[208:211], v248 offset:20480\n"
    "v_add_f32 v245, v245, v228\n"
    "v_mfma_f32_16x16x32_bf16 v[220:223], a[212:215], a[100:103], v[220:223]\n"
    "ds_read_b128 a[212:215], v249 offset:20480\n"
    "v_add_f32 v245, v245, v229\n"
    "v_mfma_f32_16x16x32_bf16 v[220:223], a[216:219], a[104:107], v[220:223]\n"
    "ds_read_b128 a[216:219], v248 offset:21504\n"
    "v_add_f32 v245, v245, v230\n"
    "v_mfma_f32_16x16x32_bf16 v[220:223], a[220:223], a[108:111], v[220:223]\n"
    "ds_read_b128 a[220:223], v249 offset:21504\n"
    "v_add_f32 v245, v245, v231\n"
    "v_mfma_f32_16x16x32_bf16 v[96:99], a[224:227], v[236:239], v[96:99]\n"
    "v_exp_f32 v216, v216\n"
    "v_mfma_f32_16x16x32_bf16 v[100:103], a[228:231], v[236:239], v[100:103]\n"
    "v_exp_f32 v217, v217\n"
    "v_mfma_f32_16x16x32_bf16 v[104:107], a[232:235], v[236:239], v[104:107]\n"
    "v_exp_f32 v218, v218\n"
    "v_mfma_f32_16x16x32_bf16 v[108:111], a[236:239], v[236:239], v[108:111]\n"
    "v_exp_f32 v219, v219\n"
    "v_mfma_f32_16x16x32_bf16 v[112:115], a[240:243], v[236:239], v[112:115]\n"
    "v_exp_f32 v220, v220\n"
    "v_mfma_f32_16x16x32_bf16 v[116:119], a[244:247], v[236:239], v[116:119]\n"
    "v_exp_f32 v221, v221\n"
    "v_mfma_f32_16x16x32_bf16 v[120:123], a[248:251], v[236:239], v[120:123]\n"
    "v_exp_f32 v222, v222\n"
    "v_mfma_f32_16x16x32_bf16 v[124:127], a[252:255], v[236:239], v[124:127]\n"
    "s_waitcnt lgkmcnt(7)\n"
    "s_mov_b32 s59, 49152\n"
    "v_exp_f32 v223, v223\n"
    "v_mfma_f32_16x16x32_bf16 v[224:227], a[192:195], a[0:3], 0\n"
    "s_waitcnt lgkmcnt(6)\n"
    "s_mov_b32 m0, 73728\n"
    "v_cvt_pk_bf16_f32 v232, v216, v217\n"
    "v_cvt_pk_bf16_f32 v233, v218, v219\n"
    "v_mfma_f32_16x16x32_bf16 v[224:227], a[196:199], a[4:7], v[224:227]\n"
    "s_waitcnt lgkmcnt(5)\n"
    "s_mov_b32 s60, 53248\n"
    "v_cvt_pk_bf16_f32 v234, v220, v221\n"
    "v_cvt_pk_bf16_f32 v235, v222, v223\n"
    "v_mfma_f32_16x16x32_bf16 v[224:227], a[200:203], a[8:11], v[224:227]\n"
    "s_waitcnt lgkmcnt(4)\n"
    "buffer_load_dwordx4 v252, s[44:47], s59 offen offset:0 nt lds\n"
    "v_mfma_f32_16x16x32_bf16 v[224:227], a[204:207], a[12:15], v[224:227]\n"
    "s_waitcnt lgkmcnt(3)\n"
    "buffer_load_dwordx4 v252, s[44:47], s59 offen offset:2048 nt lds\n"
    "v_mfma_f32_16x16x32_bf16 v[228:231], a[208:211], a[0:3], 0\n"
    "s_waitcnt lgkmcnt(2)\n"
    "s_mov_b32 m0, 74624\n"
    "ds_read_b64_tr_b16 v[192:193], v250 offset:16384\n"
    "ds_read_b64_tr_b16 v[194:195], v250 offset:20480\n"
    "v_mfma_f32_16x16x32_bf16 v[228:231], a[212:215], a[4:7], v[228:231]\n"
    "s_waitcnt lgkmcnt(3)\n"
    "buffer_load_dwordx4 v252, s[44:47], s59 offen offset:128 nt lds\n"
    "v_mfma_f32_16x16x32_bf16 v[228:231], a[216:219], a[8:11], v[228:231]\n"
    "s_waitcnt lgkmcnt(2)\n"
    "ds_read_b64_tr_b16 v[196:197], v250 offset:16392\n"
    "ds_read_b64_tr_b16 v[198:199], v250 offset:20488\n"
    "v_mfma_f32_16x16x32_bf16 v[228:231], a[220:223], a[12:15], v[228:231]\n"
    "buffer_load_dwordx4 v252, s[44:47], s59 offen offset:2176 nt lds\n"
    "v_mfma_f32_16x16x32_bf16 v[128:131], a[224:227], v[232:235], v[128:131]\n"
    "s_mov_b32 m0, 77824\n"
    "ds_read_b64_tr_b16 v[200:201], v251 offset:16384\n"
    "ds_read_b64_tr_b16 v[202:203], v251 offset:20480\n"
    "v_mfma_f32_16x16x32_bf16 v[132:135], a[228:231], v[232:235], v[132:135]\n"
    "s_mov_b32 s59, 49152\n"
    "buffer_load_dwordx4 v252, s[44:47], s60 offen offset:0 nt lds\n"
    "v_mfma_f32_16x16x32_bf16 v[136:139], a[232:235], v[232:235], v[136:139]\n"
    "ds_read_b64_tr_b16 v[204:205], v251 offset:16392\n"
    "ds_read_b64_tr_b16 v[206:207], v251 offset:20488\n"
    "v_mfma_f32_16x16x32_bf16 v[140:143], a[236:239], v[232:235], v[140:143]\n"
    "buffer_load_dwordx4 v252, s[44:47], s60 offen offset:2048 nt lds\n"
    "v_mfma_f32_16x16x32_bf16 v[144:147], a[240:243], v[232:235], v[144:147]\n"
    "s_mov_b32 m0, 78720\n"
    "ds_read_b64_tr_b16 v[208:209], v250 offset:17408\n"
    "ds_read_b64_tr_b16 v[210:211], v250 offset:21504\n"
    "v_mfma_f32_16x16x32_bf16 v[148:151], a[244:247], v[232:235], v[148:151]\n"
    "buffer_load_dwordx4 v252, s[44:47], s60 offen offset:128 nt lds\n"
    "v_mfma_f32_16x16x32_bf16 v[152:155], a[248:251], v[232:235], v[152:155]\n"
    "v_exp_f32 v224, v224\n"
    "v_mfma_f32_16x16x32_bf16 v[156:159], a[252:255], v[232:235], v[156:159]\n"
    "buffer_load_dwordx4 v252, s[44:47], s60 offen offset:2176 nt lds\n"
    "v_mfma_f32_16x16x32_bf16 v[188:191], a[112:115], v[232:235], v[188:191]\n"
    "s_mov_b32 m0, 65536\n"
    "v_exp_f32 v225, v225\n"
    "v_mfma_f32_16x16x32_bf16 v[216:219], a[192:195], a[16:19], 0\n"
    "s_mov_b32 s60, 53248\n"
    "v_exp_f32 v226, v226\n"
    "v_mfma_f32_16x16x32_bf16 v[216:219], a[196:199], a[20:23], v[216:219]\n"
    "v_exp_f32 v227, v227\n"
    "v_mfma_f32_16x16x32_bf16 v[216:219], a[200:203], a[24:27], v[216:219]\n"
    "v_exp_f32 v228, v228\n"
    "v_mfma_f32_16x16x32_bf16 v[216:219], a[204:207], a[28:31], v[216:219]\n"
    "v_exp_f32 v229, v229\n"
    "v_mfma_f32_16x16x32_bf16 v[220:223], a[208:211], a[16:19], 0\n"
    "v_exp_f32 v230, v230\n"
    "v_mfma_f32_16x16x32_bf16 v[220:223], a[212:215], a[20:23], v[220:223]\n"
    "v_exp_f32 v231, v231\n"
    "v_mfma_f32_16x16x32_bf16 v[220:223], a[216:219], a[24:27], v[220:223]\n"
    "ds_read_b64_tr_b16 v[212:213], v250 offset:17416\n"
    "ds_read_b64_tr_b16 v[214:215], v250 offset:21512\n"
    "buffer_load_dwordx4 v252, s[40:43], s59 offen offset:0 nt lds\n"
    "v_cvt_pk_bf16_f32 v236, v224, v225\n"
    "v_cvt_pk_bf16_f32 v237, v226, v227\n"
    "v_cvt_pk_bf16_f32 v238, v228, v229\n"
    "v_cvt_pk_bf16_f32 v239, v230, v231\n"
    "v_mfma_f32_16x16x32_bf16 v[220:223], a[220:223], a[28:31], v[220:223]\n"
    "s_waitcnt lgkmcnt(10)\n"
    "ds_read_b64_tr_b16 a[248:249], v251 offset:17408\n"
    "ds_read_b64_tr_b16 a[250:251], v251 offset:21504\n"
    "v_mfma_f32_16x16x32_bf16 a[128:131], v[192:195], v[236:239], a[128:131]\n"
    "s_waitcnt lgkmcnt(10)\n"
    "buffer_load_dwordx4 v252, s[40:43], s59 offen offset:2048 nt lds\n"
    "ds_read_b64_tr_b16 a[252:253], v251 offset:17416\n"
    "ds_read_b64_tr_b16 a[254:255], v251 offset:21512\n"
    "v_mfma_f32_16x16x32_bf16 a[132:135], v[196:199], v[236:239], a[132:135]\n"
    "s_waitcnt lgkmcnt(10)\n"
    "s_mov_b32 m0, 66432\n"
    "v_add_f32 v240, v240, v224\n"
    "v_add_f32 v240, v240, v225\n"
    "v_mfma_f32_16x16x32_bf16 a[136:139], v[200:203], v[236:239], a[136:139]\n"
    "s_waitcnt lgkmcnt(8)\n"
    "buffer_load_dwordx4 v252, s[40:43], s59 offen offset:128 nt lds\n"
    "v_mfma_f32_16x16x32_bf16 a[140:143], v[204:207], v[236:239], a[140:143]\n"
    "s_waitcnt lgkmcnt(6)\n"
    "buffer_load_dwordx4 v252, s[40:43], s59 offen offset:2176 nt lds\n"
    "v_mfma_f32_16x16x32_bf16 a[144:147], v[208:211], v[236:239], a[144:147]\n"
    "s_waitcnt lgkmcnt(4)\n"
    "s_mov_b32 m0, 69632\n"
    "v_add_f32 v240, v240, v226\n"
    "v_add_f32 v240, v240, v227\n"
    "v_mfma_f32_16x16x32_bf16 a[148:151], v[212:215], v[236:239], a[148:151]\n"
    "s_waitcnt lgkmcnt(2)\n"
    "buffer_load_dwordx4 v252, s[40:43], s60 offen offset:0 nt lds\n"
    "v_mfma_f32_16x16x32_bf16 a[152:155], a[248:251], v[236:239], a[152:155]\n"
    "s_waitcnt lgkmcnt(0)\n"
    "buffer_load_dwordx4 v252, s[40:43], s60 offen offset:2048 nt lds\n"
    "v_mfma_f32_16x16x32_bf16 a[156:159], a[252:255], v[236:239], a[156:159]\n"
    "s_mov_b32 m0, 70528\n"
    "v_add_f32 v240, v240, v228\n"
    "v_add_f32 v240, v240, v229\n"
    "v_mfma_f32_16x16x32_bf16 v[224:227], a[192:195], a[32:35], 0\n"
    "buffer_load_dwordx4 v252, s[40:43], s60 offen offset:128 nt lds\n"
    "v_mfma_f32_16x16x32_bf16 v[224:227], a[196:199], a[36:39], v[224:227]\n"
    "v_add_f32 v240, v240, v230\n"
    "v_add_f32 v240, v240, v231\n"
    "v_mfma_f32_16x16x32_bf16 v[224:227], a[200:203], a[40:43], v[224:227]\n"
    "buffer_load_dwordx4 v252, s[40:43], s60 offen offset:2176 nt lds\n"
    "v_mfma_f32_16x16x32_bf16 v[224:227], a[204:207], a[44:47], v[224:227]\n"
    "v_exp_f32 v216, v216\n"
    "v_mfma_f32_16x16x32_bf16 v[228:231], a[208:211], a[32:35], 0\n"
    "v_exp_f32 v217, v217\n"
    "v_mfma_f32_16x16x32_bf16 v[228:231], a[212:215], a[36:39], v[228:231]\n"
    "v_exp_f32 v218, v218\n"
    "v_exp_f32 v219, v219\n"
    "v_exp_f32 v220, v220\n"
    "v_exp_f32 v221, v221\n"
    "v_exp_f32 v222, v222\n"
    "v_exp_f32 v223, v223\n"
    "v_mfma_f32_16x16x32_bf16 v[228:231], a[216:219], a[40:43], v[228:231]\n"
    "v_cvt_pk_bf16_f32 v232, v216, v217\n"
    "v_cvt_pk_bf16_f32 v233, v218, v219\n"
    "v_cvt_pk_bf16_f32 v234, v220, v221\n"
    "v_cvt_pk_bf16_f32 v235, v222, v223\n"
    "v_mfma_f32_16x16x32_bf16 v[228:231], a[220:223], a[44:47], v[228:231]\n"
    "v_add_f32 v241, v241, v216\n"
    "v_add_f32 v241, v241, v217\n"
    "v_mfma_f32_16x16x32_bf16 a[160:163], v[192:195], v[232:235], a[160:163]\n"
    "v_add_f32 v241, v241, v218\n"
    "v_add_f32 v241, v241, v219\n"
    "v_mfma_f32_16x16x32_bf16 a[164:167], v[196:199], v[232:235], a[164:167]\n"
    "v_add_f32 v241, v241, v220\n"
    "v_add_f32 v241, v241, v221\n"
    "v_mfma_f32_16x16x32_bf16 a[168:171], v[200:203], v[232:235], a[168:171]\n"
    "v_add_f32 v241, v241, v222\n"
    "v_add_f32 v241, v241, v223\n"
    "v_mfma_f32_16x16x32_bf16 a[172:175], v[204:207], v[232:235], a[172:175]\n"
    "v_exp_f32 v224, v224\n"
    "v_mfma_f32_16x16x32_bf16 a[176:179], v[208:211], v[232:235], a[176:179]\n"
    "v_exp_f32 v225, v225\n"
    "v_mfma_f32_16x16x32_bf16 a[180:183], v[212:215], v[232:235], a[180:183]\n"
    "v_exp_f32 v226, v226\n"
    "v_mfma_f32_16x16x32_bf16 a[184:187], a[248:251], v[232:235], a[184:187]\n"
    "v_exp_f32 v227, v227\n"
    "v_mfma_f32_16x16x32_bf16 a[188:191], a[252:255], v[232:235], a[188:191]\n"
    "s_mov_b32 s59, 57344\n"
    "v_exp_f32 v228, v228\n"
    "v_mfma_f32_16x16x32_bf16 v[216:219], a[192:195], a[48:51], 0\n"
    "s_mov_b32 m0, 90112\n"
    "v_exp_f32 v229, v229\n"
    "v_mfma_f32_16x16x32_bf16 v[216:219], a[196:199], a[52:55], v[216:219]\n"
    "s_mov_b32 s60, 61440\n"
    "v_exp_f32 v230, v230\n"
    "v_mfma_f32_16x16x32_bf16 v[216:219], a[200:203], a[56:59], v[216:219]\n"
    "v_exp_f32 v231, v231\n"
    "v_mfma_f32_16x16x32_bf16 v[216:219], a[204:207], a[60:63], v[216:219]\n"
    "v_cvt_pk_bf16_f32 v236, v224, v225\n"
    "v_cvt_pk_bf16_f32 v237, v226, v227\n"
    "v_mfma_f32_16x16x32_bf16 v[220:223], a[208:211], a[48:51], 0\n"
    "v_cvt_pk_bf16_f32 v238, v228, v229\n"
    "v_cvt_pk_bf16_f32 v239, v230, v231\n"
    "v_mfma_f32_16x16x32_bf16 v[220:223], a[212:215], a[52:55], v[220:223]\n"
    "buffer_load_dwordx4 v252, s[44:47], s59 offen offset:0 nt lds\n"
    "v_mfma_f32_16x16x32_bf16 v[220:223], a[216:219], a[56:59], v[220:223]\n"
    "buffer_load_dwordx4 v252, s[44:47], s59 offen offset:2048 nt lds\n"
    "v_mfma_f32_16x16x32_bf16 v[220:223], a[220:223], a[60:63], v[220:223]\n"
    "s_mov_b32 m0, 91008\n"
    "v_exp_f32 v216, v216\n"
    "v_mfma_f32_16x16x32_bf16 v[0:3], v[192:195], v[236:239], v[0:3]\n"
    "s_waitcnt vmcnt(34)\n"
    "s_barrier\n"
    "buffer_load_dwordx4 v252, s[44:47], s59 offen offset:128 nt lds\n"
    "v_mfma_f32_16x16x32_bf16 v[4:7], v[196:199], v[236:239], v[4:7]\n"
    "buffer_load_dwordx4 v252, s[44:47], s59 offen offset:2176 nt lds\n"
    "v_mfma_f32_16x16x32_bf16 v[8:11], v[200:203], v[236:239], v[8:11]\n"
    "s_mov_b32 m0, 94208\n"
    "v_exp_f32 v217, v217\n"
    "v_mfma_f32_16x16x32_bf16 v[12:15], v[204:207], v[236:239], v[12:15]\n"
    "s_mov_b32 s59, 57344\n"
    "buffer_load_dwordx4 v252, s[44:47], s60 offen offset:0 nt lds\n"
    "v_mfma_f32_16x16x32_bf16 v[16:19], v[208:211], v[236:239], v[16:19]\n"
    "buffer_load_dwordx4 v252, s[44:47], s60 offen offset:2048 nt lds\n"
    "v_mfma_f32_16x16x32_bf16 v[20:23], v[212:215], v[236:239], v[20:23]\n"
    "s_mov_b32 m0, 95104\n"
    "v_exp_f32 v218, v218\n"
    "v_mfma_f32_16x16x32_bf16 v[24:27], a[248:251], v[236:239], v[24:27]\n"
    "buffer_load_dwordx4 v252, s[44:47], s60 offen offset:128 nt lds\n"
    "v_mfma_f32_16x16x32_bf16 v[28:31], a[252:255], v[236:239], v[28:31]\n"
    "buffer_load_dwordx4 v252, s[44:47], s60 offen offset:2176 nt lds\n"
    "v_mfma_f32_16x16x32_bf16 v[180:183], a[112:115], v[236:239], v[180:183]\n"
    "s_mov_b32 m0, 81920\n"
    "v_exp_f32 v219, v219\n"
    "v_mfma_f32_16x16x32_bf16 v[224:227], a[192:195], a[64:67], 0\n"
    "s_mov_b32 s60, 61440\n"
    "v_exp_f32 v220, v220\n"
    "v_mfma_f32_16x16x32_bf16 v[224:227], a[196:199], a[68:71], v[224:227]\n"
    "v_exp_f32 v221, v221\n"
    "v_mfma_f32_16x16x32_bf16 v[224:227], a[200:203], a[72:75], v[224:227]\n"
    "v_exp_f32 v222, v222\n"
    "v_mfma_f32_16x16x32_bf16 v[224:227], a[204:207], a[76:79], v[224:227]\n"
    "v_exp_f32 v223, v223\n"
    "v_mfma_f32_16x16x32_bf16 v[228:231], a[208:211], a[64:67], 0\n"
    "buffer_load_dwordx4 v252, s[40:43], s59 offen offset:0 nt lds\n"
    "v_mfma_f32_16x16x32_bf16 v[228:231], a[212:215], a[68:71], v[228:231]\n"
    "v_cvt_pk_bf16_f32 v232, v216, v217\n"
    "v_cvt_pk_bf16_f32 v233, v218, v219\n"
    "v_mfma_f32_16x16x32_bf16 v[228:231], a[216:219], a[72:75], v[228:231]\n"
    "v_cvt_pk_bf16_f32 v234, v220, v221\n"
    "v_cvt_pk_bf16_f32 v235, v222, v223\n"
    "v_mfma_f32_16x16x32_bf16 v[228:231], a[220:223], a[76:79], v[228:231]\n"
    "buffer_load_dwordx4 v252, s[40:43], s59 offen offset:2048 nt lds\n"
    "v_mfma_f32_16x16x32_bf16 v[32:35], v[192:195], v[232:235], v[32:35]\n"
    "s_mov_b32 m0, 82816\n"
    "v_add_f32 v243, v243, v216\n"
    "v_add_f32 v243, v243, v217\n"
    "v_mfma_f32_16x16x32_bf16 v[36:39], v[196:199], v[232:235], v[36:39]\n"
    "buffer_load_dwordx4 v252, s[40:43], s59 offen offset:128 nt lds\n"
    "v_mfma_f32_16x16x32_bf16 v[40:43], v[200:203], v[232:235], v[40:43]\n"
    "buffer_load_dwordx4 v252, s[40:43], s59 offen offset:2176 nt lds\n"
    "v_mfma_f32_16x16x32_bf16 v[44:47], v[204:207], v[232:235], v[44:47]\n"
    "s_mov_b32 m0, 86016\n"
    "v_add_f32 v243, v243, v218\n"
    "v_add_f32 v243, v243, v219\n"
    "v_mfma_f32_16x16x32_bf16 v[48:51], v[208:211], v[232:235], v[48:51]\n"
    "buffer_load_dwordx4 v252, s[40:43], s60 offen offset:0 nt lds\n"
    "v_mfma_f32_16x16x32_bf16 v[52:55], v[212:215], v[232:235], v[52:55]\n"
    "buffer_load_dwordx4 v252, s[40:43], s60 offen offset:2048 nt lds\n"
    "v_mfma_f32_16x16x32_bf16 v[56:59], a[248:251], v[232:235], v[56:59]\n"
    "s_mov_b32 m0, 86912\n"
    "v_add_f32 v243, v243, v220\n"
    "v_add_f32 v243, v243, v221\n"
    "v_mfma_f32_16x16x32_bf16 v[60:63], a[252:255], v[232:235], v[60:63]\n"
    "buffer_load_dwordx4 v252, s[40:43], s60 offen offset:128 nt lds\n"
    "v_mfma_f32_16x16x32_bf16 v[216:219], a[192:195], a[80:83], 0\n"
    "v_add_f32 v243, v243, v222\n"
    "v_add_f32 v243, v243, v223\n"
    "v_mfma_f32_16x16x32_bf16 v[216:219], a[196:199], a[84:87], v[216:219]\n"
    "buffer_load_dwordx4 v252, s[40:43], s60 offen offset:2176 nt lds\n"
    "v_mfma_f32_16x16x32_bf16 v[216:219], a[200:203], a[88:91], v[216:219]\n"
    "v_exp_f32 v224, v224\n"
    "v_mfma_f32_16x16x32_bf16 v[216:219], a[204:207], a[92:95], v[216:219]\n"
    "v_exp_f32 v225, v225\n"
    "v_mfma_f32_16x16x32_bf16 v[220:223], a[208:211], a[80:83], 0\n"
    "v_exp_f32 v226, v226\n"
    "v_mfma_f32_16x16x32_bf16 v[220:223], a[212:215], a[84:87], v[220:223]\n"
    "v_exp_f32 v227, v227\n"
    "v_exp_f32 v228, v228\n"
    "v_exp_f32 v229, v229\n"
    "v_exp_f32 v230, v230\n"
    "v_exp_f32 v231, v231\n"
    "v_mfma_f32_16x16x32_bf16 v[220:223], a[216:219], a[88:91], v[220:223]\n"
    "v_cvt_pk_bf16_f32 v236, v224, v225\n"
    "v_cvt_pk_bf16_f32 v237, v226, v227\n"
    "v_cvt_pk_bf16_f32 v238, v228, v229\n"
    "v_cvt_pk_bf16_f32 v239, v230, v231\n"
    "v_mfma_f32_16x16x32_bf16 v[220:223], a[220:223], a[92:95], v[220:223]\n"
    "v_exp_f32 v216, v216\n"
    "v_mfma_f32_16x16x32_bf16 v[64:67], v[192:195], v[236:239], v[64:67]\n"
    "v_exp_f32 v217, v217\n"
    "v_mfma_f32_16x16x32_bf16 v[68:71], v[196:199], v[236:239], v[68:71]\n"
    "v_exp_f32 v218, v218\n"
    "v_mfma_f32_16x16x32_bf16 v[72:75], v[200:203], v[236:239], v[72:75]\n"
    "v_exp_f32 v219, v219\n"
    "v_mfma_f32_16x16x32_bf16 v[76:79], v[204:207], v[236:239], v[76:79]\n"
    "v_exp_f32 v220, v220\n"
    "v_mfma_f32_16x16x32_bf16 v[80:83], v[208:211], v[236:239], v[80:83]\n"
    "v_exp_f32 v221, v221\n"
    "v_mfma_f32_16x16x32_bf16 v[84:87], v[212:215], v[236:239], v[84:87]\n"
    "v_exp_f32 v222, v222\n"
    "v_mfma_f32_16x16x32_bf16 v[88:91], a[248:251], v[236:239], v[88:91]\n"
    "v_exp_f32 v223, v223\n"
    "v_mfma_f32_16x16x32_bf16 v[92:95], a[252:255], v[236:239], v[92:95]\n"
    "v_cvt_pk_bf16_f32 v232, v216, v217\n"
    "v_cvt_pk_bf16_f32 v233, v218, v219\n"
    "v_mfma_f32_16x16x32_bf16 v[184:187], a[112:115], v[236:239], v[184:187]\n"
    "v_cvt_pk_bf16_f32 v234, v220, v221\n"
    "v_cvt_pk_bf16_f32 v235, v222, v223\n"
    "v_mfma_f32_16x16x32_bf16 v[224:227], a[192:195], a[96:99], 0\n"
    "v_xor_b32 v248, 65536, v248\n"
    "v_xor_b32 v249, 65536, v249\n"
    "v_mfma_f32_16x16x32_bf16 v[224:227], a[196:199], a[100:103], v[224:227]\n"
    "ds_read_b128 a[192:195], v248 offset:0\n"
    "ds_read_b128 a[196:199], v249 offset:0\n"
    "v_mfma_f32_16x16x32_bf16 v[224:227], a[200:203], a[104:107], v[224:227]\n"
    "ds_read_b128 a[200:203], v248 offset:1024\n"
    "v_xor_b32 v250, 65536, v250\n"
    "v_mfma_f32_16x16x32_bf16 v[224:227], a[204:207], a[108:111], v[224:227]\n"
    "ds_read_b128 a[204:207], v249 offset:1024\n"
    "v_xor_b32 v251, 65536, v251\n"
    "v_mfma_f32_16x16x32_bf16 v[228:231], a[208:211], a[96:99], 0\n"
    "ds_read_b128 a[208:211], v248 offset:4096\n"
    "v_add_f32 v245, v245, v216\n"
    "v_mfma_f32_16x16x32_bf16 v[228:231], a[212:215], a[100:103], v[228:231]\n"
    "ds_read_b128 a[212:215], v249 offset:4096\n"
    "v_add_f32 v245, v245, v217\n"
    "v_mfma_f32_16x16x32_bf16 v[228:231], a[216:219], a[104:107], v[228:231]\n"
    "v_add_f32 v245, v245, v218\n"
    "v_add_f32 v245, v245, v219\n"
    "v_mfma_f32_16x16x32_bf16 v[228:231], a[220:223], a[108:111], v[228:231]\n"
    "v_add_f32 v245, v245, v220\n"
    "v_add_f32 v245, v245, v221\n"
    "v_mfma_f32_16x16x32_bf16 v[96:99], v[192:195], v[232:235], v[96:99]\n"
    "v_add_f32 v245, v245, v222\n"
    "v_add_f32 v245, v245, v223\n"
    "v_mfma_f32_16x16x32_bf16 v[100:103], v[196:199], v[232:235], v[100:103]\n"
    "ds_read_b128 a[216:219], v248 offset:5120\n"
    "ds_read_b128 a[220:223], v249 offset:5120\n"
    "v_mfma_f32_16x16x32_bf16 v[104:107], v[200:203], v[232:235], v[104:107]\n"
    "v_exp_f32 v224, v224\n"
    "v_mfma_f32_16x16x32_bf16 v[108:111], v[204:207], v[232:235], v[108:111]\n"
    "v_exp_f32 v225, v225\n"
    "v_mfma_f32_16x16x32_bf16 v[112:115], v[208:211], v[232:235], v[112:115]\n"
    "v_exp_f32 v226, v226\n"
    "v_mfma_f32_16x16x32_bf16 v[116:119], v[212:215], v[232:235], v[116:119]\n"
    "v_exp_f32 v227, v227\n"
    "v_mfma_f32_16x16x32_bf16 v[120:123], a[248:251], v[232:235], v[120:123]\n"
    "v_exp_f32 v228, v228\n"
    "v_mfma_f32_16x16x32_bf16 v[124:127], a[252:255], v[232:235], v[124:127]\n"
    "s_waitcnt lgkmcnt(7)\n"
    "s_mov_b32 s54, 12\n"
    "s_branch LBL_%=\n"
    "LAL_%=:\n"
    ".p2align 4\n"
    "LA_%=:\n"
    "v_mfma_f32_16x16x32_bf16 v[216:219], a[192:195], a[0:3], 0\n"
    "s_waitcnt lgkmcnt(6)\n"
    "v_exp_f32 v231, v231\n"
    "v_mfma_f32_16x16x32_bf16 v[216:219], a[196:199], a[4:7], v[216:219]\n"
    "s_waitcnt lgkmcnt(5)\n"
    "v_cvt_pk_bf16_f32 v236, v224, v225\n"
    "v_cvt_pk_bf16_f32 v237, v226, v227\n"
    "v_mfma_f32_16x16x32_bf16 v[216:219], a[200:203], a[8:11], v[216:219]\n"
    "s_waitcnt lgkmcnt(4)\n"
    "v_cvt_pk_bf16_f32 v238, v228, v229\n"
    "v_cvt_pk_bf16_f32 v239, v230, v231\n"
    "v_mfma_f32_16x16x32_bf16 v[216:219], a[204:207], a[12:15], v[216:219]\n"
    "s_waitcnt lgkmcnt(3)\n"
    "v_add_f32 v247, v247, v224\n"
    "v_add_f32 v247, v247, v225\n"
    "v_mfma_f32_16x16x32_bf16 v[220:223], a[208:211], a[0:3], 0\n"
    "s_waitcnt lgkmcnt(2)\n"
    "v_add_f32 v247, v247, v226\n"
    "v_add_f32 v247, v247, v227\n"
    "v_mfma_f32_16x16x32_bf16 v[220:223], a[212:215], a[4:7], v[220:223]\n"
    "s_waitcnt lgkmcnt(1)\n"
    "v_add_f32 v247, v247, v228\n"
    "v_add_f32 v247, v247, v229\n"
    "v_mfma_f32_16x16x32_bf16 v[220:223], a[216:219], a[8:11], v[220:223]\n"
    "s_waitcnt lgkmcnt(0)\n"
    "v_add_f32 v247, v247, v230\n"
    "v_add_f32 v247, v247, v231\n"
    "v_mfma_f32_16x16x32_bf16 v[220:223], a[220:223], a[12:15], v[220:223]\n"
    "v_exp_f32 v216, v216\n"
    "v_mfma_f32_16x16x32_bf16 v[160:163], v[192:195], v[236:239], v[160:163]\n"
    "v_exp_f32 v217, v217\n"
    "v_mfma_f32_16x16x32_bf16 v[164:167], v[196:199], v[236:239], v[164:167]\n"
    "v_exp_f32 v218, v218\n"
    "v_mfma_f32_16x16x32_bf16 v[168:171], v[200:203], v[236:239], v[168:171]\n"
    "v_exp_f32 v219, v219\n"
    "v_mfma_f32_16x16x32_bf16 v[172:175], v[204:207], v[236:239], v[172:175]\n"
    "v_exp_f32 v220, v220\n"
    "v_mfma_f32_16x16x32_bf16 v[176:179], v[208:211], v[236:239], v[176:179]\n"
    "v_exp_f32 v221, v221\n"
    "v_mfma_f32_16x16x32_bf16 v[180:183], v[212:215], v[236:239], v[180:183]\n"
    "v_exp_f32 v222, v222\n"
    "v_mfma_f32_16x16x32_bf16 v[184:187], a[248:251], v[236:239], v[184:187]\n"
    "v_exp_f32 v223, v223\n"
    "v_mfma_f32_16x16x32_bf16 v[188:191], a[252:255], v[236:239], v[188:191]\n"
    "ds_read_b64_tr_b16 a[244:245], v250 offset:1032\n"
    "ds_read_b64_tr_b16 a[246:247], v250 offset:5128\n"
    "v_mfma_f32_16x16x32_bf16 v[224:227], a[192:195], a[16:19], 0\n"
    "v_cvt_pk_bf16_f32 v232, v216, v217\n"
    "v_cvt_pk_bf16_f32 v233, v218, v219\n"
    "v_mfma_f32_16x16x32_bf16 v[224:227], a[196:199], a[20:23], v[224:227]\n"
    "v_cvt_pk_bf16_f32 v234, v220, v221\n"
    "v_cvt_pk_bf16_f32 v235, v222, v223\n"
    "v_mfma_f32_16x16x32_bf16 v[224:227], a[200:203], a[24:27], v[224:227]\n"
    "ds_read_b64_tr_b16 a[248:249], v251 offset:1024\n"
    "ds_read_b64_tr_b16 a[250:251], v251 offset:5120\n"
    "v_mfma_f32_16x16x32_bf16 v[224:227], a[204:207], a[28:31], v[224:227]\n"
    "ds_read_b64_tr_b16 a[252:253], v251 offset:1032\n"
    "ds_read_b64_tr_b16 a[254:255], v251 offset:5128\n"
    "v_mfma_f32_16x16x32_bf16 v[228:231], a[208:211], a[16:19], 0\n"
    "v_add_f32 v240, v240, v216\n"
    "v_add_f32 v240, v240, v217\n"
    "v_mfma_f32_16x16x32_bf16 v[228:231], a[212:215], a[20:23], v[228:231]\n"
    "v_add_f32 v240, v240, v218\n"
    "v_add_f32 v240, v240, v219\n"
    "v_mfma_f32_16x16x32_bf16 v[228:231], a[216:219], a[24:27], v[228:231]\n"
    "v_add_f32 v240, v240, v220\n"
    "v_add_f32 v240, v240, v221\n"
    "v_mfma_f32_16x16x32_bf16 v[228:231], a[220:223], a[28:31], v[228:231]\n"
    "v_add_f32 v240, v240, v222\n"
    "v_add_f32 v240, v240, v223\n"
    "v_mfma_f32_16x16x32_bf16 a[128:131], a[224:227], v[232:235], a[128:131]\n"
    "v_exp_f32 v224, v224\n"
    "v_mfma_f32_16x16x32_bf16 a[132:135], a[228:231], v[232:235], a[132:135]\n"
    "v_exp_f32 v225, v225\n"
    "v_mfma_f32_16x16x32_bf16 a[136:139], a[232:235], v[232:235], a[136:139]\n"
    "v_exp_f32 v226, v226\n"
    "v_mfma_f32_16x16x32_bf16 a[140:143], a[236:239], v[232:235], a[140:143]\n"
    "v_exp_f32 v227, v227\n"
    "v_mfma_f32_16x16x32_bf16 a[144:147], a[240:243], v[232:235], a[144:147]\n"
    "s_waitcnt lgkmcnt(4)\n"
    "v_exp_f32 v228, v228\n"
    "v_mfma_f32_16x16x32_bf16 a[148:151], a[244:247], v[232:235], a[148:151]\n"
    "s_waitcnt lgkmcnt(2)\n"
    "v_exp_f32 v229, v229\n"
    "v_mfma_f32_16x16x32_bf16 a[152:155], a[248:251], v[232:235], a[152:155]\n"
    "s_waitcnt lgkmcnt(0)\n"
    "v_exp_f32 v230, v230\n"
    "v_mfma_f32_16x16x32_bf16 a[156:159], a[252:255], v[232:235], a[156:159]\n"
    "v_exp_f32 v231, v231\n"
    "v_mfma_f32_16x16x32_bf16 v[216:219], a[192:195], a[32:35], 0\n"
    "v_cvt_pk_bf16_f32 v236, v224, v225\n"
    "v_cvt_pk_bf16_f32 v237, v226, v227\n"
    "v_mfma_f32_16x16x32_bf16 v[216:219], a[196:199], a[36:39], v[216:219]\n"
    "v_cvt_pk_bf16_f32 v238, v228, v229\n"
    "v_cvt_pk_bf16_f32 v239, v230, v231\n"
    "v_mfma_f32_16x16x32_bf16 v[216:219], a[200:203], a[40:43], v[216:219]\n"
    "v_add_f32 v241, v241, v224\n"
    "v_add_f32 v241, v241, v225\n"
    "v_mfma_f32_16x16x32_bf16 v[216:219], a[204:207], a[44:47], v[216:219]\n"
    "v_add_f32 v241, v241, v226\n"
    "v_add_f32 v241, v241, v227\n"
    "v_mfma_f32_16x16x32_bf16 v[220:223], a[208:211], a[32:35], 0\n"
    "v_add_f32 v241, v241, v228\n"
    "v_add_f32 v241, v241, v229\n"
    "v_mfma_f32_16x16x32_bf16 v[220:223], a[212:215], a[36:39], v[220:223]\n"
    "v_add_f32 v241, v241, v230\n"
    "v_add_f32 v241, v241, v231\n"
    "v_mfma_f32_16x16x32_bf16 v[220:223], a[216:219], a[40:43], v[220:223]\n"
    "v_exp_f32 v216, v216\n"
    "v_mfma_f32_16x16x32_bf16 v[220:223], a[220:223], a[44:47], v[220:223]\n"
    "v_exp_f32 v217, v217\n"
    "v_mfma_f32_16x16x32_bf16 a[160:163], a[224:227], v[236:239], a[160:163]\n"
    "v_exp_f32 v218, v218\n"
    "v_mfma_f32_16x16x32_bf16 a[164:167], a[228:231], v[236:239], a[164:167]\n"
    "v_exp_f32 v219, v219\n"
    "v_mfma_f32_16x16x32_bf16 a[168:171], a[232:235], v[236:239], a[168:171]\n"
    "v_exp_f32 v220, v220\n"
    "v_mfma_f32_16x16x32_bf16 a[172:175], a[236:239], v[236:239], a[172:175]\n"
    "v_exp_f32 v221, v221\n"
    "v_mfma_f32_16x16x32_bf16 a[176:179], a[240:243], v[236:239], a[176:179]\n"
    "v_exp_f32 v222, v222\n"
    "v_mfma_f32_16x16x32_bf16 a[180:183], a[244:247], v[236:239], a[180:183]\n"
    "v_exp_f32 v223, v223\n"
    "v_mfma_f32_16x16x32_bf16 a[184:187], a[248:251], v[236:239], a[184:187]\n"
    "v_cvt_pk_bf16_f32 v232, v216, v217\n"
    "v_cvt_pk_bf16_f32 v233, v218, v219\n"
    "v_mfma_f32_16x16x32_bf16 a[188:191], a[252:255], v[236:239], a[188:191]\n"
    "v_cvt_pk_bf16_f32 v234, v220, v221\n"
    "v_cvt_pk_bf16_f32 v235, v222, v223\n"
    "v_mfma_f32_16x16x32_bf16 v[224:227], a[192:195], a[48:51], 0\n"
    "v_add_f32 v242, v242, v216\n"
    "v_add_f32 v242, v242, v217\n"
    "v_mfma_f32_16x16x32_bf16 v[224:227], a[196:199], a[52:55], v[224:227]\n"
    "v_add_f32 v242, v242, v218\n"
    "v_add_f32 v242, v242, v219\n"
    "v_mfma_f32_16x16x32_bf16 v[224:227], a[200:203], a[56:59], v[224:227]\n"
    "v_add_f32 v242, v242, v220\n"
    "v_add_f32 v242, v242, v221\n"
    "v_mfma_f32_16x16x32_bf16 v[224:227], a[204:207], a[60:63], v[224:227]\n"
    "v_add_f32 v242, v242, v222\n"
    "v_add_f32 v242, v242, v223\n"
    "v_mfma_f32_16x16x32_bf16 v[228:231], a[208:211], a[48:51], 0\n"
    "v_mfma_f32_16x16x32_bf16 v[228:231], a[212:215], a[52:55], v[228:231]\n"
    "v_mfma_f32_16x16x32_bf16 v[228:231], a[216:219], a[56:59], v[228:231]\n"
    "v_exp_f32 v224, v224\n"
    "v_mfma_f32_16x16x32_bf16 v[228:231], a[220:223], a[60:63], v[228:231]\n"
    "v_exp_f32 v225, v225\n"
    "v_mfma_f32_16x16x32_bf16 v[0:3], a[224:227], v[232:235], v[0:3]\n"
    "v_exp_f32 v226, v226\n"
    "v_mfma_f32_16x16x32_bf16 v[4:7], a[228:231], v[232:235], v[4:7]\n"
    "v_exp_f32 v227, v227\n"
    "v_mfma_f32_16x16x32_bf16 v[8:11], a[232:235], v[232:235], v[8:11]\n"
    "v_exp_f32 v228, v228\n"
    "v_mfma_f32_16x16x32_bf16 v[12:15], a[236:239], v[232:235], v[12:15]\n"
    "v_exp_f32 v229, v229\n"
    "v_mfma_f32_16x16x32_bf16 v[16:19], a[240:243], v[232:235], v[16:19]\n"
    "v_exp_f32 v230, v230\n"
    "v_mfma_f32_16x16x32_bf16 v[20:23], a[244:247], v[232:235], v[20:23]\n"
    "v_exp_f32 v231, v231\n"
    "v_mfma_f32_16x16x32_bf16 v[24:27], a[248:251], v[232:235], v[24:27]\n"
    "v_cvt_pk_bf16_f32 v236, v224, v225\n"
    "v_cvt_pk_bf16_f32 v237, v226, v227\n"
    "v_mfma_f32_16x16x32_bf16 v[28:31], a[252:255], v[232:235], v[28:31]\n"
    "v_cvt_pk_bf16_f32 v238, v228, v229\n"
    "v_cvt_pk_bf16_f32 v239, v230, v231\n"
    "v_mfma_f32_16x16x32_bf16 v[216:219], a[192:195], a[64:67], 0\n"
    "v_add_f32 v243, v243, v224\n"
    "v_add_f32 v243, v243, v225\n"
    "v_mfma_f32_16x16x32_bf16 v[216:219], a[196:199], a[68:71], v[216:219]\n"
    "v_add_f32 v243, v243, v226\n"
    "v_add_f32 v243, v243, v227\n"
    "v_mfma_f32_16x16x32_bf16 v[216:219], a[200:203], a[72:75], v[216:219]\n"
    "v_add_f32 v243, v243, v228\n"
    "v_add_f32 v243, v243, v229\n"
    "v_mfma_f32_16x16x32_bf16 v[216:219], a[204:207], a[76:79], v[216:219]\n"
    "v_add_f32 v243, v243, v230\n"
    "v_add_f32 v243, v243, v231\n"
    "v_mfma_f32_16x16x32_bf16 v[220:223], a[208:211], a[64:67], 0\n"
    "v_mfma_f32_16x16x32_bf16 v[220:223], a[212:215], a[68:71], v[220:223]\n"
    "v_mfma_f32_16x16x32_bf16 v[220:223], a[216:219], a[72:75], v[220:223]\n"
    "v_exp_f32 v216, v216\n"
    "v_mfma_f32_16x16x32_bf16 v[220:223], a[220:223], a[76:79], v[220:223]\n"
    "v_exp_f32 v217, v217\n"
    "v_mfma_f32_16x16x32_bf16 v[32:35], a[224:227], v[236:239], v[32:35]\n"
    "s_barrier\n"
    "v_mfma_f32_16x16x32_bf16 v[36:39], a[228:231], v[236:239], v[36:39]\n"
    "v_exp_f32 v218, v218\n"
    "v_mfma_f32_16x16x32_bf16 v[40:43], a[232:235], v[236:239], v[40:43]\n"
    "v_exp_f32 v219, v219\n"
    "v_mfma_f32_16x16x32_bf16 v[44:47], a[236:239], v[236:239], v[44:47]\n"
    "v_exp_f32 v220, v220\n"
    "v_mfma_f32_16x16x32_bf16 v[48:51], a[240:243], v[236:239], v[48:51]\n"
    "v_exp_f32 v221, v221\n"
    "v_mfma_f32_16x16x32_bf16 v[52:55], a[244:247], v[236:239], v[52:55]\n"
    "v_exp_f32 v222, v222\n"
    "v_mfma_f32_16x16x32_bf16 v[56:59], a[248:251], v[236:239], v[56:59]\n"
    "v_exp_f32 v223, v223\n"
    "v_mfma_f32_16x16x32_bf16 v[60:63], a[252:255], v[236:239], v[60:63]\n"
    "v_cvt_pk_bf16_f32 v232, v216, v217\n"
    "v_cvt_pk_bf16_f32 v233, v218, v219\n"
    "v_mfma_f32_16x16x32_bf16 v[224:227], a[192:195], a[80:83], 0\n"
    "v_cvt_pk_bf16_f32 v234, v220, v221\n"
    "v_cvt_pk_bf16_f32 v235, v222, v223\n"
    "v_mfma_f32_16x16x32_bf16 v[224:227], a[196:199], a[84:87], v[224:227]\n"
    "v_add_f32 v244, v244, v216\n"
    "v_add_f32 v244, v244, v217\n"
    "v_mfma_f32_16x16x32_bf16 v[224:227], a[200:203], a[88:91], v[224:227]\n"
    "v_add_f32 v244, v244, v218\n"
    "v_add_f32 v244, v244, v219\n"
    "v_mfma_f32_16x16x32_bf16 v[224:227], a[204:207], a[92:95], v[224:227]\n"
    "v_add_f32 v244, v244, v220\n"
    "v_add_f32 v244, v244, v221\n"
    "v_mfma_f32_16x16x32_bf16 v[228:231], a[208:211], a[80:83], 0\n"
    "v_add_f32 v244, v244, v222\n"
    "v_add_f32 v244, v244, v223\n"
    "v_mfma_f32_16x16x32_bf16 v[228:231], a[212:215], a[84:87], v[228:231]\n"
    "ds_read_b64_tr_b16 v[192:193], v250 offset:16384\n"
    "ds_read_b64_tr_b16 v[194:195], v250 offset:20480\n"
    "v_mfma_f32_16x16x32_bf16 v[228:231], a[216:219], a[88:91], v[228:231]\n"
    "v_exp_f32 v224, v224\n"
    "v_mfma_f32_16x16x32_bf16 v[228:231], a[220:223], a[92:95], v[228:231]\n"
    "v_exp_f32 v225, v225\n"
    "v_mfma_f32_16x16x32_bf16 v[64:67], a[224:227], v[232:235], v[64:67]\n"
    "v_exp_f32 v226, v226\n"
    "v_mfma_f32_16x16x32_bf16 v[68:71], a[228:231], v[232:235], v[68:71]\n"
    "v_exp_f32 v227, v227\n"
    "v_mfma_f32_16x16x32_bf16 v[72:75], a[232:235], v[232:235], v[72:75]\n"
    "v_exp_f32 v228, v228\n"
    "v_mfma_f32_16x16x32_bf16 v[76:79], a[236:239], v[232:235], v[76:79]\n"
    "v_exp_f32 v229, v229\n"
    "v_mfma_f32_16x16x32_bf16 v[80:83], a[240:243], v[232:235], v[80:83]\n"
    "v_exp_f32 v230, v230\n"
    "v_mfma_f32_16x16x32_bf16 v[84:87], a[244:247], v[232:235], v[84:87]\n"
    "v_exp_f32 v231, v231\n"
    "v_mfma_f32_16x16x32_bf16 v[88:91], a[248:251], v[232:235], v[88:91]\n"
    "v_cvt_pk_bf16_f32 v236, v224, v225\n"
    "v_cvt_pk_bf16_f32 v237, v226, v227\n"
    "v_mfma_f32_16x16x32_bf16 v[92:95], a[252:255], v[232:235], v[92:95]\n"
    "v_cvt_pk_bf16_f32 v238, v228, v229\n"
    "v_cvt_pk_bf16_f32 v239, v230, v231\n"
    "v_mfma_f32_16x16x32_bf16 v[216:219], a[192:195], a[96:99], 0\n"
    "v_add_f32 v245, v245, v224\n"
    "v_add_f32 v245, v245, v225\n"
    "v_mfma_f32_16x16x32_bf16 v[216:219], a[196:199], a[100:103], v[216:219]\n"
    "v_add_f32 v245, v245, v226\n"
    "v_add_f32 v245, v245, v227\n"
    "v_mfma_f32_16x16x32_bf16 v[216:219], a[200:203], a[104:107], v[216:219]\n"
    "v_add_f32 v245, v245, v228\n"
    "v_add_f32 v245, v245, v229\n"
    "v_mfma_f32_16x16x32_bf16 v[216:219], a[204:207], a[108:111], v[216:219]\n"
    "v_add_f32 v245, v245, v230\n"
    "v_add_f32 v245, v245, v231\n"
    "v_mfma_f32_16x16x32_bf16 v[220:223], a[208:211], a[96:99], 0\n"
    "ds_read_b64_tr_b16 v[196:197], v250 offset:16392\n"
    "ds_read_b64_tr_b16 v[198:199], v250 offset:20488\n"
    "v_mfma_f32_16x16x32_bf16 v[220:223], a[212:215], a[100:103], v[220:223]\n"
    "ds_read_b64_tr_b16 v[200:201], v251 offset:16384\n"
    "ds_read_b64_tr_b16 v[202:203], v251 offset:20480\n"
    "v_mfma_f32_16x16x32_bf16 v[220:223], a[216:219], a[104:107], v[220:223]\n"
    "v_exp_f32 v216, v216\n"
    "v_mfma_f32_16x16x32_bf16 v[220:223], a[220:223], a[108:111], v[220:223]\n"
    "v_exp_f32 v217, v217\n"
    "v_mfma_f32_16x16x32_bf16 v[96:99], a[224:227], v[236:239], v[96:99]\n"
    "v_exp_f32 v218, v218\n"
    "v_mfma_f32_16x16x32_bf16 v[100:103], a[228:231], v[236:239], v[100:103]\n"
    "v_exp_f32 v219, v219\n"
    "v_mfma_f32_16x16x32_bf16 v[104:107], a[232:235], v[236:239], v[104:107]\n"
    "v_exp_f32 v220, v220\n"
    "v_mfma_f32_16x16x32_bf16 v[108:111], a[236:239], v[236:239], v[108:111]\n"
    "v_exp_f32 v221, v221\n"
    "v_mfma_f32_16x16x32_bf16 v[112:115], a[240:243], v[236:239], v[112:115]\n"
    "v_exp_f32 v222, v222\n"
    "v_mfma_f32_16x16x32_bf16 v[116:119], a[244:247], v[236:239], v[116:119]\n"
    "v_exp_f32 v223, v223\n"
    "v_mfma_f32_16x16x32_bf16 v[120:123], a[248:251], v[236:239], v[120:123]\n"
    "v_cvt_pk_bf16_f32 v232, v216, v217\n"
    "v_cvt_pk_bf16_f32 v233, v218, v219\n"
    "v_mfma_f32_16x16x32_bf16 v[124:127], a[252:255], v[236:239], v[124:127]\n"
    "v_cvt_pk_bf16_f32 v234, v220, v221\n"
    "v_cvt_pk_bf16_f32 v235, v222, v223\n"
    "v_mfma_f32_16x16x32_bf16 v[224:227], a[192:195], a[112:115], 0\n"
    "ds_read_b128 a[192:195], v248 offset:16384\n"
    "v_add_f32 v246, v246, v216\n"
    "v_mfma_f32_16x16x32_bf16 v[224:227], a[196:199], a[116:119], v[224:227]\n"
    "ds_read_b128 a[196:199], v249 offset:16384\n"
    "v_add_f32 v246, v246, v217\n"
    "v_mfma_f32_16x16x32_bf16 v[224:227], a[200:203], a[120:123], v[224:227]\n"
    "ds_read_b128 a[200:203], v248 offset:17408\n"
    "v_add_f32 v246, v246, v218\n"
    "v_mfma_f32_16x16x32_bf16 v[224:227], a[204:207], a[124:127], v[224:227]\n"
    "ds_read_b128 a[204:207], v249 offset:17408\n"
    "v_add_f32 v246, v246, v219\n"
    "v_mfma_f32_16x16x32_bf16 v[228:231], a[208:211], a[112:115], 0\n"
    "ds_read_b128 a[208:211], v248 offset:20480\n"
    "v_add_f32 v246, v246, v220\n"
    "v_mfma_f32_16x16x32_bf16 v[228:231], a[212:215], a[116:119], v[228:231]\n"
    "ds_read_b128 a[212:215], v249 offset:20480\n"
    "v_add_f32 v246, v246, v221\n"
    "v_mfma_f32_16x16x32_bf16 v[228:231], a[216:219], a[120:123], v[228:231]\n"
    "ds_read_b128 a[216:219], v248 offset:21504\n"
    "v_add_f32 v246, v246, v222\n"
    "v_mfma_f32_16x16x32_bf16 v[228:231], a[220:223], a[124:127], v[228:231]\n"
    "ds_read_b128 a[220:223], v249 offset:21504\n"
    "v_add_f32 v246, v246, v223\n"
    "v_mfma_f32_16x16x32_bf16 v[128:131], a[224:227], v[232:235], v[128:131]\n"
    "v_exp_f32 v224, v224\n"
    "v_mfma_f32_16x16x32_bf16 v[132:135], a[228:231], v[232:235], v[132:135]\n"
    "v_exp_f32 v225, v225\n"
    "v_mfma_f32_16x16x32_bf16 v[136:139], a[232:235], v[232:235], v[136:139]\n"
    "v_exp_f32 v226, v226\n"
    "v_mfma_f32_16x16x32_bf16 v[140:143], a[236:239], v[232:235], v[140:143]\n"
    "v_exp_f32 v227, v227\n"
    "v_mfma_f32_16x16x32_bf16 v[144:147], a[240:243], v[232:235], v[144:147]\n"
    "v_exp_f32 v228, v228\n"
    "v_mfma_f32_16x16x32_bf16 v[148:151], a[244:247], v[232:235], v[148:151]\n"
    "v_exp_f32 v229, v229\n"
    "v_mfma_f32_16x16x32_bf16 v[152:155], a[248:251], v[232:235], v[152:155]\n"
    "v_exp_f32 v230, v230\n"
    "v_mfma_f32_16x16x32_bf16 v[156:159], a[252:255], v[232:235], v[156:159]\n"
    "s_waitcnt lgkmcnt(7)\n"
    "v_exp_f32 v231, v231\n"
    "v_mfma_f32_16x16x32_bf16 v[216:219], a[192:195], a[0:3], 0\n"
    "s_waitcnt lgkmcnt(6)\n"
    "v_cvt_pk_bf16_f32 v236, v224, v225\n"
    "v_cvt_pk_bf16_f32 v237, v226, v227\n"
    "v_mfma_f32_16x16x32_bf16 v[216:219], a[196:199], a[4:7], v[216:219]\n"
    "s_waitcnt lgkmcnt(5)\n"
    "v_cvt_pk_bf16_f32 v238, v228, v229\n"
    "v_cvt_pk_bf16_f32 v239, v230, v231\n"
    "v_mfma_f32_16x16x32_bf16 v[216:219], a[200:203], a[8:11], v[216:219]\n"
    "s_waitcnt lgkmcnt(4)\n"
    "v_add_f32 v247, v247, v224\n"
    "v_add_f32 v247, v247, v225\n"
    "v_mfma_f32_16x16x32_bf16 v[216:219], a[204:207], a[12:15], v[216:219]\n"
    "s_waitcnt lgkmcnt(3)\n"
    "v_add_f32 v247, v247, v226\n"
    "v_add_f32 v247, v247, v227\n"
    "v_mfma_f32_16x16x32_bf16 v[220:223], a[208:211], a[0:3], 0\n"
    "s_waitcnt lgkmcnt(2)\n"
    "v_add_f32 v247, v247, v228\n"
    "v_add_f32 v247, v247, v229\n"
    "v_mfma_f32_16x16x32_bf16 v[220:223], a[212:215], a[4:7], v[220:223]\n"
    "s_waitcnt lgkmcnt(1)\n"
    "v_add_f32 v247, v247, v230\n"
    "v_add_f32 v247, v247, v231\n"
    "v_mfma_f32_16x16x32_bf16 v[220:223], a[216:219], a[8:11], v[220:223]\n"
    "s_waitcnt lgkmcnt(0)\n"
    "ds_read_b64_tr_b16 v[204:205], v251 offset:16392\n"
    "ds_read_b64_tr_b16 v[206:207], v251 offset:20488\n"
    "v_mfma_f32_16x16x32_bf16 v[220:223], a[220:223], a[12:15], v[220:223]\n"
    "ds_read_b64_tr_b16 v[208:209], v250 offset:17408\n"
    "ds_read_b64_tr_b16 v[210:211], v250 offset:21504\n"
    "v_mfma_f32_16x16x32_bf16 v[160:163], a[224:227], v[236:239], v[160:163]\n"
    "v_exp_f32 v216, v216\n"
    "v_mfma_f32_16x16x32_bf16 v[164:167], a[228:231], v[236:239], v[164:167]\n"
    "v_exp_f32 v217, v217\n"
    "v_mfma_f32_16x16x32_bf16 v[168:171], a[232:235], v[236:239], v[168:171]\n"
    "v_exp_f32 v218, v218\n"
    "v_mfma_f32_16x16x32_bf16 v[172:175], a[236:239], v[236:239], v[172:175]\n"
    "v_exp_f32 v219, v219\n"
    "v_mfma_f32_16x16x32_bf16 v[176:179], a[240:243], v[236:239], v[176:179]\n"
    "v_exp_f32 v220, v220\n"
    "v_mfma_f32_16x16x32_bf16 v[180:183], a[244:247], v[236:239], v[180:183]\n"
    "v_exp_f32 v221, v221\n"
    "v_mfma_f32_16x16x32_bf16 v[184:187], a[248:251], v[236:239], v[184:187]\n"
    "v_exp_f32 v222, v222\n"
    "v_mfma_f32_16x16x32_bf16 v[188:191], a[252:255], v[236:239], v[188:191]\n"
    "v_exp_f32 v223, v223\n"
    "v_mfma_f32_16x16x32_bf16 v[224:227], a[192:195], a[16:19], 0\n"
    "ds_read_b64_tr_b16 v[212:213], v250 offset:17416\n"
    "ds_read_b64_tr_b16 v[214:215], v250 offset:21512\n"
    "v_mfma_f32_16x16x32_bf16 v[224:227], a[196:199], a[20:23], v[224:227]\n"
    "v_cvt_pk_bf16_f32 v232, v216, v217\n"
    "v_cvt_pk_bf16_f32 v233, v218, v219\n"
    "v_mfma_f32_16x16x32_bf16 v[224:227], a[200:203], a[24:27], v[224:227]\n"
    "v_cvt_pk_bf16_f32 v234, v220, v221\n"
    "v_cvt_pk_bf16_f32 v235, v222, v223\n"
    "v_mfma_f32_16x16x32_bf16 v[224:227], a[204:207], a[28:31], v[224:227]\n"
    "ds_read_b64_tr_b16 a[248:249], v251 offset:17408\n"
    "ds_read_b64_tr_b16 a[250:251], v251 offset:21504\n"
    "v_mfma_f32_16x16x32_bf16 v[228:231], a[208:211], a[16:19], 0\n"
    "ds_read_b64_tr_b16 a[252:253], v251 offset:17416\n"
    "ds_read_b64_tr_b16 a[254:255], v251 offset:21512\n"
    "v_mfma_f32_16x16x32_bf16 v[228:231], a[212:215], a[20:23], v[228:231]\n"
    "v_add_f32 v240, v240, v216\n"
    "v_add_f32 v240, v240, v217\n"
    "v_mfma_f32_16x16x32_bf16 v[228:231], a[216:219], a[24:27], v[228:231]\n"
    "v_add_f32 v240, v240, v218\n"
    "v_add_f32 v240, v240, v219\n"
    "v_mfma_f32_16x16x32_bf16 v[228:231], a[220:223], a[28:31], v[228:231]\n"
    "v_add_f32 v240, v240, v220\n"
    "v_add_f32 v240, v240, v221\n"
    "v_mfma_f32_16x16x32_bf16 a[128:131], v[192:195], v[232:235], a[128:131]\n"
    "v_add_f32 v240, v240, v222\n"
    "v_add_f32 v240, v240, v223\n"
    "v_mfma_f32_16x16x32_bf16 a[132:135], v[196:199], v[232:235], a[132:135]\n"
    "v_exp_f32 v224, v224\n"
    "v_mfma_f32_16x16x32_bf16 a[136:139], v[200:203], v[232:235], a[136:139]\n"
    "s_waitcnt lgkmcnt(8)\n"
    "v_exp_f32 v225, v225\n"
    "v_mfma_f32_16x16x32_bf16 a[140:143], v[204:207], v[232:235], a[140:143]\n"
    "s_waitcnt lgkmcnt(6)\n"
    "v_exp_f32 v226, v226\n"
    "v_mfma_f32_16x16x32_bf16 a[144:147], v[208:211], v[232:235], a[144:147]\n"
    "s_waitcnt lgkmcnt(4)\n"
    "v_exp_f32 v227, v227\n"
    "v_mfma_f32_16x16x32_bf16 a[148:151], v[212:215], v[232:235], a[148:151]\n"
    "s_waitcnt lgkmcnt(2)\n"
    "v_exp_f32 v228, v228\n"
    "v_mfma_f32_16x16x32_bf16 a[152:155], a[248:251], v[232:235], a[152:155]\n"
    "s_waitcnt lgkmcnt(0)\n"
    "v_exp_f32 v229, v229\n"
    "v_mfma_f32_16x16x32_bf16 a[156:159], a[252:255], v[232:235], a[156:159]\n"
    "v_exp_f32 v230, v230\n"
    "v_mfma_f32_16x16x32_bf16 v[216:219], a[192:195], a[32:35], 0\n"
    "v_exp_f32 v231, v231\n"
    "v_mfma_f32_16x16x32_bf16 v[216:219], a[196:199], a[36:39], v[216:219]\n"
    "v_cvt_pk_bf16_f32 v236, v224, v225\n"
    "v_cvt_pk_bf16_f32 v237, v226, v227\n"
    "v_mfma_f32_16x16x32_bf16 v[216:219], a[200:203], a[40:43], v[216:219]\n"
    "v_cvt_pk_bf16_f32 v238, v228, v229\n"
    "v_cvt_pk_bf16_f32 v239, v230, v231\n"
    "v_mfma_f32_16x16x32_bf16 v[216:219], a[204:207], a[44:47], v[216:219]\n"
    "v_add_f32 v241, v241, v224\n"
    "v_add_f32 v241, v241, v225\n"
    "v_mfma_f32_16x16x32_bf16 v[220:223], a[208:211], a[32:35], 0\n"
    "v_add_f32 v241, v241, v226\n"
    "v_add_f32 v241, v241, v227\n"
    "v_mfma_f32_16x16x32_bf16 v[220:223], a[212:215], a[36:39], v[220:223]\n"
    "v_add_f32 v241, v241, v228\n"
    "v_add_f32 v241, v241, v229\n"
    "v_mfma_f32_16x16x32_bf16 v[220:223], a[216:219], a[40:43], v[220:223]\n"
    "v_add_f32 v241, v241, v230\n"
    "v_add_f32 v241, v241, v231\n"
    "v_mfma_f32_16x16x32_bf16 v[220:223], a[220:223], a[44:47], v[220:223]\n"
    "v_exp_f32 v216, v216\n"
    "v_mfma_f32_16x16x32_bf16 a[160:163], v[192:195], v[236:239], a[160:163]\n"
    "v_exp_f32 v217, v217\n"
    "v_mfma_f32_16x16x32_bf16 a[164:167], v[196:199], v[236:239], a[164:167]\n"
    "v_exp_f32 v218, v218\n"
    "v_mfma_f32_16x16x32_bf16 a[168:171], v[200:203], v[236:239], a[168:171]\n"
    "v_exp_f32 v219, v219\n"
    "v_mfma_f32_16x16x32_bf16 a[172:175], v[204:207], v[236:239], a[172:175]\n"
    "v_exp_f32 v220, v220\n"
    "v_mfma_f32_16x16x32_bf16 a[176:179], v[208:211], v[236:239], a[176:179]\n"
    "v_exp_f32 v221, v221\n"
    "v_mfma_f32_16x16x32_bf16 a[180:183], v[212:215], v[236:239], a[180:183]\n"
    "v_exp_f32 v222, v222\n"
    "v_mfma_f32_16x16x32_bf16 a[184:187], a[248:251], v[236:239], a[184:187]\n"
    "v_exp_f32 v223, v223\n"
    "v_mfma_f32_16x16x32_bf16 a[188:191], a[252:255], v[236:239], a[188:191]\n"
    "v_cvt_pk_bf16_f32 v232, v216, v217\n"
    "v_cvt_pk_bf16_f32 v233, v218, v219\n"
    "v_mfma_f32_16x16x32_bf16 v[224:227], a[192:195], a[48:51], 0\n"
    "v_cvt_pk_bf16_f32 v234, v220, v221\n"
    "v_cvt_pk_bf16_f32 v235, v222, v223\n"
    "v_mfma_f32_16x16x32_bf16 v[224:227], a[196:199], a[52:55], v[224:227]\n"
    "v_add_f32 v242, v242, v216\n"
    "v_add_f32 v242, v242, v217\n"
    "v_mfma_f32_16x16x32_bf16 v[224:227], a[200:203], a[56:59], v[224:227]\n"
    "v_add_f32 v242, v242, v218\n"
    "v_add_f32 v242, v242, v219\n"
    "v_mfma_f32_16x16x32_bf16 v[224:227], a[204:207], a[60:63], v[224:227]\n"
    "v_add_f32 v242, v242, v220\n"
    "v_add_f32 v242, v242, v221\n"
    "v_mfma_f32_16x16x32_bf16 v[228:231], a[208:211], a[48:51], 0\n"
    "v_add_f32 v242, v242, v222\n"
    "v_add_f32 v242, v242, v223\n"
    "v_mfma_f32_16x16x32_bf16 v[228:231], a[212:215], a[52:55], v[228:231]\n"
    "v_xor_b32 v248, s55, v248\n"
    "v_xor_b32 v249, s55, v249\n"
    "v_mfma_f32_16x16x32_bf16 v[228:231], a[216:219], a[56:59], v[228:231]\n"
    "v_exp_f32 v224, v224\n"
    "v_mfma_f32_16x16x32_bf16 v[228:231], a[220:223], a[60:63], v[228:231]\n"
    "v_exp_f32 v225, v225\n"
    "v_mfma_f32_16x16x32_bf16 v[0:3], v[192:195], v[232:235], v[0:3]\n"
    "v_exp_f32 v226, v226\n"
    "v_mfma_f32_16x16x32_bf16 v[4:7], v[196:199], v[232:235], v[4:7]\n"
    "v_exp_f32 v227, v227\n"
    "v_mfma_f32_16x16x32_bf16 v[8:11], v[200:203], v[232:235], v[8:11]\n"
    "v_exp_f32 v228, v228\n"
    "v_mfma_f32_16x16x32_bf16 v[12:15], v[204:207], v[232:235], v[12:15]\n"
    "v_exp_f32 v229, v229\n"
    "v_mfma_f32_16x16x32_bf16 v[16:19], v[208:211], v[232:235], v[16:19]\n"
    "v_exp_f32 v230, v230\n"
    "v_mfma_f32_16x16x32_bf16 v[20:23], v[212:215], v[232:235], v[20:23]\n"
    "v_exp_f32 v231, v231\n"
    "v_mfma_f32_16x16x32_bf16 v[24:27], a[248:251], v[232:235], v[24:27]\n"
    "v_cvt_pk_bf16_f32 v236, v224, v225\n"
    "v_cvt_pk_bf16_f32 v237, v226, v227\n"
    "v_mfma_f32_16x16x32_bf16 v[28:31], a[252:255], v[232:235], v[28:31]\n"
    "v_cvt_pk_bf16_f32 v238, v228, v229\n"
    "v_cvt_pk_bf16_f32 v239, v230, v231\n"
    "v_mfma_f32_16x16x32_bf16 v[216:219], a[192:195], a[64:67], 0\n"
    "v_add_f32 v243, v243, v224\n"
    "v_add_f32 v243, v243, v225\n"
    "v_mfma_f32_16x16x32_bf16 v[216:219], a[196:199], a[68:71], v[216:219]\n"
    "v_add_f32 v243, v243, v226\n"
    "v_add_f32 v243, v243, v227\n"
    "v_mfma_f32_16x16x32_bf16 v[216:219], a[200:203], a[72:75], v[216:219]\n"
    "v_add_f32 v243, v243, v228\n"
    "v_add_f32 v243, v243, v229\n"
    "v_mfma_f32_16x16x32_bf16 v[216:219], a[204:207], a[76:79], v[216:219]\n"
    "v_add_f32 v243, v243, v230\n"
    "v_add_f32 v243, v243, v231\n"
    "v_mfma_f32_16x16x32_bf16 v[220:223], a[208:211], a[64:67], 0\n"
    "v_xor_b32 v250, s55, v250\n"
    "v_xor_b32 v251, s55, v251\n"
    "v_mfma_f32_16x16x32_bf16 v[220:223], a[212:215], a[68:71], v[220:223]\n"
    "s_xor_b32 s55, s55, 0x18000\n"
    "ds_read_b64_tr_b16 a[224:225], v250 offset:0\n"
    "ds_read_b64_tr_b16 a[226:227], v250 offset:4096\n"
    "v_mfma_f32_16x16x32_bf16 v[220:223], a[216:219], a[72:75], v[220:223]\n"
    "v_exp_f32 v216, v216\n"
    "v_mfma_f32_16x16x32_bf16 v[220:223], a[220:223], a[76:79], v[220:223]\n"
    "v_exp_f32 v217, v217\n"
    "v_mfma_f32_16x16x32_bf16 v[32:35], v[192:195], v[236:239], v[32:35]\n"
    "v_exp_f32 v218, v218\n"
    "v_mfma_f32_16x16x32_bf16 v[36:39], v[196:199], v[236:239], v[36:39]\n"
    "v_exp_f32 v219, v219\n"
    "v_mfma_f32_16x16x32_bf16 v[40:43], v[200:203], v[236:239], v[40:43]\n"
    "v_exp_f32 v220, v220\n"
    "v_mfma_f32_16x16x32_bf16 v[44:47], v[204:207], v[236:239], v[44:47]\n"
    "v_exp_f32 v221, v221\n"
    "v_mfma_f32_16x16x32_bf16 v[48:51], v[208:211], v[236:239], v[48:51]\n"
    "v_exp_f32 v222, v222\n"
    "v_mfma_f32_16x16x32_bf16 v[52:55], v[212:215], v[236:239], v[52:55]\n"
    "v_exp_f32 v223, v223\n"
    "v_mfma_f32_16x16x32_bf16 v[56:59], a[248:251], v[236:239], v[56:59]\n"
    "v_cvt_pk_bf16_f32 v232, v216, v217\n"
    "v_cvt_pk_bf16_f32 v233, v218, v219\n"
    "v_mfma_f32_16x16x32_bf16 v[60:63], a[252:255], v[236:239], v[60:63]\n"
    "v_cvt_pk_bf16_f32 v234, v220, v221\n"
    "v_cvt_pk_bf16_f32 v235, v222, v223\n"
    "v_mfma_f32_16x16x32_bf16 v[224:227], a[192:195], a[80:83], 0\n"
    "v_add_f32 v244, v244, v216\n"
    "v_add_f32 v244, v244, v217\n"
    "v_mfma_f32_16x16x32_bf16 v[224:227], a[196:199], a[84:87], v[224:227]\n"
    "v_add_f32 v244, v244, v218\n"
    "v_add_f32 v244, v244, v219\n"
    "v_mfma_f32_16x16x32_bf16 v[224:227], a[200:203], a[88:91], v[224:227]\n"
    "v_add_f32 v244, v244, v220\n"
    "v_add_f32 v244, v244, v221\n"
    "v_mfma_f32_16x16x32_bf16 v[224:227], a[204:207], a[92:95], v[224:227]\n"
    "v_add_f32 v244, v244, v222\n"
    "v_add_f32 v244, v244, v223\n"
    "v_mfma_f32_16x16x32_bf16 v[228:231], a[208:211], a[80:83], 0\n"
    "ds_read_b64_tr_b16 a[228:229], v250 offset:8\n"
    "ds_read_b64_tr_b16 a[230:231], v250 offset:4104\n"
    "v_mfma_f32_16x16x32_bf16 v[228:231], a[212:215], a[84:87], v[228:231]\n"
    "ds_read_b64_tr_b16 a[232:233], v251 offset:0\n"
    "ds_read_b64_tr_b16 a[234:235], v251 offset:4096\n"
    "v_mfma_f32_16x16x32_bf16 v[228:231], a[216:219], a[88:91], v[228:231]\n"
    "v_exp_f32 v224, v224\n"
    "v_mfma_f32_16x16x32_bf16 v[228:231], a[220:223], a[92:95], v[228:231]\n"
    "v_exp_f32 v225, v225\n"
    "v_mfma_f32_16x16x32_bf16 v[64:67], v[192:195], v[232:235], v[64:67]\n"
    "v_exp_f32 v226, v226\n"
    "v_mfma_f32_16x16x32_bf16 v[68:71], v[196:199], v[232:235], v[68:71]\n"
    "v_exp_f32 v227, v227\n"
    "v_mfma_f32_16x16x32_bf16 v[72:75], v[200:203], v[232:235], v[72:75]\n"
    "v_exp_f32 v228, v228\n"
    "v_mfma_f32_16x16x32_bf16 v[76:79], v[204:207], v[232:235], v[76:79]\n"
    "v_exp_f32 v229, v229\n"
    "v_mfma_f32_16x16x32_bf16 v[80:83], v[208:211], v[232:235], v[80:83]\n"
    "v_exp_f32 v230, v230\n"
    "v_mfma_f32_16x16x32_bf16 v[84:87], v[212:215], v[232:235], v[84:87]\n"
    "v_exp_f32 v231, v231\n"
    "v_mfma_f32_16x16x32_bf16 v[88:91], a[248:251], v[232:235], v[88:91]\n"
    "v_cvt_pk_bf16_f32 v236, v224, v225\n"
    "v_cvt_pk_bf16_f32 v237, v226, v227\n"
    "v_mfma_f32_16x16x32_bf16 v[92:95], a[252:255], v[232:235], v[92:95]\n"
    "v_cvt_pk_bf16_f32 v238, v228, v229\n"
    "v_cvt_pk_bf16_f32 v239, v230, v231\n"
    "v_mfma_f32_16x16x32_bf16 v[216:219], a[192:195], a[96:99], 0\n"
    "v_add_f32 v245, v245, v224\n"
    "v_add_f32 v245, v245, v225\n"
    "v_mfma_f32_16x16x32_bf16 v[216:219], a[196:199], a[100:103], v[216:219]\n"
    "v_add_f32 v245, v245, v226\n"
    "v_add_f32 v245, v245, v227\n"
    "v_mfma_f32_16x16x32_bf16 v[216:219], a[200:203], a[104:107], v[216:219]\n"
    "v_add_f32 v245, v245, v228\n"
    "v_add_f32 v245, v245, v229\n"
    "v_mfma_f32_16x16x32_bf16 v[216:219], a[204:207], a[108:111], v[216:219]\n"
    "v_add_f32 v245, v245, v230\n"
    "v_add_f32 v245, v245, v231\n"
    "v_mfma_f32_16x16x32_bf16 v[220:223], a[208:211], a[96:99], 0\n"
    "ds_read_b64_tr_b16 a[236:237], v251 offset:8\n"
    "ds_read_b64_tr_b16 a[238:239], v251 offset:4104\n"
    "v_mfma_f32_16x16x32_bf16 v[220:223], a[212:215], a[100:103], v[220:223]\n"
    "ds_read_b64_tr_b16 a[240:241], v250 offset:1024\n"
    "ds_read_b64_tr_b16 a[242:243], v250 offset:5120\n"
    "v_mfma_f32_16x16x32_bf16 v[220:223], a[216:219], a[104:107], v[220:223]\n"
    "v_exp_f32 v216, v216\n"
    "v_mfma_f32_16x16x32_bf16 v[220:223], a[220:223], a[108:111], v[220:223]\n"
    "v_exp_f32 v217, v217\n"
    "v_mfma_f32_16x16x32_bf16 v[96:99], v[192:195], v[236:239], v[96:99]\n"
    "v_exp_f32 v218, v218\n"
    "v_mfma_f32_16x16x32_bf16 v[100:103], v[196:199], v[236:239], v[100:103]\n"
    "v_exp_f32 v219, v219\n"
    "v_mfma_f32_16x16x32_bf16 v[104:107], v[200:203], v[236:239], v[104:107]\n"
    "v_exp_f32 v220, v220\n"
    "v_mfma_f32_16x16x32_bf16 v[108:111], v[204:207], v[236:239], v[108:111]\n"
    "v_exp_f32 v221, v221\n"
    "v_mfma_f32_16x16x32_bf16 v[112:115], v[208:211], v[236:239], v[112:115]\n"
    "v_exp_f32 v222, v222\n"
    "v_mfma_f32_16x16x32_bf16 v[116:119], v[212:215], v[236:239], v[116:119]\n"
    "v_exp_f32 v223, v223\n"
    "v_mfma_f32_16x16x32_bf16 v[120:123], a[248:251], v[236:239], v[120:123]\n"
    "v_cvt_pk_bf16_f32 v232, v216, v217\n"
    "v_cvt_pk_bf16_f32 v233, v218, v219\n"
    "v_mfma_f32_16x16x32_bf16 v[124:127], a[252:255], v[236:239], v[124:127]\n"
    "v_cvt_pk_bf16_f32 v234, v220, v221\n"
    "v_cvt_pk_bf16_f32 v235, v222, v223\n"
    "v_mfma_f32_16x16x32_bf16 v[224:227], a[192:195], a[112:115], 0\n"
    "ds_read_b128 a[192:195], v248 offset:0\n"
    "v_add_f32 v246, v246, v216\n"
    "v_mfma_f32_16x16x32_bf16 v[224:227], a[196:199], a[116:119], v[224:227]\n"
    "ds_read_b128 a[196:199], v249 offset:0\n"
    "v_add_f32 v246, v246, v217\n"
    "v_mfma_f32_16x16x32_bf16 v[224:227], a[200:203], a[120:123], v[224:227]\n"
    "ds_read_b128 a[200:203], v248 offset:1024\n"
    "v_add_f32 v246, v246, v218\n"
    "v_mfma_f32_16x16x32_bf16 v[224:227], a[204:207], a[124:127], v[224:227]\n"
    "ds_read_b128 a[204:207], v249 offset:1024\n"
    "v_add_f32 v246, v246, v219\n"
    "v_mfma_f32_16x16x32_bf16 v[228:231], a[208:211], a[112:115], 0\n"
    "ds_read_b128 a[208:211], v248 offset:4096\n"
    "v_add_f32 v246, v246, v220\n"
    "v_mfma_f32_16x16x32_bf16 v[228:231], a[212:215], a[116:119], v[228:231]\n"
    "ds_read_b128 a[212:215], v249 offset:4096\n"
    "v_add_f32 v246, v246, v221\n"
    "v_mfma_f32_16x16x32_bf16 v[228:231], a[216:219], a[120:123], v[228:231]\n"
    "ds_read_b128 a[216:219], v248 offset:5120\n"
    "v_add_f32 v246, v246, v222\n"
    "v_mfma_f32_16x16x32_bf16 v[228:231], a[220:223], a[124:127], v[228:231]\n"
    "ds_read_b128 a[220:223], v249 offset:5120\n"
    "v_add_f32 v246, v246, v223\n"
    "v_mfma_f32_16x16x32_bf16 v[128:131], v[192:195], v[232:235], v[128:131]\n"
    "v_exp_f32 v224, v224\n"
    "v_mfma_f32_16x16x32_bf16 v[132:135], v[196:199], v[232:235], v[132:135]\n"
    "v_exp_f32 v225, v225\n"
    "v_mfma_f32_16x16x32_bf16 v[136:139], v[200:203], v[232:235], v[136:139]\n"
    "v_exp_f32 v226, v226\n"
    "v_mfma_f32_16x16x32_bf16 v[140:143], v[204:207], v[232:235], v[140:143]\n"
    "v_exp_f32 v227, v227\n"
    "v_mfma_f32_16x16x32_bf16 v[144:147], v[208:211], v[232:235], v[144:147]\n"
    "s_sub_u32 s54, s54, 1\n"
    "v_exp_f32 v228, v228\n"
    "v_mfma_f32_16x16x32_bf16 v[148:151], v[212:215], v[232:235], v[148:151]\n"
    "v_exp_f32 v229, v229\n"
    "v_mfma_f32_16x16x32_bf16 v[152:155], a[248:251], v[232:235], v[152:155]\n"
    "s_cmp_lg_u32 s54, 0\n"
    "v_exp_f32 v230, v230\n"
    "v_mfma_f32_16x16x32_bf16 v[156:159], a[252:255], v[232:235], v[156:159]\n"
    "s_waitcnt lgkmcnt(7)\n"
    "s_cbranch_scc1 LA_%=\n"
    "v_mfma_f32_16x16x32_bf16 v[216:219], a[192:195], a[32:35], 0\n"
    "s_waitcnt lgkmcnt(6)\n"
    "s_add_u32 s59, s67, 32768\n"
    "v_exp_f32 v231, v231\n"
    "v_mfma_f32_16x16x32_bf16 v[216:219], a[196:199], a[36:39], v[216:219]\n"
    "s_waitcnt lgkmcnt(5)\n"
    "s_add_u32 s60, s67, 49152\n"
    "v_cvt_pk_bf16_f32 v236, v224, v225\n"
    "v_cvt_pk_bf16_f32 v237, v226, v227\n"
    "v_mfma_f32_16x16x32_bf16 v[216:219], a[200:203], a[40:43], v[216:219]\n"
    "s_waitcnt lgkmcnt(4)\n"
    "v_cvt_pk_bf16_f32 v238, v228, v229\n"
    "v_cvt_pk_bf16_f32 v239, v230, v231\n"
    "v_mfma_f32_16x16x32_bf16 v[216:219], a[204:207], a[44:47], v[216:219]\n"
    "s_waitcnt lgkmcnt(3)\n"
    "v_add_f32 v247, v247, v224\n"
    "v_add_f32 v247, v247, v225\n"
    "v_mfma_f32_16x16x32_bf16 v[220:223], a[208:211], a[32:35], 0\n"
    "s_waitcnt lgkmcnt(2)\n"
    "v_add_f32 v247, v247, v226\n"
    "v_add_f32 v247, v247, v227\n"
    "v_mfma_f32_16x16x32_bf16 v[220:223], a[212:215], a[36:39], v[220:223]\n"
    "s_waitcnt lgkmcnt(1)\n"
    "v_add_f32 v247, v247, v228\n"
    "v_add_f32 v247, v247, v229\n"
    "v_mfma_f32_16x16x32_bf16 v[220:223], a[216:219], a[40:43], v[220:223]\n"
    "s_waitcnt lgkmcnt(0)\n"
    "v_add_f32 v247, v247, v230\n"
    "v_add_f32 v247, v247, v231\n"
    "v_mfma_f32_16x16x32_bf16 v[220:223], a[220:223], a[44:47], v[220:223]\n"
    "v_exp_f32 v216, v216\n"
    "v_mfma_f32_16x16x32_bf16 v[160:163], v[192:195], v[236:239], v[160:163]\n"
    "v_exp_f32 v217, v217\n"
    "v_mfma_f32_16x16x32_bf16 v[164:167], v[196:199], v[236:239], v[164:167]\n"
    "v_exp_f32 v218, v218\n"
    "v_mfma_f32_16x16x32_bf16 v[168:171], v[200:203], v[236:239], v[168:171]\n"
    "v_exp_f32 v219, v219\n"
    "v_mfma_f32_16x16x32_bf16 v[172:175], v[204:207], v[236:239], v[172:175]\n"
    "v_exp_f32 v220, v220\n"
    "v_mfma_f32_16x16x32_bf16 v[176:179], v[208:211], v[236:239], v[176:179]\n"
    "v_exp_f32 v221, v221\n"
    "v_mfma_f32_16x16x32_bf16 v[180:183], v[212:215], v[236:239], v[180:183]\n"
    "v_exp_f32 v222, v222\n"
    "v_mfma_f32_16x16x32_bf16 v[184:187], a[248:251], v[236:239], v[184:187]\n"
    "v_exp_f32 v223, v223\n"
    "v_mfma_f32_16x16x32_bf16 v[188:191], a[252:255], v[236:239], v[188:191]\n"
    "ds_read_b64_tr_b16 a[244:245], v250 offset:1032\n"
    "ds_read_b64_tr_b16 a[246:247], v250 offset:5128\n"
    "v_mfma_f32_16x16x32_bf16 v[224:227], a[192:195], a[48:51], 0\n"
    "v_cvt_pk_bf16_f32 v232, v216, v217\n"
    "v_cvt_pk_bf16_f32 v233, v218, v219\n"
    "v_mfma_f32_16x16x32_bf16 v[224:227], a[196:199], a[52:55], v[224:227]\n"
    "v_cvt_pk_bf16_f32 v234, v220, v221\n"
    "v_cvt_pk_bf16_f32 v235, v222, v223\n"
    "v_mfma_f32_16x16x32_bf16 v[224:227], a[200:203], a[56:59], v[224:227]\n"
    "ds_read_b64_tr_b16 a[248:249], v251 offset:1024\n"
    "ds_read_b64_tr_b16 a[250:251], v251 offset:5120\n"
    "v_mfma_f32_16x16x32_bf16 v[224:227], a[204:207], a[60:63], v[224:227]\n"
    "ds_read_b64_tr_b16 a[252:253], v251 offset:1032\n"
    "ds_read_b64_tr_b16 a[254:255], v251 offset:5128\n"
    "v_mfma_f32_16x16x32_bf16 v[228:231], a[208:211], a[48:51], 0\n"
    "v_add_f32 v242, v242, v216\n"
    "v_add_f32 v242, v242, v217\n"
    "v_mfma_f32_16x16x32_bf16 v[228:231], a[212:215], a[52:55], v[228:231]\n"
    "v_add_f32 v242, v242, v218\n"
    "v_add_f32 v242, v242, v219\n"
    "v_mfma_f32_16x16x32_bf16 v[228:231], a[216:219], a[56:59], v[228:231]\n"
    "v_add_f32 v242, v242, v220\n"
    "v_add_f32 v242, v242, v221\n"
    "v_mfma_f32_16x16x32_bf16 v[228:231], a[220:223], a[60:63], v[228:231]\n"
    "v_add_f32 v242, v242, v222\n"
    "v_add_f32 v242, v242, v223\n"
    "v_mfma_f32_16x16x32_bf16 v[0:3], a[224:227], v[232:235], v[0:3]\n"
    "v_exp_f32 v224, v224\n"
    "v_mfma_f32_16x16x32_bf16 v[4:7], a[228:231], v[232:235], v[4:7]\n"
    "v_exp_f32 v225, v225\n"
    "v_mfma_f32_16x16x32_bf16 v[8:11], a[232:235], v[232:235], v[8:11]\n"
    "v_exp_f32 v226, v226\n"
    "v_mfma_f32_16x16x32_bf16 v[12:15], a[236:239], v[232:235], v[12:15]\n"
    "v_exp_f32 v227, v227\n"
    "v_mfma_f32_16x16x32_bf16 v[16:19], a[240:243], v[232:235], v[16:19]\n"
    "s_waitcnt lgkmcnt(4)\n"
    "v_exp_f32 v228, v228\n"
    "v_mfma_f32_16x16x32_bf16 v[20:23], a[244:247], v[232:235], v[20:23]\n"
    "s_waitcnt lgkmcnt(2)\n"
    "v_exp_f32 v229, v229\n"
    "v_mfma_f32_16x16x32_bf16 v[24:27], a[248:251], v[232:235], v[24:27]\n"
    "s_waitcnt lgkmcnt(0)\n"
    "v_exp_f32 v230, v230\n"
    "v_mfma_f32_16x16x32_bf16 v[28:31], a[252:255], v[232:235], v[28:31]\n"
    "v_exp_f32 v231, v231\n"
    "v_mfma_f32_16x16x32_bf16 v[216:219], a[192:195], a[64:67], 0\n"
    "v_cvt_pk_bf16_f32 v236, v224, v225\n"
    "v_cvt_pk_bf16_f32 v237, v226, v227\n"
    "v_mfma_f32_16x16x32_bf16 v[216:219], a[196:199], a[68:71], v[216:219]\n"
    "v_cvt_pk_bf16_f32 v238, v228, v229\n"
    "v_cvt_pk_bf16_f32 v239, v230, v231\n"
    "v_mfma_f32_16x16x32_bf16 v[216:219], a[200:203], a[72:75], v[216:219]\n"
    "v_add_f32 v243, v243, v224\n"
    "v_add_f32 v243, v243, v225\n"
    "v_mfma_f32_16x16x32_bf16 v[216:219], a[204:207], a[76:79], v[216:219]\n"
    "v_add_f32 v243, v243, v226\n"
    "v_add_f32 v243, v243, v227\n"
    "v_mfma_f32_16x16x32_bf16 v[220:223], a[208:211], a[64:67], 0\n"
    "v_add_f32 v243, v243, v228\n"
    "v_add_f32 v243, v243, v229\n"
    "v_mfma_f32_16x16x32_bf16 v[220:223], a[212:215], a[68:71], v[220:223]\n"
    "v_add_f32 v243, v243, v230\n"
    "v_add_f32 v243, v243, v231\n"
    "v_mfma_f32_16x16x32_bf16 v[220:223], a[216:219], a[72:75], v[220:223]\n"
    "v_exp_f32 v216, v216\n"
    "v_mfma_f32_16x16x32_bf16 v[220:223], a[220:223], a[76:79], v[220:223]\n"
    "v_exp_f32 v217, v217\n"
    "v_mfma_f32_16x16x32_bf16 v[32:35], a[224:227], v[236:239], v[32:35]\n"
    "v_exp_f32 v218, v218\n"
    "v_mfma_f32_16x16x32_bf16 v[36:39], a[228:231], v[236:239], v[36:39]\n"
    "v_exp_f32 v219, v219\n"
    "v_mfma_f32_16x16x32_bf16 v[40:43], a[232:235], v[236:239], v[40:43]\n"
    "v_exp_f32 v220, v220\n"
    "v_mfma_f32_16x16x32_bf16 v[44:47], a[236:239], v[236:239], v[44:47]\n"
    "v_exp_f32 v221, v221\n"
    "v_mfma_f32_16x16x32_bf16 v[48:51], a[240:243], v[236:239], v[48:51]\n"
    "v_exp_f32 v222, v222\n"
    "v_mfma_f32_16x16x32_bf16 v[52:55], a[244:247], v[236:239], v[52:55]\n"
    "v_exp_f32 v223, v223\n"
    "v_mfma_f32_16x16x32_bf16 v[56:59], a[248:251], v[236:239], v[56:59]\n"
    "v_cvt_pk_bf16_f32 v232, v216, v217\n"
    "v_cvt_pk_bf16_f32 v233, v218, v219\n"
    "v_mfma_f32_16x16x32_bf16 v[60:63], a[252:255], v[236:239], v[60:63]\n"
    "v_cvt_pk_bf16_f32 v234, v220, v221\n"
    "v_cvt_pk_bf16_f32 v235, v222, v223\n"
    "v_mfma_f32_16x16x32_bf16 v[224:227], a[192:195], a[80:83], 0\n"
    "v_add_f32 v244, v244, v216\n"
    "v_add_f32 v244, v244, v217\n"
    "v_mfma_f32_16x16x32_bf16 v[224:227], a[196:199], a[84:87], v[224:227]\n"
    "v_add_f32 v244, v244, v218\n"
    "v_add_f32 v244, v244, v219\n"
    "v_mfma_f32_16x16x32_bf16 v[224:227], a[200:203], a[88:91], v[224:227]\n"
    "v_add_f32 v244, v244, v220\n"
    "v_add_f32 v244, v244, v221\n"
    "v_mfma_f32_16x16x32_bf16 v[224:227], a[204:207], a[92:95], v[224:227]\n"
    "v_add_f32 v244, v244, v222\n"
    "v_add_f32 v244, v244, v223\n"
    "v_mfma_f32_16x16x32_bf16 v[228:231], a[208:211], a[80:83], 0\n"
    "v_mfma_f32_16x16x32_bf16 v[228:231], a[212:215], a[84:87], v[228:231]\n"
    "v_mfma_f32_16x16x32_bf16 v[228:231], a[216:219], a[88:91], v[228:231]\n"
    "v_exp_f32 v224, v224\n"
    "v_mfma_f32_16x16x32_bf16 v[228:231], a[220:223], a[92:95], v[228:231]\n"
    "v_exp_f32 v225, v225\n"
    "v_mfma_f32_16x16x32_bf16 v[64:67], a[224:227], v[232:235], v[64:67]\n"
    "s_waitcnt vmcnt(63)\n"
    "s_barrier\n"
    "ds_read_b128 a[192:195], v248 offset:16384\n"
    "ds_read_b128 a[196:199], v249 offset:16384\n"
    "v_mfma_f32_16x16x32_bf16 v[68:71], a[228:231], v[232:235], v[68:71]\n"
    "ds_read_b128 a[200:203], v248 offset:17408\n"
    "ds_read_b128 a[204:207], v249 offset:17408\n"
    "v_mfma_f32_16x16x32_bf16 v[72:75], a[232:235], v[232:235], v[72:75]\n"
    "ds_read_b128 a[208:211], v248 offset:20480\n"
    "ds_read_b128 a[212:215], v249 offset:20480\n"
    "v_mfma_f32_16x16x32_bf16 v[76:79], a[236:239], v[232:235], v[76:79]\n"
    "ds_read_b128 a[216:219], v248 offset:21504\n"
    "ds_read_b128 a[220:223], v249 offset:21504\n"
    "v_mfma_f32_16x16x32_bf16 v[80:83], a[240:243], v[232:235], v[80:83]\n"
    "v_exp_f32 v226, v226\n"
    "v_mfma_f32_16x16x32_bf16 v[84:87], a[244:247], v[232:235], v[84:87]\n"
    "v_exp_f32 v227, v227\n"
    "v_mfma_f32_16x16x32_bf16 v[88:91], a[248:251], v[232:235], v[88:91]\n"
    "v_exp_f32 v228, v228\n"
    "v_mfma_f32_16x16x32_bf16 v[92:95], a[252:255], v[232:235], v[92:95]\n"
    "s_waitcnt lgkmcnt(7)\n"
    "v_exp_f32 v229, v229\n"
    "v_mfma_f32_16x16x32_bf16 v[216:219], a[192:195], a[32:35], 0\n"
    "s_waitcnt lgkmcnt(6)\n"
    "v_exp_f32 v230, v230\n"
    "v_mfma_f32_16x16x32_bf16 v[216:219], a[196:199], a[36:39], v[216:219]\n"
    "s_waitcnt lgkmcnt(5)\n"
    "v_exp_f32 v231, v231\n"
    "v_mfma_f32_16x16x32_bf16 v[216:219], a[200:203], a[40:43], v[216:219]\n"
    "s_waitcnt lgkmcnt(4)\n"
    "v_cvt_pk_bf16_f32 v236, v224, v225\n"
    "v_cvt_pk_bf16_f32 v237, v226, v227\n"
    "v_mfma_f32_16x16x32_bf16 v[216:219], a[204:207], a[44:47], v[216:219]\n"
    "s_waitcnt lgkmcnt(3)\n"
    "v_cvt_pk_bf16_f32 v238, v228, v229\n"
    "v_cvt_pk_bf16_f32 v239, v230, v231\n"
    "v_mfma_f32_16x16x32_bf16 v[220:223], a[208:211], a[32:35], 0\n"
    "s_waitcnt lgkmcnt(2)\n"
    "v_add_f32 v245, v245, v224\n"
    "v_add_f32 v245, v245, v225\n"
    "v_mfma_f32_16x16x32_bf16 v[220:223], a[212:215], a[36:39], v[220:223]\n"
    "s_waitcnt lgkmcnt(1)\n"
    "v_add_f32 v245, v245, v226\n"
    "v_add_f32 v245, v245, v227\n"
    "v_mfma_f32_16x16x32_bf16 v[220:223], a[216:219], a[40:43], v[220:223]\n"
    "s_waitcnt lgkmcnt(0)\n"
    "ds_read_b64_tr_b16 v[192:193], v250 offset:16384\n"
    "ds_read_b64_tr_b16 v[194:195], v250 offset:20480\n"
    "v_mfma_f32_16x16x32_bf16 v[220:223], a[220:223], a[44:47], v[220:223]\n"
    "ds_read_b64_tr_b16 v[196:197], v250 offset:16392\n"
    "ds_read_b64_tr_b16 v[198:199], v250 offset:20488\n"
    "v_mfma_f32_16x16x32_bf16 v[96:99], a[224:227], v[236:239], v[96:99]\n"
    "ds_read_b64_tr_b16 v[200:201], v251 offset:16384\n"
    "ds_read_b64_tr_b16 v[202:203], v251 offset:20480\n"
    "v_mfma_f32_16x16x32_bf16 v[100:103], a[228:231], v[236:239], v[100:103]\n"
    "v_add_f32 v245, v245, v228\n"
    "v_add_f32 v245, v245, v229\n"
    "v_mfma_f32_16x16x32_bf16 v[104:107], a[232:235], v[236:239], v[104:107]\n"
    "v_add_f32 v245, v245, v230\n"
    "v_add_f32 v245, v245, v231\n"
    "v_mfma_f32_16x16x32_bf16 v[108:111], a[236:239], v[236:239], v[108:111]\n"
    "ds_read_b64_tr_b16 v[204:205], v251 offset:16392\n"
    "ds_read_b64_tr_b16 v[206:207], v251 offset:20488\n"
    "v_mfma_f32_16x16x32_bf16 v[112:115], a[240:243], v[236:239], v[112:115]\n"
    "ds_read_b64_tr_b16 v[208:209], v250 offset:17408\n"
    "ds_read_b64_tr_b16 v[210:211], v250 offset:21504\n"
    "v_mfma_f32_16x16x32_bf16 v[116:119], a[244:247], v[236:239], v[116:119]\n"
    "v_exp_f32 v216, v216\n"
    "v_mfma_f32_16x16x32_bf16 v[120:123], a[248:251], v[236:239], v[120:123]\n"
    "v_exp_f32 v217, v217\n"
    "v_mfma_f32_16x16x32_bf16 v[124:127], a[252:255], v[236:239], v[124:127]\n"
    "v_exp_f32 v218, v218\n"
    "v_mfma_f32_16x16x32_bf16 v[224:227], a[192:195], a[48:51], 0\n"
    "v_exp_f32 v219, v219\n"
    "v_mfma_f32_16x16x32_bf16 v[224:227], a[196:199], a[52:55], v[224:227]\n"
    "v_exp_f32 v220, v220\n"
    "v_mfma_f32_16x16x32_bf16 v[224:227], a[200:203], a[56:59], v[224:227]\n"
    "v_exp_f32 v221, v221\n"
    "v_mfma_f32_16x16x32_bf16 v[224:227], a[204:207], a[60:63], v[224:227]\n"
    "v_exp_f32 v222, v222\n"
    "v_mfma_f32_16x16x32_bf16 v[228:231], a[208:211], a[48:51], 0\n"
    "v_exp_f32 v223, v223\n"
    "v_mfma_f32_16x16x32_bf16 v[228:231], a[212:215], a[52:55], v[228:231]\n"
    "ds_read_b64_tr_b16 v[212:213], v250 offset:17416\n"
    "ds_read_b64_tr_b16 v[214:215], v250 offset:21512\n"
    "v_mfma_f32_16x16x32_bf16 v[228:231], a[216:219], a[56:59], v[228:231]\n"
    "v_cvt_pk_bf16_f32 v232, v216, v217\n"
    "v_cvt_pk_bf16_f32 v233, v218, v219\n"
    "v_cvt_pk_bf16_f32 v234, v220, v221\n"
    "v_cvt_pk_bf16_f32 v235, v222, v223\n"
    "v_mfma_f32_16x16x32_bf16 v[228:231], a[220:223], a[60:63], v[228:231]\n"
    "s_waitcnt lgkmcnt(10)\n"
    "ds_read_b64_tr_b16 a[248:249], v251 offset:17408\n"
    "ds_read_b64_tr_b16 a[250:251], v251 offset:21504\n"
    "v_mfma_f32_16x16x32_bf16 v[0:3], v[192:195], v[232:235], v[0:3]\n"
    "s_waitcnt lgkmcnt(10)\n"
    "ds_read_b64_tr_b16 a[252:253], v251 offset:17416\n"
    "ds_read_b64_tr_b16 a[254:255], v251 offset:21512\n"
    "v_mfma_f32_16x16x32_bf16 v[4:7], v[196:199], v[232:235], v[4:7]\n"
    "s_waitcnt lgkmcnt(10)\n"
    "v_add_f32 v242, v242, v216\n"
    "v_add_f32 v242, v242, v217\n"
    "v_mfma_f32_16x16x32_bf16 v[8:11], v[200:203], v[232:235], v[8:11]\n"
    "s_waitcnt lgkmcnt(8)\n"
    "v_add_f32 v242, v242, v218\n"
    "v_add_f32 v242, v242, v219\n"
    "v_mfma_f32_16x16x32_bf16 v[12:15], v[204:207], v[232:235], v[12:15]\n"
    "s_waitcnt lgkmcnt(6)\n"
    "v_add_f32 v242, v242, v220\n"
    "v_add_f32 v242, v242, v221\n"
    "v_mfma_f32_16x16x32_bf16 v[16:19], v[208:211], v[232:235], v[16:19]\n"
    "s_waitcnt lgkmcnt(4)\n"
    "v_add_f32 v242, v242, v222\n"
    "v_add_f32 v242, v242, v223\n"
    "v_mfma_f32_16x16x32_bf16 v[20:23], v[212:215], v[232:235], v[20:23]\n"
    "s_waitcnt lgkmcnt(2)\n"
    "v_exp_f32 v224, v224\n"
    "v_mfma_f32_16x16x32_bf16 v[24:27], a[248:251], v[232:235], v[24:27]\n"
    "s_waitcnt lgkmcnt(0)\n"
    "v_exp_f32 v225, v225\n"
    "v_mfma_f32_16x16x32_bf16 v[28:31], a[252:255], v[232:235], v[28:31]\n"
    "v_exp_f32 v226, v226\n"
    "v_mfma_f32_16x16x32_bf16 v[216:219], a[192:195], a[64:67], 0\n"
    "v_exp_f32 v227, v227\n"
    "v_mfma_f32_16x16x32_bf16 v[216:219], a[196:199], a[68:71], v[216:219]\n"
    "v_exp_f32 v228, v228\n"
    "v_mfma_f32_16x16x32_bf16 v[216:219], a[200:203], a[72:75], v[216:219]\n"
    "v_exp_f32 v229, v229\n"
    "v_mfma_f32_16x16x32_bf16 v[216:219], a[204:207], a[76:79], v[216:219]\n"
    "v_exp_f32 v230, v230\n"
    "v_mfma_f32_16x16x32_bf16 v[220:223], a[208:211], a[64:67], 0\n"
    "v_exp_f32 v231, v231\n"
    "v_mfma_f32_16x16x32_bf16 v[220:223], a[212:215], a[68:71], v[220:223]\n"
    "v_cvt_pk_bf16_f32 v236, v224, v225\n"
    "v_cvt_pk_bf16_f32 v237, v226, v227\n"
    "v_mfma_f32_16x16x32_bf16 v[220:223], a[216:219], a[72:75], v[220:223]\n"
    "v_cvt_pk_bf16_f32 v238, v228, v229\n"
    "v_cvt_pk_bf16_f32 v239, v230, v231\n"
    "v_mfma_f32_16x16x32_bf16 v[220:223], a[220:223], a[76:79], v[220:223]\n"
    "v_add_f32 v243, v243, v224\n"
    "v_add_f32 v243, v243, v225\n"
    "v_mfma_f32_16x16x32_bf16 v[32:35], v[192:195], v[236:239], v[32:35]\n"
    "v_add_f32 v243, v243, v226\n"
    "v_add_f32 v243, v243, v227\n"
    "v_mfma_f32_16x16x32_bf16 v[36:39], v[196:199], v[236:239], v[36:39]\n"
    "v_add_f32 v243, v243, v228\n"
    "v_add_f32 v243, v243, v229\n"
    "v_mfma_f32_16x16x32_bf16 v[40:43], v[200:203], v[236:239], v[40:43]\n"
    "v_add_f32 v243, v243, v230\n"
    "v_add_f32 v243, v243, v231\n"
    "v_mfma_f32_16x16x32_bf16 v[44:47], v[204:207], v[236:239], v[44:47]\n"
    "v_exp_f32 v216, v216\n"
    "v_mfma_f32_16x16x32_bf16 v[48:51], v[208:211], v[236:239], v[48:51]\n"
    "v_exp_f32 v217, v217\n"
    "v_mfma_f32_16x16x32_bf16 v[52:55], v[212:215], v[236:239], v[52:55]\n"
    "v_exp_f32 v218, v218\n"
    "v_mfma_f32_16x16x32_bf16 v[56:59], a[248:251], v[236:239], v[56:59]\n"
    "v_exp_f32 v219, v219\n"
    "v_mfma_f32_16x16x32_bf16 v[60:63], a[252:255], v[236:239], v[60:63]\n"
    "v_exp_f32 v220, v220\n"
    "v_mfma_f32_16x16x32_bf16 v[224:227], a[192:195], a[80:83], 0\n"
    "v_exp_f32 v221, v221\n"
    "v_mfma_f32_16x16x32_bf16 v[224:227], a[196:199], a[84:87], v[224:227]\n"
    "v_exp_f32 v222, v222\n"
    "v_mfma_f32_16x16x32_bf16 v[224:227], a[200:203], a[88:91], v[224:227]\n"
    "v_exp_f32 v223, v223\n"
    "v_mfma_f32_16x16x32_bf16 v[224:227], a[204:207], a[92:95], v[224:227]\n"
    "v_cvt_pk_bf16_f32 v232, v216, v217\n"
    "v_cvt_pk_bf16_f32 v233, v218, v219\n"
    "v_mfma_f32_16x16x32_bf16 v[228:231], a[208:211], a[80:83], 0\n"
    "v_cvt_pk_bf16_f32 v234, v220, v221\n"
    "v_cvt_pk_bf16_f32 v235, v222, v223\n"
    "v_mfma_f32_16x16x32_bf16 v[228:231], a[212:215], a[84:87], v[228:231]\n"
    "v_xor_b32 v248, 32768, v248\n"
    "v_xor_b32 v249, 32768, v249\n"
    "v_mfma_f32_16x16x32_bf16 v[228:231], a[216:219], a[88:91], v[228:231]\n"
    "ds_read_b128 a[192:195], v248 offset:0\n"
    "ds_read_b128 a[196:199], v249 offset:0\n"
    "v_mfma_f32_16x16x32_bf16 v[228:231], a[220:223], a[92:95], v[228:231]\n"
    "ds_read_b128 a[200:203], v248 offset:1024\n"
    "ds_read_b128 a[204:207], v249 offset:1024\n"
    "v_mfma_f32_16x16x32_bf16 v[64:67], v[192:195], v[232:235], v[64:67]\n"
    "ds_read_b128 a[208:211], v248 offset:4096\n"
    "ds_read_b128 a[212:215], v249 offset:4096\n"
    "v_mfma_f32_16x16x32_bf16 v[68:71], v[196:199], v[232:235], v[68:71]\n"
    "v_add_f32 v244, v244, v216\n"
    "v_add_f32 v244, v244, v217\n"
    "v_mfma_f32_16x16x32_bf16 v[72:75], v[200:203], v[232:235], v[72:75]\n"
    "v_add_f32 v244, v244, v218\n"
    "v_add_f32 v244, v244, v219\n"
    "v_mfma_f32_16x16x32_bf16 v[76:79], v[204:207], v[232:235], v[76:79]\n"
    "ds_read_b128 a[216:219], v248 offset:5120\n"
    "ds_read_b128 a[220:223], v249 offset:5120\n"
    "v_mfma_f32_16x16x32_bf16 v[80:83], v[208:211], v[232:235], v[80:83]\n"
    "v_add_f32 v244, v244, v220\n"
    "v_add_f32 v244, v244, v221\n"
    "v_mfma_f32_16x16x32_bf16 v[84:87], v[212:215], v[232:235], v[84:87]\n"
    "v_add_f32 v244, v244, v222\n"
    "v_add_f32 v244, v244, v223\n"
    "v_mfma_f32_16x16x32_bf16 v[88:91], a[248:251], v[232:235], v[88:91]\n"
    "v_exp_f32 v224, v224\n"
    "v_mfma_f32_16x16x32_bf16 v[92:95], a[252:255], v[232:235], v[92:95]\n"
    "s_waitcnt lgkmcnt(7)\n"
    "v_exp_f32 v225, v225\n"
    "v_mfma_f32_16x16x32_bf16 v[216:219], a[192:195], a[32:35], 0\n"
    "s_waitcnt lgkmcnt(6)\n"
    "v_exp_f32 v226, v226\n"
    "v_mfma_f32_16x16x32_bf16 v[216:219], a[196:199], a[36:39], v[216:219]\n"
    "s_waitcnt lgkmcnt(5)\n"
    "v_exp_f32 v227, v227\n"
    "v_mfma_f32_16x16x32_bf16 v[216:219], a[200:203], a[40:43], v[216:219]\n"
    "s_waitcnt lgkmcnt(4)\n"
    "v_exp_f32 v228, v228\n"
    "v_mfma_f32_16x16x32_bf16 v[216:219], a[204:207], a[44:47], v[216:219]\n"
    "s_waitcnt lgkmcnt(3)\n"
    "v_exp_f32 v229, v229\n"
    "v_mfma_f32_16x16x32_bf16 v[220:223], a[208:211], a[32:35], 0\n"
    "s_waitcnt lgkmcnt(2)\n"
    "v_exp_f32 v230, v230\n"
    "v_mfma_f32_16x16x32_bf16 v[220:223], a[212:215], a[36:39], v[220:223]\n"
    "s_waitcnt lgkmcnt(1)\n"
    "v_exp_f32 v231, v231\n"
    "v_mfma_f32_16x16x32_bf16 v[220:223], a[216:219], a[40:43], v[220:223]\n"
    "s_waitcnt lgkmcnt(0)\n"
    "v_cvt_pk_bf16_f32 v236, v224, v225\n"
    "v_cvt_pk_bf16_f32 v237, v226, v227\n"
    "v_cvt_pk_bf16_f32 v238, v228, v229\n"
    "v_cvt_pk_bf16_f32 v239, v230, v231\n"
    "v_mfma_f32_16x16x32_bf16 v[220:223], a[220:223], a[44:47], v[220:223]\n"
    "v_add_f32 v245, v245, v224\n"
    "v_add_f32 v245, v245, v225\n"
    "v_mfma_f32_16x16x32_bf16 v[96:99], v[192:195], v[236:239], v[96:99]\n"
    "v_add_f32 v245, v245, v226\n"
    "v_add_f32 v245, v245, v227\n"
    "v_mfma_f32_16x16x32_bf16 v[100:103], v[196:199], v[236:239], v[100:103]\n"
    "v_xor_b32 v250, 32768, v250\n"
    "v_xor_b32 v251, 32768, v251\n"
    "v_mfma_f32_16x16x32_bf16 v[104:107], v[200:203], v[236:239], v[104:107]\n"
    "ds_read_b64_tr_b16 a[224:225], v250 offset:0\n"
    "ds_read_b64_tr_b16 a[226:227], v250 offset:4096\n"
    "v_mfma_f32_16x16x32_bf16 v[108:111], v[204:207], v[236:239], v[108:111]\n"
    "ds_read_b64_tr_b16 a[228:229], v250 offset:8\n"
    "ds_read_b64_tr_b16 a[230:231], v250 offset:4104\n"
    "v_mfma_f32_16x16x32_bf16 v[112:115], v[208:211], v[236:239], v[112:115]\n"
    "ds_read_b64_tr_b16 a[232:233], v251 offset:0\n"
    "ds_read_b64_tr_b16 a[234:235], v251 offset:4096\n"
    "v_mfma_f32_16x16x32_bf16 v[116:119], v[212:215], v[236:239], v[116:119]\n"
    "v_add_f32 v245, v245, v228\n"
    "v_add_f32 v245, v245, v229\n"
    "v_mfma_f32_16x16x32_bf16 v[120:123], a[248:251], v[236:239], v[120:123]\n"
    "v_add_f32 v245, v245, v230\n"
    "v_add_f32 v245, v245, v231\n"
    "v_mfma_f32_16x16x32_bf16 v[124:127], a[252:255], v[236:239], v[124:127]\n"
    "ds_read_b64_tr_b16 a[236:237], v251 offset:8\n"
    "ds_read_b64_tr_b16 a[238:239], v251 offset:4104\n"
    "v_mfma_f32_16x16x32_bf16 v[224:227], a[192:195], a[48:51], 0\n"
    "ds_read_b64_tr_b16 a[240:241], v250 offset:1024\n"
    "ds_read_b64_tr_b16 a[242:243], v250 offset:5120\n"
    "v_mfma_f32_16x16x32_bf16 v[224:227], a[196:199], a[52:55], v[224:227]\n"
    "v_exp_f32 v216, v216\n"
    "v_mfma_f32_16x16x32_bf16 v[224:227], a[200:203], a[56:59], v[224:227]\n"
    "v_exp_f32 v217, v217\n"
    "v_mfma_f32_16x16x32_bf16 v[224:227], a[204:207], a[60:63], v[224:227]\n"
    "v_exp_f32 v218, v218\n"
    "v_mfma_f32_16x16x32_bf16 v[228:231], a[208:211], a[48:51], 0\n"
    "v_exp_f32 v219, v219\n"
    "v_mfma_f32_16x16x32_bf16 v[228:231], a[212:215], a[52:55], v[228:231]\n"
    "v_exp_f32 v220, v220\n"
    "v_exp_f32 v221, v221\n"
    "v_exp_f32 v222, v222\n"
    "v_exp_f32 v223, v223\n"
    "v_mfma_f32_16x16x32_bf16 v[228:231], a[216:219], a[56:59], v[228:231]\n"
    "ds_read_b64_tr_b16 a[244:245], v250 offset:1032\n"
    "ds_read_b64_tr_b16 a[246:247], v250 offset:5128\n"
    "v_cvt_pk_bf16_f32 v232, v216, v217\n"
    "v_cvt_pk_bf16_f32 v233, v218, v219\n"
    "v_cvt_pk_bf16_f32 v234, v220, v221\n"
    "v_cvt_pk_bf16_f32 v235, v222, v223\n"
    "v_mfma_f32_16x16x32_bf16 v[228:231], a[220:223], a[60:63], v[228:231]\n"
    "s_waitcnt lgkmcnt(10)\n"
    "ds_read_b64_tr_b16 a[248:249], v251 offset:1024\n"
    "ds_read_b64_tr_b16 a[250:251], v251 offset:5120\n"
    "v_mfma_f32_16x16x32_bf16 v[0:3], a[224:227], v[232:235], v[0:3]\n"
    "s_waitcnt lgkmcnt(10)\n"
    "ds_read_b64_tr_b16 a[252:253], v251 offset:1032\n"
    "ds_read_b64_tr_b16 a[254:255], v251 offset:5128\n"
    "v_mfma_f32_16x16x32_bf16 v[4:7], a[228:231], v[232:235], v[4:7]\n"
    "s_waitcnt lgkmcnt(10)\n"
    "v_add_f32 v242, v242, v216\n"
    "v_add_f32 v242, v242, v217\n"
    "v_mfma_f32_16x16x32_bf16 v[8:11], a[232:235], v[232:235], v[8:11]\n"
    "s_waitcnt lgkmcnt(8)\n"
    "v_add_f32 v242, v242, v218\n"
    "v_add_f32 v242, v242, v219\n"
    "v_mfma_f32_16x16x32_bf16 v[12:15], a[236:239], v[232:235], v[12:15]\n"
    "s_waitcnt lgkmcnt(6)\n"
    "v_add_f32 v242, v242, v220\n"
    "v_add_f32 v242, v242, v221\n"
    "v_mfma_f32_16x16x32_bf16 v[16:19], a[240:243], v[232:235], v[16:19]\n"
    "s_waitcnt lgkmcnt(4)\n"
    "v_add_f32 v242, v242, v222\n"
    "v_add_f32 v242, v242, v223\n"
    "v_mfma_f32_16x16x32_bf16 v[20:23], a[244:247], v[232:235], v[20:23]\n"
    "s_waitcnt lgkmcnt(2)\n"
    "v_exp_f32 v224, v224\n"
    "v_mfma_f32_16x16x32_bf16 v[24:27], a[248:251], v[232:235], v[24:27]\n"
    "s_waitcnt lgkmcnt(0)\n"
    "v_exp_f32 v225, v225\n"
    "v_mfma_f32_16x16x32_bf16 v[28:31], a[252:255], v[232:235], v[28:31]\n"
    "v_exp_f32 v226, v226\n"
    "v_mfma_f32_16x16x32_bf16 v[216:219], a[192:195], a[64:67], 0\n"
    "v_exp_f32 v227, v227\n"
    "v_mfma_f32_16x16x32_bf16 v[216:219], a[196:199], a[68:71], v[216:219]\n"
    "v_exp_f32 v228, v228\n"
    "v_mfma_f32_16x16x32_bf16 v[216:219], a[200:203], a[72:75], v[216:219]\n"
    "v_exp_f32 v229, v229\n"
    "v_mfma_f32_16x16x32_bf16 v[216:219], a[204:207], a[76:79], v[216:219]\n"
    "v_exp_f32 v230, v230\n"
    "v_mfma_f32_16x16x32_bf16 v[220:223], a[208:211], a[64:67], 0\n"
    "v_exp_f32 v231, v231\n"
    "v_mfma_f32_16x16x32_bf16 v[220:223], a[212:215], a[68:71], v[220:223]\n"
    "v_cvt_pk_bf16_f32 v236, v224, v225\n"
    "v_cvt_pk_bf16_f32 v237, v226, v227\n"
    "v_mfma_f32_16x16x32_bf16 v[220:223], a[216:219], a[72:75], v[220:223]\n"
    "v_cvt_pk_bf16_f32 v238, v228, v229\n"
    "v_cvt_pk_bf16_f32 v239, v230, v231\n"
    "v_mfma_f32_16x16x32_bf16 v[220:223], a[220:223], a[76:79], v[220:223]\n"
    "v_add_f32 v243, v243, v224\n"
    "v_add_f32 v243, v243, v225\n"
    "v_mfma_f32_16x16x32_bf16 v[32:35], a[224:227], v[236:239], v[32:35]\n"
    "v_add_f32 v243, v243, v226\n"
    "v_add_f32 v243, v243, v227\n"
    "v_mfma_f32_16x16x32_bf16 v[36:39], a[228:231], v[236:239], v[36:39]\n"
    "v_add_f32 v243, v243, v228\n"
    "v_add_f32 v243, v243, v229\n"
    "v_mfma_f32_16x16x32_bf16 v[40:43], a[232:235], v[236:239], v[40:43]\n"
    "v_add_f32 v243, v243, v230\n"
    "v_add_f32 v243, v243, v231\n"
    "v_mfma_f32_16x16x32_bf16 v[44:47], a[236:239], v[236:239], v[44:47]\n"
    "v_exp_f32 v216, v216\n"
    "v_mfma_f32_16x16x32_bf16 v[48:51], a[240:243], v[236:239], v[48:51]\n"
    "v_exp_f32 v217, v217\n"
    "v_mfma_f32_16x16x32_bf16 v[52:55], a[244:247], v[236:239], v[52:55]\n"
    "v_exp_f32 v218, v218\n"
    "v_mfma_f32_16x16x32_bf16 v[56:59], a[248:251], v[236:239], v[56:59]\n"
    "v_exp_f32 v219, v219\n"
    "v_mfma_f32_16x16x32_bf16 v[60:63], a[252:255], v[236:239], v[60:63]\n"
    "v_exp_f32 v220, v220\n"
    "v_mfma_f32_16x16x32_bf16 v[224:227], a[192:195], a[80:83], 0\n"
    "v_exp_f32 v221, v221\n"
    "v_mfma_f32_16x16x32_bf16 v[224:227], a[196:199], a[84:87], v[224:227]\n"
    "v_exp_f32 v222, v222\n"
    "v_mfma_f32_16x16x32_bf16 v[224:227], a[200:203], a[88:91], v[224:227]\n"
    "v_exp_f32 v223, v223\n"
    "v_mfma_f32_16x16x32_bf16 v[224:227], a[204:207], a[92:95], v[224:227]\n"
    "v_cvt_pk_bf16_f32 v232, v216, v217\n"
    "v_cvt_pk_bf16_f32 v233, v218, v219\n"
    "v_mfma_f32_16x16x32_bf16 v[228:231], a[208:211], a[80:83], 0\n"
    "v_cvt_pk_bf16_f32 v234, v220, v221\n"
    "v_cvt_pk_bf16_f32 v235, v222, v223\n"
    "v_mfma_f32_16x16x32_bf16 v[228:231], a[212:215], a[84:87], v[228:231]\n"
    "v_add_f32 v244, v244, v216\n"
    "v_add_f32 v244, v244, v217\n"
    "v_mfma_f32_16x16x32_bf16 v[228:231], a[216:219], a[88:91], v[228:231]\n"
    "v_add_f32 v244, v244, v218\n"
    "v_add_f32 v244, v244, v219\n"
    "v_mfma_f32_16x16x32_bf16 v[228:231], a[220:223], a[92:95], v[228:231]\n"
    "v_add_f32 v244, v244, v220\n"
    "v_add_f32 v244, v244, v221\n"
    "v_mfma_f32_16x16x32_bf16 v[64:67], a[224:227], v[232:235], v[64:67]\n"
    "s_waitcnt vmcnt(63)\n"
    "s_barrier\n"
    "ds_read_b128 a[192:195], v248 offset:16384\n"
    "ds_read_b128 a[196:199], v249 offset:16384\n"
    "v_mfma_f32_16x16x32_bf16 v[68:71], a[228:231], v[232:235], v[68:71]\n"
    "ds_read_b128 a[200:203], v248 offset:17408\n"
    "ds_read_b128 a[204:207], v249 offset:17408\n"
    "v_mfma_f32_16x16x32_bf16 v[72:75], a[232:235], v[232:235], v[72:75]\n"
    "ds_read_b128 a[208:211], v248 offset:20480\n"
    "ds_read_b128 a[212:215], v249 offset:20480\n"
    "v_mfma_f32_16x16x32_bf16 v[76:79], a[236:239], v[232:235], v[76:79]\n"
    "ds_read_b128 a[216:219], v248 offset:21504\n"
    "ds_read_b128 a[220:223], v249 offset:21504\n"
    "v_mfma_f32_16x16x32_bf16 v[80:83], a[240:243], v[232:235], v[80:83]\n"
    "v_add_f32 v244, v244, v222\n"
    "v_add_f32 v244, v244, v223\n"
    "v_mfma_f32_16x16x32_bf16 v[84:87], a[244:247], v[232:235], v[84:87]\n"
    "v_exp_f32 v224, v224\n"
    "v_mfma_f32_16x16x32_bf16 v[88:91], a[248:251], v[232:235], v[88:91]\n"
    "v_exp_f32 v225, v225\n"
    "v_mfma_f32_16x16x32_bf16 v[92:95], a[252:255], v[232:235], v[92:95]\n"
    "s_waitcnt lgkmcnt(7)\n"
    "v_exp_f32 v226, v226\n"
    "v_mfma_f32_16x16x32_bf16 v[216:219], a[192:195], a[32:35], 0\n"
    "s_waitcnt lgkmcnt(6)\n"
    "v_exp_f32 v227, v227\n"
    "v_mfma_f32_16x16x32_bf16 v[216:219], a[196:199], a[36:39], v[216:219]\n"
    "s_waitcnt lgkmcnt(5)\n"
    "v_exp_f32 v228, v228\n"
    "v_mfma_f32_16x16x32_bf16 v[216:219], a[200:203], a[40:43], v[216:219]\n"
    "s_waitcnt lgkmcnt(4)\n"
    "v_exp_f32 v229, v229\n"
    "v_mfma_f32_16x16x32_bf16 v[216:219], a[204:207], a[44:47], v[216:219]\n"
    "s_waitcnt lgkmcnt(3)\n"
    "v_exp_f32 v230, v230\n"
    "v_mfma_f32_16x16x32_bf16 v[220:223], a[208:211], a[32:35], 0\n"
    "s_waitcnt lgkmcnt(2)\n"
    "v_exp_f32 v231, v231\n"
    "v_mfma_f32_16x16x32_bf16 v[220:223], a[212:215], a[36:39], v[220:223]\n"
    "s_waitcnt lgkmcnt(1)\n"
    "v_cvt_pk_bf16_f32 v236, v224, v225\n"
    "v_cvt_pk_bf16_f32 v237, v226, v227\n"
    "v_mfma_f32_16x16x32_bf16 v[220:223], a[216:219], a[40:43], v[220:223]\n"
    "s_waitcnt lgkmcnt(0)\n"
    "v_cvt_pk_bf16_f32 v238, v228, v229\n"
    "v_cvt_pk_bf16_f32 v239, v230, v231\n"
    "v_mfma_f32_16x16x32_bf16 v[220:223], a[220:223], a[44:47], v[220:223]\n"
    "v_add_f32 v245, v245, v224\n"
    "v_add_f32 v245, v245, v225\n"
    "v_mfma_f32_16x16x32_bf16 v[96:99], a[224:227], v[236:239], v[96:99]\n"
    "v_add_f32 v245, v245, v226\n"
    "v_add_f32 v245, v245, v227\n"
    "v_mfma_f32_16x16x32_bf16 v[100:103], a[228:231], v[236:239], v[100:103]\n"
    "ds_read_b64_tr_b16 v[192:193], v250 offset:16384\n"
    "ds_read_b64_tr_b16 v[194:195], v250 offset:20480\n"
    "v_mfma_f32_16x16x32_bf16 v[104:107], a[232:235], v[236:239], v[104:107]\n"
    "ds_read_b64_tr_b16 v[196:197], v250 offset:16392\n"
    "ds_read_b64_tr_b16 v[198:199], v250 offset:20488\n"
    "v_mfma_f32_16x16x32_bf16 v[108:111], a[236:239], v[236:239], v[108:111]\n"
    "ds_read_b64_tr_b16 v[200:201], v251 offset:16384\n"
    "ds_read_b64_tr_b16 v[202:203], v251 offset:20480\n"
    "v_mfma_f32_16x16x32_bf16 v[112:115], a[240:243], v[236:239], v[112:115]\n"
    "v_add_f32 v245, v245, v228\n"
    "v_add_f32 v245, v245, v229\n"
    "v_mfma_f32_16x16x32_bf16 v[116:119], a[244:247], v[236:239], v[116:119]\n"
    "v_add_f32 v245, v245, v230\n"
    "v_add_f32 v245, v245, v231\n"
    "v_mfma_f32_16x16x32_bf16 v[120:123], a[248:251], v[236:239], v[120:123]\n"
    "ds_read_b64_tr_b16 v[204:205], v251 offset:16392\n"
    "ds_read_b64_tr_b16 v[206:207], v251 offset:20488\n"
    "v_mfma_f32_16x16x32_bf16 v[124:127], a[252:255], v[236:239], v[124:127]\n"
    "ds_read_b64_tr_b16 v[208:209], v250 offset:17408\n"
    "ds_read_b64_tr_b16 v[210:211], v250 offset:21504\n"
    "v_mfma_f32_16x16x32_bf16 v[224:227], a[192:195], a[48:51], 0\n"
    "v_exp_f32 v216, v216\n"
    "v_mfma_f32_16x16x32_bf16 v[224:227], a[196:199], a[52:55], v[224:227]\n"
    "v_exp_f32 v217, v217\n"
    "v_mfma_f32_16x16x32_bf16 v[224:227], a[200:203], a[56:59], v[224:227]\n"
    "v_exp_f32 v218, v218\n"
    "v_mfma_f32_16x16x32_bf16 v[224:227], a[204:207], a[60:63], v[224:227]\n"
    "v_exp_f32 v219, v219\n"
    "v_mfma_f32_16x16x32_bf16 v[228:231], a[208:211], a[48:51], 0\n"
    "v_exp_f32 v220, v220\n"
    "v_mfma_f32_16x16x32_bf16 v[228:231], a[212:215], a[52:55], v[228:231]\n"
    "v_exp_f32 v221, v221\n"
    "v_exp_f32 v222, v222\n"
    "v_exp_f32 v223, v223\n"
    "v_mfma_f32_16x16x32_bf16 v[228:231], a[216:219], a[56:59], v[228:231]\n"
    "ds_read_b64_tr_b16 v[212:213], v250 offset:17416\n"
    "ds_read_b64_tr_b16 v[214:215], v250 offset:21512\n"
    "v_cvt_pk_bf16_f32 v232, v216, v217\n"
    "v_cvt_pk_bf16_f32 v233, v218, v219\n"
    "v_cvt_pk_bf16_f32 v234, v220, v221\n"
    "v_cvt_pk_bf16_f32 v235, v222, v223\n"
    "v_mfma_f32_16x16x32_bf16 v[228:231], a[220:223], a[60:63], v[228:231]\n"
    "s_waitcnt lgkmcnt(10)\n"
    "ds_read_b64_tr_b16 a[248:249], v251 offset:17408\n"
    "ds_read_b64_tr_b16 a[250:251], v251 offset:21504\n"
    "v_mfma_f32_16x16x32_bf16 v[0:3], v[192:195], v[232:235], v[0:3]\n"
    "s_waitcnt lgkmcnt(10)\n"
    "ds_read_b64_tr_b16 a[252:253], v251 offset:17416\n"
    "ds_read_b64_tr_b16 a[254:255], v251 offset:21512\n"
    "v_mfma_f32_16x16x32_bf16 v[4:7], v[196:199], v[232:235], v[4:7]\n"
    "s_waitcnt lgkmcnt(10)\n"
    "v_add_f32 v242, v242, v216\n"
    "v_add_f32 v242, v242, v217\n"
    "v_mfma_f32_16x16x32_bf16 v[8:11], v[200:203], v[232:235], v[8:11]\n"
    "s_waitcnt lgkmcnt(8)\n"
    "v_add_f32 v242, v242, v218\n"
    "v_add_f32 v242, v242, v219\n"
    "v_mfma_f32_16x16x32_bf16 v[12:15], v[204:207], v[232:235], v[12:15]\n"
    "s_waitcnt lgkmcnt(6)\n"
    "v_add_f32 v242, v242, v220\n"
    "v_add_f32 v242, v242, v221\n"
    "v_mfma_f32_16x16x32_bf16 v[16:19], v[208:211], v[232:235], v[16:19]\n"
    "s_waitcnt lgkmcnt(4)\n"
    "v_add_f32 v242, v242, v222\n"
    "v_add_f32 v242, v242, v223\n"
    "v_mfma_f32_16x16x32_bf16 v[20:23], v[212:215], v[232:235], v[20:23]\n"
    "s_waitcnt lgkmcnt(2)\n"
    "v_exp_f32 v224, v224\n"
    "v_mfma_f32_16x16x32_bf16 v[24:27], a[248:251], v[232:235], v[24:27]\n"
    "s_waitcnt lgkmcnt(0)\n"
    "v_exp_f32 v225, v225\n"
    "v_mfma_f32_16x16x32_bf16 v[28:31], a[252:255], v[232:235], v[28:31]\n"
    "v_exp_f32 v226, v226\n"
    "v_mfma_f32_16x16x32_bf16 v[216:219], a[192:195], a[64:67], 0\n"
    "v_exp_f32 v227, v227\n"
    "v_mfma_f32_16x16x32_bf16 v[216:219], a[196:199], a[68:71], v[216:219]\n"
    "v_exp_f32 v228, v228\n"
    "v_mfma_f32_16x16x32_bf16 v[216:219], a[200:203], a[72:75], v[216:219]\n"
    "v_exp_f32 v229, v229\n"
    "v_mfma_f32_16x16x32_bf16 v[216:219], a[204:207], a[76:79], v[216:219]\n"
    "v_exp_f32 v230, v230\n"
    "v_mfma_f32_16x16x32_bf16 v[220:223], a[208:211], a[64:67], 0\n"
    "v_exp_f32 v231, v231\n"
    "v_mfma_f32_16x16x32_bf16 v[220:223], a[212:215], a[68:71], v[220:223]\n"
    "v_cvt_pk_bf16_f32 v236, v224, v225\n"
    "v_cvt_pk_bf16_f32 v237, v226, v227\n"
    "v_mfma_f32_16x16x32_bf16 v[220:223], a[216:219], a[72:75], v[220:223]\n"
    "v_cvt_pk_bf16_f32 v238, v228, v229\n"
    "v_cvt_pk_bf16_f32 v239, v230, v231\n"
    "v_mfma_f32_16x16x32_bf16 v[220:223], a[220:223], a[76:79], v[220:223]\n"
    "v_mov_b32 v253, v242\n"
    "v_add_f32 v243, v243, v224\n"
    "v_mfma_f32_16x16x32_bf16 v[32:35], v[192:195], v[236:239], v[32:35]\n"
    "v_permlane32_swap_b32 v242, v253\n"
    "v_add_f32 v242, v242, v253\n"
    "v_mfma_f32_16x16x32_bf16 v[36:39], v[196:199], v[236:239], v[36:39]\n"
    "v_mov_b32 v253, v242\n"
    "v_add_f32 v243, v243, v225\n"
    "v_mfma_f32_16x16x32_bf16 v[40:43], v[200:203], v[236:239], v[40:43]\n"
    "v_permlane16_swap_b32 v242, v253\n"
    "v_add_f32 v242, v242, v253\n"
    "v_rcp_f32 v242, v242\n"
    "v_mfma_f32_16x16x32_bf16 v[44:47], v[204:207], v[236:239], v[44:47]\n"
    "v_pk_mul_f32 v[0:1], v[0:1], v[242:243] op_sel_hi:[1,0]\n"
    "v_pk_mul_f32 v[2:3], v[2:3], v[242:243] op_sel_hi:[1,0]\n"
    "v_pk_mul_f32 v[4:5], v[4:5], v[242:243] op_sel_hi:[1,0]\n"
    "v_pk_mul_f32 v[6:7], v[6:7], v[242:243] op_sel_hi:[1,0]\n"
    "v_pk_mul_f32 v[8:9], v[8:9], v[242:243] op_sel_hi:[1,0]\n"
    "v_pk_mul_f32 v[10:11], v[10:11], v[242:243] op_sel_hi:[1,0]\n"
    "v_pk_mul_f32 v[12:13], v[12:13], v[242:243] op_sel_hi:[1,0]\n"
    "v_pk_mul_f32 v[14:15], v[14:15], v[242:243] op_sel_hi:[1,0]\n"
    "v_pk_mul_f32 v[16:17], v[16:17], v[242:243] op_sel_hi:[1,0]\n"
    "v_pk_mul_f32 v[18:19], v[18:19], v[242:243] op_sel_hi:[1,0]\n"
    "v_pk_mul_f32 v[20:21], v[20:21], v[242:243] op_sel_hi:[1,0]\n"
    "v_pk_mul_f32 v[22:23], v[22:23], v[242:243] op_sel_hi:[1,0]\n"
    "v_pk_mul_f32 v[24:25], v[24:25], v[242:243] op_sel_hi:[1,0]\n"
    "v_pk_mul_f32 v[26:27], v[26:27], v[242:243] op_sel_hi:[1,0]\n"
    "v_pk_mul_f32 v[28:29], v[28:29], v[242:243] op_sel_hi:[1,0]\n"
    "v_pk_mul_f32 v[30:31], v[30:31], v[242:243] op_sel_hi:[1,0]\n"
    "v_cvt_pk_bf16_f32 v0, v0, v1\n"
    "v_cvt_pk_bf16_f32 v1, v2, v3\n"
    "v_cvt_pk_bf16_f32 v2, v4, v5\n"
    "v_cvt_pk_bf16_f32 v3, v6, v7\n"
    "v_mfma_f32_16x16x32_bf16 v[48:51], v[208:211], v[236:239], v[48:51]\n"
    "buffer_store_dwordx4 v[0:3], v255, s[48:51], s59 offen offset:0 sc1\n"
    "v_cvt_pk_bf16_f32 v8, v8, v9\n"
    "v_cvt_pk_bf16_f32 v9, v10, v11\n"
    "v_cvt_pk_bf16_f32 v10, v12, v13\n"
    "v_cvt_pk_bf16_f32 v11, v14, v15\n"
    "v_mfma_f32_16x16x32_bf16 v[52:55], v[212:215], v[236:239], v[52:55]\n"
    "buffer_store_dwordx4 v[8:11], v255, s[48:51], s59 offen offset:64 sc1\n"
    "v_mfma_f32_16x16x32_bf16 v[56:59], a[248:251], v[236:239], v[56:59]\n"
    "v_add_f32 v243, v243, v226\n"
    "v_add_f32 v243, v243, v227\n"
    "v_mfma_f32_16x16x32_bf16 v[60:63], a[252:255], v[236:239], v[60:63]\n"
    "v_add_f32 v243, v243, v228\n"
    "v_add_f32 v243, v243, v229\n"
    "v_mfma_f32_16x16x32_bf16 v[224:227], a[192:195], a[80:83], 0\n"
    "v_add_f32 v243, v243, v230\n"
    "v_add_f32 v243, v243, v231\n"
    "v_mfma_f32_16x16x32_bf16 v[224:227], a[196:199], a[84:87], v[224:227]\n"
    "v_exp_f32 v216, v216\n"
    "v_mfma_f32_16x16x32_bf16 v[224:227], a[200:203], a[88:91], v[224:227]\n"
    "v_exp_f32 v217, v217\n"
    "v_mfma_f32_16x16x32_bf16 v[224:227], a[204:207], a[92:95], v[224:227]\n"
    "v_exp_f32 v218, v218\n"
    "v_mfma_f32_16x16x32_bf16 v[228:231], a[208:211], a[80:83], 0\n"
    "v_exp_f32 v219, v219\n"
    "v_mfma_f32_16x16x32_bf16 v[228:231], a[212:215], a[84:87], v[228:231]\n"
    "v_exp_f32 v220, v220\n"
    "v_exp_f32 v221, v221\n"
    "v_exp_f32 v222, v222\n"
    "v_exp_f32 v223, v223\n"
    "v_mfma_f32_16x16x32_bf16 v[228:231], a[216:219], a[88:91], v[228:231]\n"
    "v_cvt_pk_bf16_f32 v232, v216, v217\n"
    "v_cvt_pk_bf16_f32 v233, v218, v219\n"
    "v_cvt_pk_bf16_f32 v234, v220, v221\n"
    "v_cvt_pk_bf16_f32 v235, v222, v223\n"
    "v_mfma_f32_16x16x32_bf16 v[228:231], a[220:223], a[92:95], v[228:231]\n"
    "v_add_f32 v244, v244, v216\n"
    "v_add_f32 v244, v244, v217\n"
    "v_mfma_f32_16x16x32_bf16 v[64:67], v[192:195], v[232:235], v[64:67]\n"
    "v_add_f32 v244, v244, v218\n"
    "v_add_f32 v244, v244, v219\n"
    "v_mfma_f32_16x16x32_bf16 v[68:71], v[196:199], v[232:235], v[68:71]\n"
    "v_cvt_pk_bf16_f32 v16, v16, v17\n"
    "v_cvt_pk_bf16_f32 v17, v18, v19\n"
    "v_mfma_f32_16x16x32_bf16 v[72:75], v[200:203], v[232:235], v[72:75]\n"
    "v_cvt_pk_bf16_f32 v18, v20, v21\n"
    "v_cvt_pk_bf16_f32 v19, v22, v23\n"
    "v_mfma_f32_16x16x32_bf16 v[76:79], v[204:207], v[232:235], v[76:79]\n"
    "v_add_f32 v244, v244, v220\n"
    "v_add_f32 v244, v244, v221\n"
    "v_mfma_f32_16x16x32_bf16 v[80:83], v[208:211], v[232:235], v[80:83]\n"
    "v_add_f32 v244, v244, v222\n"
    "v_add_f32 v244, v244, v223\n"
    "v_mfma_f32_16x16x32_bf16 v[84:87], v[212:215], v[232:235], v[84:87]\n"
    "v_cvt_pk_bf16_f32 v24, v24, v25\n"
    "v_cvt_pk_bf16_f32 v25, v26, v27\n"
    "v_mfma_f32_16x16x32_bf16 v[88:91], a[248:251], v[232:235], v[88:91]\n"
    "v_cvt_pk_bf16_f32 v26, v28, v29\n"
    "v_cvt_pk_bf16_f32 v27, v30, v31\n"
    "v_mfma_f32_16x16x32_bf16 v[92:95], a[252:255], v[232:235], v[92:95]\n"
    "v_exp_f32 v224, v224\n"
    "v_mfma_f32_16x16x32_bf16 v[216:219], a[192:195], a[0:3], 0\n"
    "v_exp_f32 v225, v225\n"
    "v_mfma_f32_16x16x32_bf16 v[216:219], a[196:199], a[4:7], v[216:219]\n"
    "v_exp_f32 v226, v226\n"
    "v_mfma_f32_16x16x32_bf16 v[216:219], a[200:203], a[8:11], v[216:219]\n"
    "v_exp_f32 v227, v227\n"
    "v_mfma_f32_16x16x32_bf16 v[216:219], a[204:207], a[12:15], v[216:219]\n"
    "v_exp_f32 v228, v228\n"
    "v_mfma_f32_16x16x32_bf16 v[220:223], a[208:211], a[0:3], 0\n"
    "buffer_store_dwordx4 v[16:19], v255, s[48:51], s59 offen offset:128 sc1\n"
    "v_mfma_f32_16x16x32_bf16 v[220:223], a[212:215], a[4:7], v[220:223]\n"
    "buffer_store_dwordx4 v[24:27], v255, s[48:51], s59 offen offset:192 sc1\n"
    "v_exp_f32 v229, v229\n"
    "v_exp_f32 v230, v230\n"
    "v_exp_f32 v231, v231\n"
    "v_mfma_f32_16x16x32_bf16 v[220:223], a[216:219], a[8:11], v[220:223]\n"
    "s_add_u32 s59, s67, 65536\n"
    "v_cvt_pk_bf16_f32 v236, v224, v225\n"
    "v_cvt_pk_bf16_f32 v237, v226, v227\n"
    "v_cvt_pk_bf16_f32 v238, v228, v229\n"
    "v_cvt_pk_bf16_f32 v239, v230, v231\n"
    "v_mfma_f32_16x16x32_bf16 v[220:223], a[220:223], a[12:15], v[220:223]\n"
    "v_add_f32 v245, v245, v224\n"
    "v_add_f32 v245, v245, v225\n"
    "v_mfma_f32_16x16x32_bf16 v[96:99], v[192:195], v[236:239], v[96:99]\n"
    "v_add_f32 v245, v245, v226\n"
    "v_add_f32 v245, v245, v227\n"
    "v_mfma_f32_16x16x32_bf16 v[100:103], v[196:199], v[236:239], v[100:103]\n"
    "v_add_f32 v245, v245, v228\n"
    "v_add_f32 v245, v245, v229\n"
    "v_mfma_f32_16x16x32_bf16 v[104:107], v[200:203], v[236:239], v[104:107]\n"
    "v_add_f32 v245, v245, v230\n"
    "v_add_f32 v245, v245, v231\n"
    "v_mfma_f32_16x16x32_bf16 v[108:111], v[204:207], v[236:239], v[108:111]\n"
    "v_exp_f32 v216, v216\n"
    "v_mfma_f32_16x16x32_bf16 v[112:115], v[208:211], v[236:239], v[112:115]\n"
    "v_exp_f32 v217, v217\n"
    "v_mfma_f32_16x16x32_bf16 v[116:119], v[212:215], v[236:239], v[116:119]\n"
    "v_exp_f32 v218, v218\n"
    "v_mfma_f32_16x16x32_bf16 v[120:123], a[248:251], v[236:239], v[120:123]\n"
    "v_exp_f32 v219, v219\n"
    "v_mfma_f32_16x16x32_bf16 v[124:127], a[252:255], v[236:239], v[124:127]\n"
    "v_exp_f32 v220, v220\n"
    "v_mfma_f32_16x16x32_bf16 v[224:227], a[192:195], a[16:19], 0\n"
    "v_exp_f32 v221, v221\n"
    "v_mfma_f32_16x16x32_bf16 v[224:227], a[196:199], a[20:23], v[224:227]\n"
    "v_exp_f32 v222, v222\n"
    "v_mfma_f32_16x16x32_bf16 v[224:227], a[200:203], a[24:27], v[224:227]\n"
    "v_exp_f32 v223, v223\n"
    "v_mfma_f32_16x16x32_bf16 v[224:227], a[204:207], a[28:31], v[224:227]\n"
    "v_cvt_pk_bf16_f32 v232, v216, v217\n"
    "v_cvt_pk_bf16_f32 v233, v218, v219\n"
    "v_mfma_f32_16x16x32_bf16 v[228:231], a[208:211], a[16:19], 0\n"
    "v_cvt_pk_bf16_f32 v234, v220, v221\n"
    "v_cvt_pk_bf16_f32 v235, v222, v223\n"
    "v_mfma_f32_16x16x32_bf16 v[228:231], a[212:215], a[20:23], v[228:231]\n"
    "v_mov_b32 v254, v243\n"
    "v_add_f32 v240, v240, v216\n"
    "v_mfma_f32_16x16x32_bf16 v[228:231], a[216:219], a[24:27], v[228:231]\n"
    "v_permlane32_swap_b32 v243, v254\n"
    "v_add_f32 v243, v243, v254\n"
    "v_mfma_f32_16x16x32_bf16 v[228:231], a[220:223], a[28:31], v[228:231]\n"
    "v_mov_b32 v254, v243\n"
    "v_add_f32 v240, v240, v217\n"
    "v_mfma_f32_16x16x32_bf16 a[128:131], v[192:195], v[232:235], a[128:131]\n"
    "v_permlane16_swap_b32 v243, v254\n"
    "v_add_f32 v243, v243, v254\n"
    "v_mfma_f32_16x16x32_bf16 a[132:135], v[196:199], v[232:235], a[132:135]\n"
    "v_rcp_f32 v243, v243\n"
    "v_mfma_f32_16x16x32_bf16 a[136:139], v[200:203], v[232:235], a[136:139]\n"
    "v_pk_mul_f32 v[32:33], v[32:33], v[242:243] op_sel:[0,1] op_sel_hi:[1,1]\n"
    "v_pk_mul_f32 v[34:35], v[34:35], v[242:243] op_sel:[0,1] op_sel_hi:[1,1]\n"
    "v_pk_mul_f32 v[36:37], v[36:37], v[242:243] op_sel:[0,1] op_sel_hi:[1,1]\n"
    "v_pk_mul_f32 v[38:39], v[38:39], v[242:243] op_sel:[0,1] op_sel_hi:[1,1]\n"
    "v_pk_mul_f32 v[40:41], v[40:41], v[242:243] op_sel:[0,1] op_sel_hi:[1,1]\n"
    "v_pk_mul_f32 v[42:43], v[42:43], v[242:243] op_sel:[0,1] op_sel_hi:[1,1]\n"
    "v_pk_mul_f32 v[44:45], v[44:45], v[242:243] op_sel:[0,1] op_sel_hi:[1,1]\n"
    "v_pk_mul_f32 v[46:47], v[46:47], v[242:243] op_sel:[0,1] op_sel_hi:[1,1]\n"
    "v_pk_mul_f32 v[48:49], v[48:49], v[242:243] op_sel:[0,1] op_sel_hi:[1,1]\n"
    "v_pk_mul_f32 v[50:51], v[50:51], v[242:243] op_sel:[0,1] op_sel_hi:[1,1]\n"
    "v_pk_mul_f32 v[52:53], v[52:53], v[242:243] op_sel:[0,1] op_sel_hi:[1,1]\n"
    "v_pk_mul_f32 v[54:55], v[54:55], v[242:243] op_sel:[0,1] op_sel_hi:[1,1]\n"
    "v_pk_mul_f32 v[56:57], v[56:57], v[242:243] op_sel:[0,1] op_sel_hi:[1,1]\n"
    "v_pk_mul_f32 v[58:59], v[58:59], v[242:243] op_sel:[0,1] op_sel_hi:[1,1]\n"
    "v_pk_mul_f32 v[60:61], v[60:61], v[242:243] op_sel:[0,1] op_sel_hi:[1,1]\n"
    "v_pk_mul_f32 v[62:63], v[62:63], v[242:243] op_sel:[0,1] op_sel_hi:[1,1]\n"
    "v_cvt_pk_bf16_f32 v32, v32, v33\n"
    "v_mfma_f32_16x16x32_bf16 a[140:143], v[204:207], v[232:235], a[140:143]\n"
    "v_cvt_pk_bf16_f32 v33, v34, v35\n"
    "v_cvt_pk_bf16_f32 v34, v36, v37\n"
    "v_cvt_pk_bf16_f32 v35, v38, v39\n"
    "v_mfma_f32_16x16x32_bf16 a[144:147], v[208:211], v[232:235], a[144:147]\n"
    "buffer_store_dwordx4 v[32:35], v255, s[48:51], s60 offen offset:0 sc1\n"
    "v_cvt_pk_bf16_f32 v40, v40, v41\n"
    "v_cvt_pk_bf16_f32 v41, v42, v43\n"
    "v_cvt_pk_bf16_f32 v42, v44, v45\n"
    "v_cvt_pk_bf16_f32 v43, v46, v47\n"
    "v_mfma_f32_16x16x32_bf16 a[148:151], v[212:215], v[232:235], a[148:151]\n"
    "buffer_store_dwordx4 v[40:43], v255, s[48:51], s60 offen offset:64 sc1\n"
    "v_mfma_f32_16x16x32_bf16 a[152:155], a[248:251], v[232:235], a[152:155]\n"
    "v_add_f32 v240, v240, v218\n"
    "v_add_f32 v240, v240, v219\n"
    "v_mfma_f32_16x16x32_bf16 a[156:159], a[252:255], v[232:235], a[156:159]\n"
    "v_add_f32 v240, v240, v220\n"
    "v_add_f32 v240, v240, v221\n"
    "v_mfma_f32_16x16x32_bf16 v[216:219], a[192:195], a[96:99], 0\n"
    "v_add_f32 v240, v240, v222\n"
    "v_add_f32 v240, v240, v223\n"
    "v_mfma_f32_16x16x32_bf16 v[216:219], a[196:199], a[100:103], v[216:219]\n"
    "v_exp_f32 v224, v224\n"
    "v_mfma_f32_16x16x32_bf16 v[216:219], a[200:203], a[104:107], v[216:219]\n"
    "v_exp_f32 v225, v225\n"
    "v_mfma_f32_16x16x32_bf16 v[216:219], a[204:207], a[108:111], v[216:219]\n"
    "v_exp_f32 v226, v226\n"
    "v_mfma_f32_16x16x32_bf16 v[220:223], a[208:211], a[96:99], 0\n"
    "v_exp_f32 v227, v227\n"
    "v_mfma_f32_16x16x32_bf16 v[220:223], a[212:215], a[100:103], v[220:223]\n"
    "v_exp_f32 v228, v228\n"
    "v_exp_f32 v229, v229\n"
    "v_exp_f32 v230, v230\n"
    "v_exp_f32 v231, v231\n"
    "v_mfma_f32_16x16x32_bf16 v[220:223], a[216:219], a[104:107], v[220:223]\n"
    "v_cvt_pk_bf16_f32 v236, v224, v225\n"
    "v_cvt_pk_bf16_f32 v237, v226, v227\n"
    "v_cvt_pk_bf16_f32 v238, v228, v229\n"
    "v_cvt_pk_bf16_f32 v239, v230, v231\n"
    "v_mfma_f32_16x16x32_bf16 v[220:223], a[220:223], a[108:111], v[220:223]\n"
    "v_add_f32 v241, v241, v224\n"
    "v_add_f32 v241, v241, v225\n"
    "v_mfma_f32_16x16x32_bf16 a[160:163], v[192:195], v[236:239], a[160:163]\n"
    "v_add_f32 v241, v241, v226\n"
    "v_add_f32 v241, v241, v227\n"
    "v_mfma_f32_16x16x32_bf16 a[164:167], v[196:199], v[236:239], a[164:167]\n"
    "v_cvt_pk_bf16_f32 v48, v48, v49\n"
    "v_cvt_pk_bf16_f32 v49, v50, v51\n"
    "v_mfma_f32_16x16x32_bf16 a[168:171], v[200:203], v[236:239], a[168:171]\n"
    "v_cvt_pk_bf16_f32 v50, v52, v53\n"
    "v_cvt_pk_bf16_f32 v51, v54, v55\n"
    "v_mfma_f32_16x16x32_bf16 a[172:175], v[204:207], v[236:239], a[172:175]\n"
    "v_add_f32 v241, v241, v228\n"
    "v_add_f32 v241, v241, v229\n"
    "v_mfma_f32_16x16x32_bf16 a[176:179], v[208:211], v[236:239], a[176:179]\n"
    "v_add_f32 v241, v241, v230\n"
    "v_add_f32 v241, v241, v231\n"
    "v_mfma_f32_16x16x32_bf16 a[180:183], v[212:215], v[236:239], a[180:183]\n"
    "v_cvt_pk_bf16_f32 v56, v56, v57\n"
    "v_cvt_pk_bf16_f32 v57, v58, v59\n"
    "v_mfma_f32_16x16x32_bf16 a[184:187], a[248:251], v[236:239], a[184:187]\n"
    "v_cvt_pk_bf16_f32 v58, v60, v61\n"
    "v_cvt_pk_bf16_f32 v59, v62, v63\n"
    "v_mfma_f32_16x16x32_bf16 a[188:191], a[252:255], v[236:239], a[188:191]\n"
    "v_exp_f32 v216, v216\n"
    "v_mfma_f32_16x16x32_bf16 v[224:227], a[192:195], a[112:115], 0\n"
    "v_exp_f32 v217, v217\n"
    "v_mfma_f32_16x16x32_bf16 v[224:227], a[196:199], a[116:119], v[224:227]\n"
    "v_exp_f32 v218, v218\n"
    "v_mfma_f32_16x16x32_bf16 v[224:227], a[200:203], a[120:123], v[224:227]\n"
    "v_exp_f32 v219, v219\n"
    "v_mfma_f32_16x16x32_bf16 v[224:227], a[204:207], a[124:127], v[224:227]\n"
    "v_exp_f32 v220, v220\n"
    "v_mfma_f32_16x16x32_bf16 v[228:231], a[208:211], a[112:115], 0\n"
    "buffer_store_dwordx4 v[48:51], v255, s[48:51], s60 offen offset:128 sc1\n"
    "v_mfma_f32_16x16x32_bf16 v[228:231], a[212:215], a[116:119], v[228:231]\n"
    "buffer_store_dwordx4 v[56:59], v255, s[48:51], s60 offen offset:192 sc1\n"
    "v_exp_f32 v221, v221\n"
    "v_exp_f32 v222, v222\n"
    "v_exp_f32 v223, v223\n"
    "v_mfma_f32_16x16x32_bf16 v[228:231], a[216:219], a[120:123], v[228:231]\n"
    "s_add_u32 s60, s67, 81920\n"
    "v_cvt_pk_bf16_f32 v232, v216, v217\n"
    "v_cvt_pk_bf16_f32 v233, v218, v219\n"
    "v_cvt_pk_bf16_f32 v234, v220, v221\n"
    "v_cvt_pk_bf16_f32 v235, v222, v223\n"
    "v_mfma_f32_16x16x32_bf16 v[228:231], a[220:223], a[124:127], v[228:231]\n"
    "ds_read_b128 a[192:195], v248 offset:0\n"
    "ds_read_b128 a[196:199], v249 offset:0\n"
    "v_mfma_f32_16x16x32_bf16 v[128:131], v[192:195], v[232:235], v[128:131]\n"
    "ds_read_b128 a[200:203], v248 offset:1024\n"
    "ds_read_b128 a[204:207], v249 offset:1024\n"
    "v_mfma_f32_16x16x32_bf16 v[132:135], v[196:199], v[232:235], v[132:135]\n"
    "ds_read_b128 a[208:211], v248 offset:4096\n"
    "ds_read_b128 a[212:215], v249 offset:4096\n"
    "v_mfma_f32_16x16x32_bf16 v[136:139], v[200:203], v[232:235], v[136:139]\n"
    "v_add_f32 v246, v246, v216\n"
    "v_add_f32 v246, v246, v217\n"
    "v_mfma_f32_16x16x32_bf16 v[140:143], v[204:207], v[232:235], v[140:143]\n"
    "v_add_f32 v246, v246, v218\n"
    "v_add_f32 v246, v246, v219\n"
    "v_mfma_f32_16x16x32_bf16 v[144:147], v[208:211], v[232:235], v[144:147]\n"
    "ds_read_b128 a[216:219], v248 offset:5120\n"
    "ds_read_b128 a[220:223], v249 offset:5120\n"
    "v_mfma_f32_16x16x32_bf16 v[148:151], v[212:215], v[232:235], v[148:151]\n"
    "v_add_f32 v246, v246, v220\n"
    "v_add_f32 v246, v246, v221\n"
    "v_mfma_f32_16x16x32_bf16 v[152:155], a[248:251], v[232:235], v[152:155]\n"
    "v_add_f32 v246, v246, v222\n"
    "v_add_f32 v246, v246, v223\n"
    "v_mfma_f32_16x16x32_bf16 v[156:159], a[252:255], v[232:235], v[156:159]\n"
    "s_waitcnt lgkmcnt(7)\n"
    "v_exp_f32 v224, v224\n"
    "v_mfma_f32_16x16x32_bf16 v[216:219], a[192:195], a[0:3], 0\n"
    "s_waitcnt lgkmcnt(6)\n"
    "v_exp_f32 v225, v225\n"
    "v_mfma_f32_16x16x32_bf16 v[216:219], a[196:199], a[4:7], v[216:219]\n"
    "s_waitcnt lgkmcnt(5)\n"
    "v_exp_f32 v226, v226\n"
    "v_mfma_f32_16x16x32_bf16 v[216:219], a[200:203], a[8:11], v[216:219]\n"
    "s_waitcnt lgkmcnt(4)\n"
    "v_exp_f32 v227, v227\n"
    "v_mfma_f32_16x16x32_bf16 v[216:219], a[204:207], a[12:15], v[216:219]\n"
    "s_waitcnt lgkmcnt(3)\n"
    "v_exp_f32 v228, v228\n"
    "v_mfma_f32_16x16x32_bf16 v[220:223], a[208:211], a[0:3], 0\n"
    "s_waitcnt lgkmcnt(2)\n"
    "v_exp_f32 v229, v229\n"
    "v_mfma_f32_16x16x32_bf16 v[220:223], a[212:215], a[4:7], v[220:223]\n"
    "s_waitcnt lgkmcnt(1)\n"
    "v_exp_f32 v230, v230\n"
    "v_exp_f32 v231, v231\n"
    "v_mfma_f32_16x16x32_bf16 v[220:223], a[216:219], a[8:11], v[220:223]\n"
    "s_waitcnt lgkmcnt(0)\n"
    "v_cvt_pk_bf16_f32 v236, v224, v225\n"
    "v_cvt_pk_bf16_f32 v237, v226, v227\n"
    "v_cvt_pk_bf16_f32 v238, v228, v229\n"
    "v_cvt_pk_bf16_f32 v239, v230, v231\n"
    "v_mfma_f32_16x16x32_bf16 v[220:223], a[220:223], a[12:15], v[220:223]\n"
    "v_mov_b32 v253, v244\n"
    "v_add_f32 v247, v247, v224\n"
    "v_mfma_f32_16x16x32_bf16 v[160:163], v[192:195], v[236:239], v[160:163]\n"
    "v_permlane32_swap_b32 v244, v253\n"
    "v_add_f32 v244, v244, v253\n"
    "v_mfma_f32_16x16x32_bf16 v[164:167], v[196:199], v[236:239], v[164:167]\n"
    "v_mov_b32 v253, v244\n"
    "v_add_f32 v247, v247, v225\n"
    "v_mfma_f32_16x16x32_bf16 v[168:171], v[200:203], v[236:239], v[168:171]\n"
    "v_permlane16_swap_b32 v244, v253\n"
    "v_add_f32 v244, v244, v253\n"
    "v_rcp_f32 v244, v244\n"
    "v_mfma_f32_16x16x32_bf16 v[172:175], v[204:207], v[236:239], v[172:175]\n"
    "v_pk_mul_f32 v[64:65], v[64:65], v[244:245] op_sel_hi:[1,0]\n"
    "v_pk_mul_f32 v[66:67], v[66:67], v[244:245] op_sel_hi:[1,0]\n"
    "v_pk_mul_f32 v[68:69], v[68:69], v[244:245] op_sel_hi:[1,0]\n"
    "v_pk_mul_f32 v[70:71], v[70:71], v[244:245] op_sel_hi:[1,0]\n"
    "v_pk_mul_f32 v[72:73], v[72:73], v[244:245] op_sel_hi:[1,0]\n"
    "v_pk_mul_f32 v[74:75], v[74:75], v[244:245] op_sel_hi:[1,0]\n"
    "v_pk_mul_f32 v[76:77], v[76:77], v[244:245] op_sel_hi:[1,0]\n"
    "v_pk_mul_f32 v[78:79], v[78:79], v[244:245] op_sel_hi:[1,0]\n"
    "v_pk_mul_f32 v[80:81], v[80:81], v[244:245] op_sel_hi:[1,0]\n"
    "v_pk_mul_f32 v[82:83], v[82:83], v[244:245] op_sel_hi:[1,0]\n"
    "v_pk_mul_f32 v[84:85], v[84:85], v[244:245] op_sel_hi:[1,0]\n"
    "v_pk_mul_f32 v[86:87], v[86:87], v[244:245] op_sel_hi:[1,0]\n"
    "v_pk_mul_f32 v[88:89], v[88:89], v[244:245] op_sel_hi:[1,0]\n"
    "v_pk_mul_f32 v[90:91], v[90:91], v[244:245] op_sel_hi:[1,0]\n"
    "v_pk_mul_f32 v[92:93], v[92:93], v[244:245] op_sel_hi:[1,0]\n"
    "v_pk_mul_f32 v[94:95], v[94:95], v[244:245] op_sel_hi:[1,0]\n"
    "v_cvt_pk_bf16_f32 v64, v64, v65\n"
    "v_cvt_pk_bf16_f32 v65, v66, v67\n"
    "v_cvt_pk_bf16_f32 v66, v68, v69\n"
    "v_cvt_pk_bf16_f32 v67, v70, v71\n"
    "v_mfma_f32_16x16x32_bf16 v[176:179], v[208:211], v[236:239], v[176:179]\n"
    "buffer_store_dwordx4 v[64:67], v255, s[48:51], s59 offen offset:0 sc1\n"
    "v_cvt_pk_bf16_f32 v72, v72, v73\n"
    "v_cvt_pk_bf16_f32 v73, v74, v75\n"
    "v_cvt_pk_bf16_f32 v74, v76, v77\n"
    "v_cvt_pk_bf16_f32 v75, v78, v79\n"
    "v_mfma_f32_16x16x32_bf16 v[180:183], v[212:215], v[236:239], v[180:183]\n"
    "buffer_store_dwordx4 v[72:75], v255, s[48:51], s59 offen offset:64 sc1\n"
    "v_mfma_f32_16x16x32_bf16 v[184:187], a[248:251], v[236:239], v[184:187]\n"
    "v_add_f32 v247, v247, v226\n"
    "v_add_f32 v247, v247, v227\n"
    "v_mfma_f32_16x16x32_bf16 v[188:191], a[252:255], v[236:239], v[188:191]\n"
    "ds_read_b64_tr_b16 a[224:225], v250 offset:0\n"
    "ds_read_b64_tr_b16 a[226:227], v250 offset:4096\n"
    "v_mfma_f32_16x16x32_bf16 v[224:227], a[192:195], a[16:19], 0\n"
    "ds_read_b64_tr_b16 a[228:229], v250 offset:8\n"
    "ds_read_b64_tr_b16 a[230:231], v250 offset:4104\n"
    "v_mfma_f32_16x16x32_bf16 v[224:227], a[196:199], a[20:23], v[224:227]\n"
    "ds_read_b64_tr_b16 a[232:233], v251 offset:0\n"
    "ds_read_b64_tr_b16 a[234:235], v251 offset:4096\n"
    "v_mfma_f32_16x16x32_bf16 v[224:227], a[200:203], a[24:27], v[224:227]\n"
    "v_add_f32 v247, v247, v228\n"
    "v_add_f32 v247, v247, v229\n"
    "v_mfma_f32_16x16x32_bf16 v[224:227], a[204:207], a[28:31], v[224:227]\n"
    "v_add_f32 v247, v247, v230\n"
    "v_add_f32 v247, v247, v231\n"
    "v_mfma_f32_16x16x32_bf16 v[228:231], a[208:211], a[16:19], 0\n"
    "ds_read_b64_tr_b16 a[236:237], v251 offset:8\n"
    "ds_read_b64_tr_b16 a[238:239], v251 offset:4104\n"
    "v_mfma_f32_16x16x32_bf16 v[228:231], a[212:215], a[20:23], v[228:231]\n"
    "ds_read_b64_tr_b16 a[240:241], v250 offset:1024\n"
    "ds_read_b64_tr_b16 a[242:243], v250 offset:5120\n"
    "v_exp_f32 v216, v216\n"
    "v_exp_f32 v217, v217\n"
    "v_exp_f32 v218, v218\n"
    "v_exp_f32 v219, v219\n"
    "v_exp_f32 v220, v220\n"
    "v_exp_f32 v221, v221\n"
    "v_exp_f32 v222, v222\n"
    "v_exp_f32 v223, v223\n"
    "v_mfma_f32_16x16x32_bf16 v[228:231], a[216:219], a[24:27], v[228:231]\n"
    "ds_read_b64_tr_b16 a[244:245], v250 offset:1032\n"
    "ds_read_b64_tr_b16 a[246:247], v250 offset:5128\n"
    "v_cvt_pk_bf16_f32 v232, v216, v217\n"
    "v_cvt_pk_bf16_f32 v233, v218, v219\n"
    "v_cvt_pk_bf16_f32 v234, v220, v221\n"
    "v_cvt_pk_bf16_f32 v235, v222, v223\n"
    "v_mfma_f32_16x16x32_bf16 v[228:231], a[220:223], a[28:31], v[228:231]\n"
    "s_waitcnt lgkmcnt(10)\n"
    "ds_read_b64_tr_b16 a[248:249], v251 offset:1024\n"
    "ds_read_b64_tr_b16 a[250:251], v251 offset:5120\n"
    "v_mfma_f32_16x16x32_bf16 a[128:131], a[224:227], v[232:235], a[128:131]\n"
    "s_waitcnt lgkmcnt(10)\n"
    "ds_read_b64_tr_b16 a[252:253], v251 offset:1032\n"
    "ds_read_b64_tr_b16 a[254:255], v251 offset:5128\n"
    "v_mfma_f32_16x16x32_bf16 a[132:135], a[228:231], v[232:235], a[132:135]\n"
    "s_waitcnt lgkmcnt(10)\n"
    "v_add_f32 v240, v240, v216\n"
    "v_add_f32 v240, v240, v217\n"
    "v_mfma_f32_16x16x32_bf16 a[136:139], a[232:235], v[232:235], a[136:139]\n"
    "s_waitcnt lgkmcnt(8)\n"
    "v_add_f32 v240, v240, v218\n"
    "v_add_f32 v240, v240, v219\n"
    "v_mfma_f32_16x16x32_bf16 a[140:143], a[236:239], v[232:235], a[140:143]\n"
    "s_waitcnt lgkmcnt(6)\n"
    "v_cvt_pk_bf16_f32 v80, v80, v81\n"
    "v_cvt_pk_bf16_f32 v81, v82, v83\n"
    "v_mfma_f32_16x16x32_bf16 a[144:147], a[240:243], v[232:235], a[144:147]\n"
    "s_waitcnt lgkmcnt(4)\n"
    "v_cvt_pk_bf16_f32 v82, v84, v85\n"
    "v_cvt_pk_bf16_f32 v83, v86, v87\n"
    "v_mfma_f32_16x16x32_bf16 a[148:151], a[244:247], v[232:235], a[148:151]\n"
    "s_waitcnt lgkmcnt(2)\n"
    "v_add_f32 v240, v240, v220\n"
    "v_add_f32 v240, v240, v221\n"
    "v_mfma_f32_16x16x32_bf16 a[152:155], a[248:251], v[232:235], a[152:155]\n"
    "s_waitcnt lgkmcnt(0)\n"
    "v_add_f32 v240, v240, v222\n"
    "v_add_f32 v240, v240, v223\n"
    "v_mfma_f32_16x16x32_bf16 a[156:159], a[252:255], v[232:235], a[156:159]\n"
    "v_cvt_pk_bf16_f32 v88, v88, v89\n"
    "v_cvt_pk_bf16_f32 v89, v90, v91\n"
    "v_mfma_f32_16x16x32_bf16 v[216:219], a[192:195], a[96:99], 0\n"
    "v_cvt_pk_bf16_f32 v90, v92, v93\n"
    "v_cvt_pk_bf16_f32 v91, v94, v95\n"
    "v_mfma_f32_16x16x32_bf16 v[216:219], a[196:199], a[100:103], v[216:219]\n"
    "v_exp_f32 v224, v224\n"
    "v_mfma_f32_16x16x32_bf16 v[216:219], a[200:203], a[104:107], v[216:219]\n"
    "v_exp_f32 v225, v225\n"
    "v_mfma_f32_16x16x32_bf16 v[216:219], a[204:207], a[108:111], v[216:219]\n"
    "v_exp_f32 v226, v226\n"
    "v_mfma_f32_16x16x32_bf16 v[220:223], a[208:211], a[96:99], 0\n"
    "buffer_store_dwordx4 v[80:83], v255, s[48:51], s59 offen offset:128 sc1\n"
    "v_mfma_f32_16x16x32_bf16 v[220:223], a[212:215], a[100:103], v[220:223]\n"
    "buffer_store_dwordx4 v[88:91], v255, s[48:51], s59 offen offset:192 sc1\n"
    "v_exp_f32 v227, v227\n"
    "v_exp_f32 v228, v228\n"
    "v_exp_f32 v229, v229\n"
    "v_exp_f32 v230, v230\n"
    "v_exp_f32 v231, v231\n"
    "v_mfma_f32_16x16x32_bf16 v[220:223], a[216:219], a[104:107], v[220:223]\n"
    "s_add_u32 s59, s67, 0\n"
    "v_cvt_pk_bf16_f32 v236, v224, v225\n"
    "v_cvt_pk_bf16_f32 v237, v226, v227\n"
    "v_cvt_pk_bf16_f32 v238, v228, v229\n"
    "v_cvt_pk_bf16_f32 v239, v230, v231\n"
    "v_mfma_f32_16x16x32_bf16 v[220:223], a[220:223], a[108:111], v[220:223]\n"
    "v_add_f32 v241, v241, v224\n"
    "v_add_f32 v241, v241, v225\n"
    "v_mfma_f32_16x16x32_bf16 a[160:163], a[224:227], v[236:239], a[160:163]\n"
    "v_add_f32 v241, v241, v226\n"
    "v_add_f32 v241, v241, v227\n"
    "v_mfma_f32_16x16x32_bf16 a[164:167], a[228:231], v[236:239], a[164:167]\n"
    "v_add_f32 v241, v241, v228\n"
    "v_add_f32 v241, v241, v229\n"
    "v_mfma_f32_16x16x32_bf16 a[168:171], a[232:235], v[236:239], a[168:171]\n"
    "v_add_f32 v241, v241, v230\n"
    "v_add_f32 v241, v241, v231\n"
    "v_mfma_f32_16x16x32_bf16 a[172:175], a[236:239], v[236:239], a[172:175]\n"
    "v_exp_f32 v216, v216\n"
    "v_mfma_f32_16x16x32_bf16 a[176:179], a[240:243], v[236:239], a[176:179]\n"
    "v_exp_f32 v217, v217\n"
    "v_mfma_f32_16x16x32_bf16 a[180:183], a[244:247], v[236:239], a[180:183]\n"
    "v_exp_f32 v218, v218\n"
    "v_mfma_f32_16x16x32_bf16 a[184:187], a[248:251], v[236:239], a[184:187]\n"
    "v_exp_f32 v219, v219\n"
    "v_mfma_f32_16x16x32_bf16 a[188:191], a[252:255], v[236:239], a[188:191]\n"
    "v_exp_f32 v220, v220\n"
    "v_mfma_f32_16x16x32_bf16 v[224:227], a[192:195], a[112:115], 0\n"
    "v_exp_f32 v221, v221\n"
    "v_mfma_f32_16x16x32_bf16 v[224:227], a[196:199], a[116:119], v[224:227]\n"
    "v_exp_f32 v222, v222\n"
    "v_mfma_f32_16x16x32_bf16 v[224:227], a[200:203], a[120:123], v[224:227]\n"
    "v_exp_f32 v223, v223\n"
    "v_mfma_f32_16x16x32_bf16 v[224:227], a[204:207], a[124:127], v[224:227]\n"
    "v_cvt_pk_bf16_f32 v232, v216, v217\n"
    "v_cvt_pk_bf16_f32 v233, v218, v219\n"
    "v_mfma_f32_16x16x32_bf16 v[228:231], a[208:211], a[112:115], 0\n"
    "v_cvt_pk_bf16_f32 v234, v220, v221\n"
    "v_cvt_pk_bf16_f32 v235, v222, v223\n"
    "v_mfma_f32_16x16x32_bf16 v[228:231], a[212:215], a[116:119], v[228:231]\n"
    "ds_read_b128 a[192:195], v248 offset:49152\n"
    "v_mov_b32 v254, v245\n"
    "v_mfma_f32_16x16x32_bf16 v[228:231], a[216:219], a[120:123], v[228:231]\n"
    "s_nop 0\n"
    "v_permlane32_swap_b32 v245, v254\n"
    "v_add_f32 v245, v245, v254\n"
    "v_mfma_f32_16x16x32_bf16 v[228:231], a[220:223], a[124:127], v[228:231]\n"
    "ds_read_b128 a[196:199], v249 offset:49152\n"
    "v_mov_b32 v254, v245\n"
    "v_mfma_f32_16x16x32_bf16 v[128:131], a[224:227], v[232:235], v[128:131]\n"
    "s_nop 0\n"
    "v_permlane16_swap_b32 v245, v254\n"
    "v_add_f32 v245, v245, v254\n"
    "v_mfma_f32_16x16x32_bf16 v[132:135], a[228:231], v[232:235], v[132:135]\n"
    "v_rcp_f32 v245, v245\n"
    "v_mfma_f32_16x16x32_bf16 v[136:139], a[232:235], v[232:235], v[136:139]\n"
    "v_pk_mul_f32 v[96:97], v[96:97], v[244:245] op_sel:[0,1] op_sel_hi:[1,1]\n"
    "v_pk_mul_f32 v[98:99], v[98:99], v[244:245] op_sel:[0,1] op_sel_hi:[1,1]\n"
    "v_pk_mul_f32 v[100:101], v[100:101], v[244:245] op_sel:[0,1] op_sel_hi:[1,1]\n"
    "v_pk_mul_f32 v[102:103], v[102:103], v[244:245] op_sel:[0,1] op_sel_hi:[1,1]\n"
    "v_pk_mul_f32 v[104:105], v[104:105], v[244:245] op_sel:[0,1] op_sel_hi:[1,1]\n"
    "v_pk_mul_f32 v[106:107], v[106:107], v[244:245] op_sel:[0,1] op_sel_hi:[1,1]\n"
    "v_pk_mul_f32 v[108:109], v[108:109], v[244:245] op_sel:[0,1] op_sel_hi:[1,1]\n"
    "v_pk_mul_f32 v[110:111], v[110:111], v[244:245] op_sel:[0,1] op_sel_hi:[1,1]\n"
    "v_pk_mul_f32 v[112:113], v[112:113], v[244:245] op_sel:[0,1] op_sel_hi:[1,1]\n"
    "v_pk_mul_f32 v[114:115], v[114:115], v[244:245] op_sel:[0,1] op_sel_hi:[1,1]\n"
    "v_pk_mul_f32 v[116:117], v[116:117], v[244:245] op_sel:[0,1] op_sel_hi:[1,1]\n"
    "v_pk_mul_f32 v[118:119], v[118:119], v[244:245] op_sel:[0,1] op_sel_hi:[1,1]\n"
    "v_pk_mul_f32 v[120:121], v[120:121], v[244:245] op_sel:[0,1] op_sel_hi:[1,1]\n"
    "v_pk_mul_f32 v[122:123], v[122:123], v[244:245] op_sel:[0,1] op_sel_hi:[1,1]\n"
    "v_pk_mul_f32 v[124:125], v[124:125], v[244:245] op_sel:[0,1] op_sel_hi:[1,1]\n"
    "v_pk_mul_f32 v[126:127], v[126:127], v[244:245] op_sel:[0,1] op_sel_hi:[1,1]\n"
    "v_cvt_pk_bf16_f32 v96, v96, v97\n"
    "v_mfma_f32_16x16x32_bf16 v[140:143], a[236:239], v[232:235], v[140:143]\n"
    "ds_read_b128 a[200:203], v248 offset:50176\n"
    "v_cvt_pk_bf16_f32 v97, v98, v99\n"
    "v_cvt_pk_bf16_f32 v98, v100, v101\n"
    "v_cvt_pk_bf16_f32 v99, v102, v103\n"
    "v_mfma_f32_16x16x32_bf16 v[144:147], a[240:243], v[232:235], v[144:147]\n"
    "buffer_store_dwordx4 v[96:99], v255, s[48:51], s60 offen offset:0 sc1\n"
    "ds_read_b128 a[204:207], v249 offset:50176\n"
    "v_cvt_pk_bf16_f32 v104, v104, v105\n"
    "v_cvt_pk_bf16_f32 v105, v106, v107\n"
    "v_cvt_pk_bf16_f32 v106, v108, v109\n"
    "v_cvt_pk_bf16_f32 v107, v110, v111\n"
    "v_mfma_f32_16x16x32_bf16 v[148:151], a[244:247], v[232:235], v[148:151]\n"
    "buffer_store_dwordx4 v[104:107], v255, s[48:51], s60 offen offset:64 sc1\n"
    "ds_read_b128 a[208:211], v248 offset:53248\n"
    "v_mfma_f32_16x16x32_bf16 v[152:155], a[248:251], v[232:235], v[152:155]\n"
    "ds_read_b128 a[212:215], v249 offset:53248\n"
    "v_add_f32 v246, v246, v216\n"
    "v_mfma_f32_16x16x32_bf16 v[156:159], a[252:255], v[232:235], v[156:159]\n"
    "s_waitcnt lgkmcnt(5)\n"
    "ds_read_b128 a[216:219], v248 offset:54272\n"
    "v_add_f32 v246, v246, v217\n"
    "v_add_f32 v246, v246, v218\n"
    "v_add_f32 v246, v246, v219\n"
    "v_mfma_f32_16x16x32_bf16 v[216:219], a[192:195], a[0:3], 0\n"
    "s_waitcnt lgkmcnt(5)\n"
    "ds_read_b128 a[220:223], v249 offset:54272\n"
    "v_add_f32 v246, v246, v220\n"
    "v_mfma_f32_16x16x32_bf16 v[216:219], a[196:199], a[4:7], v[216:219]\n"
    "s_waitcnt lgkmcnt(5)\n"
    "v_add_f32 v246, v246, v221\n"
    "v_add_f32 v246, v246, v222\n"
    "v_mfma_f32_16x16x32_bf16 v[216:219], a[200:203], a[8:11], v[216:219]\n"
    "s_waitcnt lgkmcnt(4)\n"
    "ds_read_b64_tr_b16 v[192:193], v250 offset:49152\n"
    "v_add_f32 v246, v246, v223\n"
    "v_mfma_f32_16x16x32_bf16 v[216:219], a[204:207], a[12:15], v[216:219]\n"
    "s_waitcnt lgkmcnt(4)\n"
    "v_exp_f32 v224, v224\n"
    "v_mfma_f32_16x16x32_bf16 v[220:223], a[208:211], a[0:3], 0\n"
    "s_waitcnt lgkmcnt(3)\n"
    "v_exp_f32 v225, v225\n"
    "v_mfma_f32_16x16x32_bf16 v[220:223], a[212:215], a[4:7], v[220:223]\n"
    "s_waitcnt lgkmcnt(2)\n"
    "v_exp_f32 v226, v226\n"
    "v_exp_f32 v227, v227\n"
    "v_exp_f32 v228, v228\n"
    "v_exp_f32 v229, v229\n"
    "v_exp_f32 v230, v230\n"
    "v_exp_f32 v231, v231\n"
    "v_mfma_f32_16x16x32_bf16 v[220:223], a[216:219], a[8:11], v[220:223]\n"
    "s_waitcnt lgkmcnt(1)\n"
    "v_cvt_pk_bf16_f32 v236, v224, v225\n"
    "v_cvt_pk_bf16_f32 v237, v226, v227\n"
    "v_cvt_pk_bf16_f32 v238, v228, v229\n"
    "v_cvt_pk_bf16_f32 v239, v230, v231\n"
    "v_mfma_f32_16x16x32_bf16 v[220:223], a[220:223], a[12:15], v[220:223]\n"
    "v_add_f32 v247, v247, v224\n"
    "v_add_f32 v247, v247, v225\n"
    "v_mfma_f32_16x16x32_bf16 v[160:163], a[224:227], v[236:239], v[160:163]\n"
    "v_add_f32 v247, v247, v226\n"
    "v_add_f32 v247, v247, v227\n"
    "v_mfma_f32_16x16x32_bf16 v[164:167], a[228:231], v[236:239], v[164:167]\n"
    "ds_read_b64_tr_b16 v[194:195], v250 offset:53248\n"
    "ds_read_b64_tr_b16 v[196:197], v250 offset:49160\n"
    "v_mfma_f32_16x16x32_bf16 v[168:171], a[232:235], v[236:239], v[168:171]\n"
    "ds_read_b64_tr_b16 v[198:199], v250 offset:53256\n"
    "v_cvt_pk_bf16_f32 v112, v112, v113\n"
    "v_mfma_f32_16x16x32_bf16 v[172:175], a[236:239], v[236:239], v[172:175]\n"
    "v_cvt_pk_bf16_f32 v113, v114, v115\n"
    "v_cvt_pk_bf16_f32 v114, v116, v117\n"
    "v_mfma_f32_16x16x32_bf16 v[176:179], a[240:243], v[236:239], v[176:179]\n"
    "ds_read_b64_tr_b16 v[200:201], v251 offset:49152\n"
    "v_cvt_pk_bf16_f32 v115, v118, v119\n"
    "v_mfma_f32_16x16x32_bf16 v[180:183], a[244:247], v[236:239], v[180:183]\n"
    "ds_read_b64_tr_b16 v[202:203], v251 offset:53248\n"
    "v_add_f32 v247, v247, v228\n"
    "v_mfma_f32_16x16x32_bf16 v[184:187], a[248:251], v[236:239], v[184:187]\n"
    "v_add_f32 v247, v247, v229\n"
    "v_add_f32 v247, v247, v230\n"
    "v_mfma_f32_16x16x32_bf16 v[188:191], a[252:255], v[236:239], v[188:191]\n"
    "v_add_f32 v247, v247, v231\n"
    "v_cvt_pk_bf16_f32 v120, v120, v121\n"
    "v_mfma_f32_16x16x32_bf16 v[224:227], a[192:195], a[16:19], 0\n"
    "v_cvt_pk_bf16_f32 v121, v122, v123\n"
    "v_cvt_pk_bf16_f32 v122, v124, v125\n"
    "v_mfma_f32_16x16x32_bf16 v[224:227], a[196:199], a[20:23], v[224:227]\n"
    "ds_read_b64_tr_b16 v[204:205], v251 offset:49160\n"
    "v_cvt_pk_bf16_f32 v123, v126, v127\n"
    "v_mfma_f32_16x16x32_bf16 v[224:227], a[200:203], a[24:27], v[224:227]\n"
    "ds_read_b64_tr_b16 v[206:207], v251 offset:53256\n"
    "ds_read_b64_tr_b16 v[208:209], v250 offset:50176\n"
    "v_mfma_f32_16x16x32_bf16 v[224:227], a[204:207], a[28:31], v[224:227]\n"
    "ds_read_b64_tr_b16 v[210:211], v250 offset:54272\n"
    "ds_read_b64_tr_b16 v[212:213], v250 offset:50184\n"
    "v_mfma_f32_16x16x32_bf16 v[228:231], a[208:211], a[16:19], 0\n"
    "buffer_store_dwordx4 v[112:115], v255, s[48:51], s60 offen offset:128 sc1\n"
    "v_mfma_f32_16x16x32_bf16 v[228:231], a[212:215], a[20:23], v[228:231]\n"
    "buffer_store_dwordx4 v[120:123], v255, s[48:51], s60 offen offset:192 sc1\n"
    "v_exp_f32 v216, v216\n"
    "v_exp_f32 v217, v217\n"
    "v_exp_f32 v218, v218\n"
    "v_exp_f32 v219, v219\n"
    "v_exp_f32 v220, v220\n"
    "v_exp_f32 v221, v221\n"
    "v_exp_f32 v222, v222\n"
    "v_exp_f32 v223, v223\n"
    "v_mfma_f32_16x16x32_bf16 v[228:231], a[216:219], a[24:27], v[228:231]\n"
    "s_add_u32 s60, s67, 16384\n"
    "ds_read_b64_tr_b16 v[214:215], v250 offset:54280\n"
    "v_cvt_pk_bf16_f32 v232, v216, v217\n"
    "v_cvt_pk_bf16_f32 v233, v218, v219\n"
    "v_cvt_pk_bf16_f32 v234, v220, v221\n"
    "v_cvt_pk_bf16_f32 v235, v222, v223\n"
    "v_mfma_f32_16x16x32_bf16 v[228:231], a[220:223], a[28:31], v[228:231]\n"
    "s_waitcnt lgkmcnt(10)\n"
    "ds_read_b64_tr_b16 a[248:249], v251 offset:50176\n"
    "ds_read_b64_tr_b16 a[250:251], v251 offset:54272\n"
    "v_mfma_f32_16x16x32_bf16 a[128:131], v[192:195], v[232:235], a[128:131]\n"
    "s_waitcnt lgkmcnt(10)\n"
    "ds_read_b64_tr_b16 a[252:253], v251 offset:50184\n"
    "ds_read_b64_tr_b16 a[254:255], v251 offset:54280\n"
    "v_mfma_f32_16x16x32_bf16 a[132:135], v[196:199], v[232:235], a[132:135]\n"
    "s_waitcnt lgkmcnt(10)\n"
    "ds_read_b128 a[192:195], v248 offset:32768\n"
    "ds_read_b128 a[196:199], v249 offset:32768\n"
    "v_mfma_f32_16x16x32_bf16 a[136:139], v[200:203], v[232:235], a[136:139]\n"
    "s_waitcnt lgkmcnt(10)\n"
    "ds_read_b128 a[200:203], v248 offset:33792\n"
    "ds_read_b128 a[204:207], v249 offset:33792\n"
    "v_mfma_f32_16x16x32_bf16 a[140:143], v[204:207], v[232:235], a[140:143]\n"
    "s_waitcnt lgkmcnt(10)\n"
    "ds_read_b128 a[208:211], v248 offset:36864\n"
    "ds_read_b128 a[212:215], v249 offset:36864\n"
    "v_mfma_f32_16x16x32_bf16 a[144:147], v[208:211], v[232:235], a[144:147]\n"
    "s_waitcnt lgkmcnt(10)\n"
    "v_add_f32 v240, v240, v216\n"
    "v_add_f32 v240, v240, v217\n"
    "v_mfma_f32_16x16x32_bf16 a[148:151], v[212:215], v[232:235], a[148:151]\n"
    "s_waitcnt lgkmcnt(8)\n"
    "v_add_f32 v240, v240, v218\n"
    "v_add_f32 v240, v240, v219\n"
    "v_mfma_f32_16x16x32_bf16 a[152:155], a[248:251], v[232:235], a[152:155]\n"
    "s_waitcnt lgkmcnt(6)\n"
    "ds_read_b128 a[216:219], v248 offset:37888\n"
    "ds_read_b128 a[220:223], v249 offset:37888\n"
    "v_mfma_f32_16x16x32_bf16 a[156:159], a[252:255], v[232:235], a[156:159]\n"
    "s_waitcnt lgkmcnt(7)\n"
    "v_add_f32 v240, v240, v220\n"
    "v_add_f32 v240, v240, v221\n"
    "v_mfma_f32_16x16x32_bf16 v[216:219], a[192:195], a[0:3], 0\n"
    "s_waitcnt lgkmcnt(6)\n"
    "v_add_f32 v240, v240, v222\n"
    "v_add_f32 v240, v240, v223\n"
    "v_mfma_f32_16x16x32_bf16 v[216:219], a[196:199], a[4:7], v[216:219]\n"
    "s_waitcnt lgkmcnt(5)\n"
    "v_exp_f32 v224, v224\n"
    "v_mfma_f32_16x16x32_bf16 v[216:219], a[200:203], a[8:11], v[216:219]\n"
    "s_waitcnt lgkmcnt(4)\n"
    "v_exp_f32 v225, v225\n"
    "v_mfma_f32_16x16x32_bf16 v[216:219], a[204:207], a[12:15], v[216:219]\n"
    "s_waitcnt lgkmcnt(3)\n"
    "v_exp_f32 v226, v226\n"
    "v_mfma_f32_16x16x32_bf16 v[220:223], a[208:211], a[0:3], 0\n"
    "s_waitcnt lgkmcnt(2)\n"
    "v_exp_f32 v227, v227\n"
    "v_mfma_f32_16x16x32_bf16 v[220:223], a[212:215], a[4:7], v[220:223]\n"
    "s_waitcnt lgkmcnt(1)\n"
    "v_exp_f32 v228, v228\n"
    "v_exp_f32 v229, v229\n"
    "v_exp_f32 v230, v230\n"
    "v_exp_f32 v231, v231\n"
    "v_mfma_f32_16x16x32_bf16 v[220:223], a[216:219], a[8:11], v[220:223]\n"
    "s_waitcnt lgkmcnt(0)\n"
    "v_cvt_pk_bf16_f32 v236, v224, v225\n"
    "v_cvt_pk_bf16_f32 v237, v226, v227\n"
    "v_cvt_pk_bf16_f32 v238, v228, v229\n"
    "v_cvt_pk_bf16_f32 v239, v230, v231\n"
    "v_mfma_f32_16x16x32_bf16 v[220:223], a[220:223], a[12:15], v[220:223]\n"
    "v_add_f32 v241, v241, v224\n"
    "v_add_f32 v241, v241, v225\n"
    "v_mfma_f32_16x16x32_bf16 a[160:163], v[192:195], v[236:239], a[160:163]\n"
    "v_add_f32 v241, v241, v226\n"
    "v_add_f32 v241, v241, v227\n"
    "v_mfma_f32_16x16x32_bf16 a[164:167], v[196:199], v[236:239], a[164:167]\n"
    "ds_read_b64_tr_b16 a[224:225], v250 offset:32768\n"
    "ds_read_b64_tr_b16 a[226:227], v250 offset:36864\n"
    "v_mfma_f32_16x16x32_bf16 a[168:171], v[200:203], v[236:239], a[168:171]\n"
    "ds_read_b64_tr_b16 a[228:229], v250 offset:32776\n"
    "ds_read_b64_tr_b16 a[230:231], v250 offset:36872\n"
    "v_mfma_f32_16x16x32_bf16 a[172:175], v[204:207], v[236:239], a[172:175]\n"
    "ds_read_b64_tr_b16 a[232:233], v251 offset:32768\n"
    "ds_read_b64_tr_b16 a[234:235], v251 offset:36864\n"
    "v_mfma_f32_16x16x32_bf16 a[176:179], v[208:211], v[236:239], a[176:179]\n"
    "v_add_f32 v241, v241, v228\n"
    "v_add_f32 v241, v241, v229\n"
    "v_mfma_f32_16x16x32_bf16 a[180:183], v[212:215], v[236:239], a[180:183]\n"
    "v_add_f32 v241, v241, v230\n"
    "v_add_f32 v241, v241, v231\n"
    "v_mfma_f32_16x16x32_bf16 a[184:187], a[248:251], v[236:239], a[184:187]\n"
    "ds_read_b64_tr_b16 a[236:237], v251 offset:32776\n"
    "ds_read_b64_tr_b16 a[238:239], v251 offset:36872\n"
    "v_mfma_f32_16x16x32_bf16 a[188:191], a[252:255], v[236:239], a[188:191]\n"
    "ds_read_b64_tr_b16 a[240:241], v250 offset:33792\n"
    "ds_read_b64_tr_b16 a[242:243], v250 offset:37888\n"
    "v_mfma_f32_16x16x32_bf16 v[224:227], a[192:195], a[16:19], 0\n"
    "v_exp_f32 v216, v216\n"
    "v_mfma_f32_16x16x32_bf16 v[224:227], a[196:199], a[20:23], v[224:227]\n"
    "v_exp_f32 v217, v217\n"
    "v_mfma_f32_16x16x32_bf16 v[224:227], a[200:203], a[24:27], v[224:227]\n"
    "v_exp_f32 v218, v218\n"
    "v_mfma_f32_16x16x32_bf16 v[224:227], a[204:207], a[28:31], v[224:227]\n"
    "v_exp_f32 v219, v219\n"
    "v_mfma_f32_16x16x32_bf16 v[228:231], a[208:211], a[16:19], 0\n"
    "v_exp_f32 v220, v220\n"
    "v_mfma_f32_16x16x32_bf16 v[228:231], a[212:215], a[20:23], v[228:231]\n"
    "v_exp_f32 v221, v221\n"
    "v_exp_f32 v222, v222\n"
    "v_exp_f32 v223, v223\n"
    "v_mfma_f32_16x16x32_bf16 v[228:231], a[216:219], a[24:27], v[228:231]\n"
    "ds_read_b64_tr_b16 a[244:245], v250 offset:33800\n"
    "ds_read_b64_tr_b16 a[246:247], v250 offset:37896\n"
    "v_cvt_pk_bf16_f32 v232, v216, v217\n"
    "v_cvt_pk_bf16_f32 v233, v218, v219\n"
    "v_cvt_pk_bf16_f32 v234, v220, v221\n"
    "v_cvt_pk_bf16_f32 v235, v222, v223\n"
    "v_mfma_f32_16x16x32_bf16 v[228:231], a[220:223], a[28:31], v[228:231]\n"
    "s_waitcnt lgkmcnt(10)\n"
    "ds_read_b64_tr_b16 a[248:249], v251 offset:33792\n"
    "ds_read_b64_tr_b16 a[250:251], v251 offset:37888\n"
    "v_mfma_f32_16x16x32_bf16 a[128:131], a[224:227], v[232:235], a[128:131]\n"
    "s_waitcnt lgkmcnt(10)\n"
    "ds_read_b64_tr_b16 a[252:253], v251 offset:33800\n"
    "ds_read_b64_tr_b16 a[254:255], v251 offset:37896\n"
    "v_mfma_f32_16x16x32_bf16 a[132:135], a[228:231], v[232:235], a[132:135]\n"
    "s_waitcnt lgkmcnt(10)\n"
    "v_add_f32 v240, v240, v216\n"
    "v_add_f32 v240, v240, v217\n"
    "v_mfma_f32_16x16x32_bf16 a[136:139], a[232:235], v[232:235], a[136:139]\n"
    "s_waitcnt lgkmcnt(8)\n"
    "v_add_f32 v240, v240, v218\n"
    "v_add_f32 v240, v240, v219\n"
    "v_mfma_f32_16x16x32_bf16 a[140:143], a[236:239], v[232:235], a[140:143]\n"
    "s_waitcnt lgkmcnt(6)\n"
    "v_add_f32 v240, v240, v220\n"
    "v_add_f32 v240, v240, v221\n"
    "v_mfma_f32_16x16x32_bf16 a[144:147], a[240:243], v[232:235], a[144:147]\n"
    "s_waitcnt lgkmcnt(4)\n"
    "v_add_f32 v240, v240, v222\n"
    "v_add_f32 v240, v240, v223\n"
    "v_mfma_f32_16x16x32_bf16 a[148:151], a[244:247], v[232:235], a[148:151]\n"
    "s_waitcnt lgkmcnt(2)\n"
    "v_exp_f32 v224, v224\n"
    "v_mfma_f32_16x16x32_bf16 a[152:155], a[248:251], v[232:235], a[152:155]\n"
    "s_waitcnt lgkmcnt(0)\n"
    "v_exp_f32 v225, v225\n"
    "v_mfma_f32_16x16x32_bf16 a[156:159], a[252:255], v[232:235], a[156:159]\n"
    "v_exp_f32 v226, v226\n"
    "v_mfma_f32_16x16x32_bf16 v[216:219], a[192:195], a[96:99], 0\n"
    "v_exp_f32 v227, v227\n"
    "v_mfma_f32_16x16x32_bf16 v[216:219], a[196:199], a[100:103], v[216:219]\n"
    "v_exp_f32 v228, v228\n"
    "v_mfma_f32_16x16x32_bf16 v[216:219], a[200:203], a[104:107], v[216:219]\n"
    "v_exp_f32 v229, v229\n"
    "v_mfma_f32_16x16x32_bf16 v[216:219], a[204:207], a[108:111], v[216:219]\n"
    "v_exp_f32 v230, v230\n"
    "v_mfma_f32_16x16x32_bf16 v[220:223], a[208:211], a[96:99], 0\n"
    "v_exp_f32 v231, v231\n"
    "v_mfma_f32_16x16x32_bf16 v[220:223], a[212:215], a[100:103], v[220:223]\n"
    "v_cvt_pk_bf16_f32 v236, v224, v225\n"
    "v_cvt_pk_bf16_f32 v237, v226, v227\n"
    "v_mfma_f32_16x16x32_bf16 v[220:223], a[216:219], a[104:107], v[220:223]\n"
    "v_cvt_pk_bf16_f32 v238, v228, v229\n"
    "v_cvt_pk_bf16_f32 v239, v230, v231\n"
    "v_mfma_f32_16x16x32_bf16 v[220:223], a[220:223], a[108:111], v[220:223]\n"
    "v_mov_b32 v253, v240\n"
    "v_accvgpr_read_b32 v68, a128\n"
    "v_mfma_f32_16x16x32_bf16 a[160:163], a[224:227], v[236:239], a[160:163]\n"
    "v_permlane32_swap_b32 v240, v253\n"
    "v_add_f32 v240, v240, v253\n"
    "v_mfma_f32_16x16x32_bf16 a[164:167], a[228:231], v[236:239], a[164:167]\n"
    "v_mov_b32 v253, v240\n"
    "v_accvgpr_read_b32 v69, a129\n"
    "v_mfma_f32_16x16x32_bf16 a[168:171], a[232:235], v[236:239], a[168:171]\n"
    "v_permlane16_swap_b32 v240, v253\n"
    "v_add_f32 v240, v240, v253\n"
    "v_rcp_f32 v240, v240\n"
    "v_mfma_f32_16x16x32_bf16 a[172:175], a[236:239], v[236:239], a[172:175]\n"
    "v_accvgpr_read_b32 v70, a130\n"
    "v_accvgpr_read_b32 v71, a131\n"
    "v_accvgpr_read_b32 v76, a132\n"
    "v_accvgpr_read_b32 v77, a133\n"
    "v_accvgpr_read_b32 v78, a134\n"
    "v_accvgpr_read_b32 v79, a135\n"
    "v_mul_f32 v68, v68, v240\n"
    "v_mul_f32 v69, v69, v240\n"
    "v_mul_f32 v70, v70, v240\n"
    "v_mul_f32 v71, v71, v240\n"
    "v_mul_f32 v76, v76, v240\n"
    "v_mul_f32 v77, v77, v240\n"
    "v_mul_f32 v78, v78, v240\n"
    "v_mul_f32 v79, v79, v240\n"
    "v_cvt_pk_bf16_f32 v4, v68, v69\n"
    "v_cvt_pk_bf16_f32 v5, v70, v71\n"
    "v_cvt_pk_bf16_f32 v6, v76, v77\n"
    "v_cvt_pk_bf16_f32 v7, v78, v79\n"
    "v_mfma_f32_16x16x32_bf16 a[176:179], a[240:243], v[236:239], a[176:179]\n"
    "buffer_store_dwordx4 v[4:7], v255, s[48:51], s59 offen offset:0 sc1\n"
    "v_accvgpr_read_b32 v84, a136\n"
    "v_accvgpr_read_b32 v85, a137\n"
    "v_accvgpr_read_b32 v86, a138\n"
    "v_accvgpr_read_b32 v87, a139\n"
    "v_accvgpr_read_b32 v92, a140\n"
    "v_accvgpr_read_b32 v93, a141\n"
    "v_accvgpr_read_b32 v94, a142\n"
    "v_accvgpr_read_b32 v95, a143\n"
    "v_mul_f32 v84, v84, v240\n"
    "v_mul_f32 v85, v85, v240\n"
    "v_mul_f32 v86, v86, v240\n"
    "v_mul_f32 v87, v87, v240\n"
    "v_mul_f32 v92, v92, v240\n"
    "v_mul_f32 v93, v93, v240\n"
    "v_mul_f32 v94, v94, v240\n"
    "v_mul_f32 v95, v95, v240\n"
    "v_cvt_pk_bf16_f32 v12, v84, v85\n"
    "v_cvt_pk_bf16_f32 v13, v86, v87\n"
    "v_cvt_pk_bf16_f32 v14, v92, v93\n"
    "v_cvt_pk_bf16_f32 v15, v94, v95\n"
    "v_mfma_f32_16x16x32_bf16 a[180:183], a[244:247], v[236:239], a[180:183]\n"
    "buffer_store_dwordx4 v[12:15], v255, s[48:51], s59 offen offset:64 sc1\n"
    "v_mfma_f32_16x16x32_bf16 a[184:187], a[248:251], v[236:239], a[184:187]\n"
    "v_add_f32 v241, v241, v224\n"
    "v_add_f32 v241, v241, v225\n"
    "v_mfma_f32_16x16x32_bf16 a[188:191], a[252:255], v[236:239], a[188:191]\n"
    "v_add_f32 v241, v241, v226\n"
    "v_add_f32 v241, v241, v227\n"
    "v_mfma_f32_16x16x32_bf16 v[224:227], a[192:195], a[112:115], 0\n"
    "v_add_f32 v241, v241, v228\n"
    "v_add_f32 v241, v241, v229\n"
    "v_mfma_f32_16x16x32_bf16 v[224:227], a[196:199], a[116:119], v[224:227]\n"
    "v_add_f32 v241, v241, v230\n"
    "v_add_f32 v241, v241, v231\n"
    "v_mfma_f32_16x16x32_bf16 v[224:227], a[200:203], a[120:123], v[224:227]\n"
    "v_accvgpr_read_b32 v68, a144\n"
    "v_accvgpr_read_b32 v69, a145\n"
    "v_mfma_f32_16x16x32_bf16 v[224:227], a[204:207], a[124:127], v[224:227]\n"
    "v_accvgpr_read_b32 v70, a146\n"
    "v_accvgpr_read_b32 v71, a147\n"
    "v_mfma_f32_16x16x32_bf16 v[228:231], a[208:211], a[112:115], 0\n"
    "v_accvgpr_read_b32 v76, a148\n"
    "v_accvgpr_read_b32 v77, a149\n"
    "v_mfma_f32_16x16x32_bf16 v[228:231], a[212:215], a[116:119], v[228:231]\n"
    "v_accvgpr_read_b32 v78, a150\n"
    "v_accvgpr_read_b32 v79, a151\n"
    "v_mul_f32 v68, v68, v240\n"
    "v_mul_f32 v69, v69, v240\n"
    "v_mul_f32 v70, v70, v240\n"
    "v_mul_f32 v71, v71, v240\n"
    "v_mul_f32 v76, v76, v240\n"
    "v_mul_f32 v77, v77, v240\n"
    "v_mul_f32 v78, v78, v240\n"
    "v_mul_f32 v79, v79, v240\n"
    "v_cvt_pk_bf16_f32 v20, v68, v69\n"
    "v_cvt_pk_bf16_f32 v21, v70, v71\n"
    "v_cvt_pk_bf16_f32 v22, v76, v77\n"
    "v_cvt_pk_bf16_f32 v23, v78, v79\n"
    "v_exp_f32 v216, v216\n"
    "v_exp_f32 v217, v217\n"
    "v_exp_f32 v218, v218\n"
    "v_exp_f32 v219, v219\n"
    "v_exp_f32 v220, v220\n"
    "v_exp_f32 v221, v221\n"
    "v_exp_f32 v222, v222\n"
    "v_exp_f32 v223, v223\n"
    "v_mfma_f32_16x16x32_bf16 v[228:231], a[216:219], a[120:123], v[228:231]\n"
    "buffer_store_dwordx4 v[20:23], v255, s[48:51], s59 offen offset:128 sc1\n"
    "v_accvgpr_read_b32 v84, a152\n"
    "v_accvgpr_read_b32 v85, a153\n"
    "v_accvgpr_read_b32 v86, a154\n"
    "v_accvgpr_read_b32 v87, a155\n"
    "v_accvgpr_read_b32 v92, a156\n"
    "v_accvgpr_read_b32 v93, a157\n"
    "v_accvgpr_read_b32 v94, a158\n"
    "v_accvgpr_read_b32 v95, a159\n"
    "v_mul_f32 v84, v84, v240\n"
    "v_mul_f32 v85, v85, v240\n"
    "v_mul_f32 v86, v86, v240\n"
    "v_mul_f32 v87, v87, v240\n"
    "v_mul_f32 v92, v92, v240\n"
    "v_mul_f32 v93, v93, v240\n"
    "v_mul_f32 v94, v94, v240\n"
    "v_mul_f32 v95, v95, v240\n"
    "v_cvt_pk_bf16_f32 v28, v84, v85\n"
    "v_cvt_pk_bf16_f32 v29, v86, v87\n"
    "v_cvt_pk_bf16_f32 v30, v92, v93\n"
    "v_cvt_pk_bf16_f32 v31, v94, v95\n"
    "v_cvt_pk_bf16_f32 v232, v216, v217\n"
    "v_cvt_pk_bf16_f32 v233, v218, v219\n"
    "v_cvt_pk_bf16_f32 v234, v220, v221\n"
    "v_cvt_pk_bf16_f32 v235, v222, v223\n"
    "v_mfma_f32_16x16x32_bf16 v[228:231], a[220:223], a[124:127], v[228:231]\n"
    "buffer_store_dwordx4 v[28:31], v255, s[48:51], s59 offen offset:192 sc1\n"
    "v_mfma_f32_16x16x32_bf16 v[128:131], a[224:227], v[232:235], v[128:131]\n"
    "s_add_u32 s59, s67, 98304\n"
    "ds_read_b128 a[192:195], v248 offset:49152\n"
    "ds_read_b128 a[196:199], v249 offset:49152\n"
    "v_mfma_f32_16x16x32_bf16 v[132:135], a[228:231], v[232:235], v[132:135]\n"
    "ds_read_b128 a[200:203], v248 offset:50176\n"
    "v_mov_b32 v254, v241\n"
    "v_mfma_f32_16x16x32_bf16 v[136:139], a[232:235], v[232:235], v[136:139]\n"
    "ds_read_b128 a[204:207], v249 offset:50176\n"
    "v_permlane32_swap_b32 v241, v254\n"
    "v_mfma_f32_16x16x32_bf16 v[140:143], a[236:239], v[232:235], v[140:143]\n"
    "v_add_f32 v241, v241, v254\n"
    "v_mov_b32 v254, v241\n"
    "v_mfma_f32_16x16x32_bf16 v[144:147], a[240:243], v[232:235], v[144:147]\n"
    "ds_read_b128 a[208:211], v248 offset:53248\n"
    "v_permlane16_swap_b32 v241, v254\n"
    "v_mfma_f32_16x16x32_bf16 v[148:151], a[244:247], v[232:235], v[148:151]\n"
    "ds_read_b128 a[212:215], v249 offset:53248\n"
    "v_add_f32 v241, v241, v254\n"
    "v_mfma_f32_16x16x32_bf16 v[152:155], a[248:251], v[232:235], v[152:155]\n"
    "v_rcp_f32 v241, v241\n"
    "v_mfma_f32_16x16x32_bf16 v[156:159], a[252:255], v[232:235], v[156:159]\n"
    "s_waitcnt lgkmcnt(5)\n"
    "ds_read_b128 a[216:219], v248 offset:54272\n"
    "v_add_f32 v246, v246, v216\n"
    "v_add_f32 v246, v246, v217\n"
    "v_add_f32 v246, v246, v218\n"
    "v_add_f32 v246, v246, v219\n"
    "v_accvgpr_read_b32 v100, a160\n"
    "v_accvgpr_read_b32 v101, a161\n"
    "v_accvgpr_read_b32 v102, a162\n"
    "v_accvgpr_read_b32 v103, a163\n"
    "v_accvgpr_read_b32 v108, a164\n"
    "v_accvgpr_read_b32 v109, a165\n"
    "v_accvgpr_read_b32 v110, a166\n"
    "v_accvgpr_read_b32 v111, a167\n"
    "v_mul_f32 v100, v100, v241\n"
    "v_mul_f32 v101, v101, v241\n"
    "v_mul_f32 v102, v102, v241\n"
    "v_mul_f32 v103, v103, v241\n"
    "v_mul_f32 v108, v108, v241\n"
    "v_mul_f32 v109, v109, v241\n"
    "v_mul_f32 v110, v110, v241\n"
    "v_mul_f32 v111, v111, v241\n"
    "v_cvt_pk_bf16_f32 v36, v100, v101\n"
    "v_cvt_pk_bf16_f32 v37, v102, v103\n"
    "v_cvt_pk_bf16_f32 v38, v108, v109\n"
    "v_cvt_pk_bf16_f32 v39, v110, v111\n"
    "v_mfma_f32_16x16x32_bf16 v[216:219], a[192:195], a[96:99], 0\n"
    "s_waitcnt lgkmcnt(5)\n"
    "buffer_store_dwordx4 v[36:39], v255, s[48:51], s60 offen offset:0 sc1\n"
    "ds_read_b128 a[220:223], v249 offset:54272\n"
    "v_accvgpr_read_b32 v116, a168\n"
    "v_accvgpr_read_b32 v117, a169\n"
    "v_accvgpr_read_b32 v118, a170\n"
    "v_accvgpr_read_b32 v119, a171\n"
    "v_accvgpr_read_b32 v124, a172\n"
    "v_accvgpr_read_b32 v125, a173\n"
    "v_accvgpr_read_b32 v126, a174\n"
    "v_accvgpr_read_b32 v127, a175\n"
    "v_mul_f32 v116, v116, v241\n"
    "v_mul_f32 v117, v117, v241\n"
    "v_mul_f32 v118, v118, v241\n"
    "v_mul_f32 v119, v119, v241\n"
    "v_mul_f32 v124, v124, v241\n"
    "v_mul_f32 v125, v125, v241\n"
    "v_mul_f32 v126, v126, v241\n"
    "v_mul_f32 v127, v127, v241\n"
    "v_cvt_pk_bf16_f32 v44, v116, v117\n"
    "v_cvt_pk_bf16_f32 v45, v118, v119\n"
    "v_cvt_pk_bf16_f32 v46, v124, v125\n"
    "v_cvt_pk_bf16_f32 v47, v126, v127\n"
    "v_mfma_f32_16x16x32_bf16 v[216:219], a[196:199], a[100:103], v[216:219]\n"
    "s_waitcnt lgkmcnt(5)\n"
    "buffer_store_dwordx4 v[44:47], v255, s[48:51], s60 offen offset:64 sc1\n"
    "v_mfma_f32_16x16x32_bf16 v[216:219], a[200:203], a[104:107], v[216:219]\n"
    "s_waitcnt lgkmcnt(4)\n"
    "v_add_f32 v246, v246, v220\n"
    "v_add_f32 v246, v246, v221\n"
    "v_mfma_f32_16x16x32_bf16 v[216:219], a[204:207], a[108:111], v[216:219]\n"
    "s_waitcnt lgkmcnt(3)\n"
    "v_add_f32 v246, v246, v222\n"
    "v_add_f32 v246, v246, v223\n"
    "v_mfma_f32_16x16x32_bf16 v[220:223], a[208:211], a[96:99], 0\n"
    "s_waitcnt lgkmcnt(2)\n"
    "v_exp_f32 v224, v224\n"
    "v_mfma_f32_16x16x32_bf16 v[220:223], a[212:215], a[100:103], v[220:223]\n"
    "s_waitcnt lgkmcnt(1)\n"
    "v_exp_f32 v225, v225\n"
    "v_exp_f32 v226, v226\n"
    "v_exp_f32 v227, v227\n"
    "v_exp_f32 v228, v228\n"
    "v_exp_f32 v229, v229\n"
    "v_exp_f32 v230, v230\n"
    "v_exp_f32 v231, v231\n"
    "v_mfma_f32_16x16x32_bf16 v[220:223], a[216:219], a[104:107], v[220:223]\n"
    "s_waitcnt lgkmcnt(0)\n"
    "v_cvt_pk_bf16_f32 v236, v224, v225\n"
    "v_cvt_pk_bf16_f32 v237, v226, v227\n"
    "v_cvt_pk_bf16_f32 v238, v228, v229\n"
    "v_cvt_pk_bf16_f32 v239, v230, v231\n"
    "v_mfma_f32_16x16x32_bf16 v[220:223], a[220:223], a[108:111], v[220:223]\n"
    "v_accvgpr_read_b32 v100, a176\n"
    "v_accvgpr_read_b32 v101, a177\n"
    "v_mfma_f32_16x16x32_bf16 v[160:163], a[224:227], v[236:239], v[160:163]\n"
    "v_accvgpr_read_b32 v102, a178\n"
    "v_accvgpr_read_b32 v103, a179\n"
    "v_mfma_f32_16x16x32_bf16 v[164:167], a[228:231], v[236:239], v[164:167]\n"
    "v_accvgpr_read_b32 v108, a180\n"
    "v_accvgpr_read_b32 v109, a181\n"
    "v_accvgpr_read_b32 v110, a182\n"
    "v_accvgpr_read_b32 v111, a183\n"
    "v_mul_f32 v100, v100, v241\n"
    "v_mul_f32 v101, v101, v241\n"
    "v_mul_f32 v102, v102, v241\n"
    "v_mul_f32 v103, v103, v241\n"
    "v_mul_f32 v108, v108, v241\n"
    "v_mul_f32 v109, v109, v241\n"
    "v_mul_f32 v110, v110, v241\n"
    "v_mul_f32 v111, v111, v241\n"
    "v_cvt_pk_bf16_f32 v52, v100, v101\n"
    "v_cvt_pk_bf16_f32 v53, v102, v103\n"
    "v_cvt_pk_bf16_f32 v54, v108, v109\n"
    "v_cvt_pk_bf16_f32 v55, v110, v111\n"
    "v_mfma_f32_16x16x32_bf16 v[168:171], a[232:235], v[236:239], v[168:171]\n"
    "buffer_store_dwordx4 v[52:55], v255, s[48:51], s60 offen offset:128 sc1\n"
    "v_accvgpr_read_b32 v116, a184\n"
    "v_accvgpr_read_b32 v117, a185\n"
    "v_accvgpr_read_b32 v118, a186\n"
    "v_accvgpr_read_b32 v119, a187\n"
    "v_accvgpr_read_b32 v124, a188\n"
    "v_accvgpr_read_b32 v125, a189\n"
    "v_accvgpr_read_b32 v126, a190\n"
    "v_accvgpr_read_b32 v127, a191\n"
    "v_mul_f32 v116, v116, v241\n"
    "v_mul_f32 v117, v117, v241\n"
    "v_mul_f32 v118, v118, v241\n"
    "v_mul_f32 v119, v119, v241\n"
    "v_mul_f32 v124, v124, v241\n"
    "v_mul_f32 v125, v125, v241\n"
    "v_mul_f32 v126, v126, v241\n"
    "v_mul_f32 v127, v127, v241\n"
    "v_cvt_pk_bf16_f32 v60, v116, v117\n"
    "v_cvt_pk_bf16_f32 v61, v118, v119\n"
    "v_cvt_pk_bf16_f32 v62, v124, v125\n"
    "v_cvt_pk_bf16_f32 v63, v126, v127\n"
    "v_mfma_f32_16x16x32_bf16 v[172:175], a[236:239], v[236:239], v[172:175]\n"
    "buffer_store_dwordx4 v[60:63], v255, s[48:51], s60 offen offset:192 sc1\n"
    "v_mfma_f32_16x16x32_bf16 v[176:179], a[240:243], v[236:239], v[176:179]\n"
    "s_add_u32 s60, s67, 114688\n"
    "v_add_f32 v247, v247, v224\n"
    "v_add_f32 v247, v247, v225\n"
    "v_mfma_f32_16x16x32_bf16 v[180:183], a[244:247], v[236:239], v[180:183]\n"
    "v_add_f32 v247, v247, v226\n"
    "v_add_f32 v247, v247, v227\n"
    "v_mfma_f32_16x16x32_bf16 v[184:187], a[248:251], v[236:239], v[184:187]\n"
    "ds_read_b64_tr_b16 v[192:193], v250 offset:49152\n"
    "ds_read_b64_tr_b16 v[194:195], v250 offset:53248\n"
    "v_mfma_f32_16x16x32_bf16 v[188:191], a[252:255], v[236:239], v[188:191]\n"
    "ds_read_b64_tr_b16 v[196:197], v250 offset:49160\n"
    "ds_read_b64_tr_b16 v[198:199], v250 offset:53256\n"
    "v_mfma_f32_16x16x32_bf16 v[224:227], a[192:195], a[112:115], 0\n"
    "ds_read_b64_tr_b16 v[200:201], v251 offset:49152\n"
    "ds_read_b64_tr_b16 v[202:203], v251 offset:53248\n"
    "v_mfma_f32_16x16x32_bf16 v[224:227], a[196:199], a[116:119], v[224:227]\n"
    "v_add_f32 v247, v247, v228\n"
    "v_add_f32 v247, v247, v229\n"
    "v_mfma_f32_16x16x32_bf16 v[224:227], a[200:203], a[120:123], v[224:227]\n"
    "v_add_f32 v247, v247, v230\n"
    "v_add_f32 v247, v247, v231\n"
    "v_mfma_f32_16x16x32_bf16 v[224:227], a[204:207], a[124:127], v[224:227]\n"
    "ds_read_b64_tr_b16 v[204:205], v251 offset:49160\n"
    "ds_read_b64_tr_b16 v[206:207], v251 offset:53256\n"
    "v_mfma_f32_16x16x32_bf16 v[228:231], a[208:211], a[112:115], 0\n"
    "ds_read_b64_tr_b16 v[208:209], v250 offset:50176\n"
    "ds_read_b64_tr_b16 v[210:211], v250 offset:54272\n"
    "v_mfma_f32_16x16x32_bf16 v[228:231], a[212:215], a[116:119], v[228:231]\n"
    "v_exp_f32 v216, v216\n"
    "v_exp_f32 v217, v217\n"
    "v_exp_f32 v218, v218\n"
    "v_exp_f32 v219, v219\n"
    "v_exp_f32 v220, v220\n"
    "v_exp_f32 v221, v221\n"
    "v_exp_f32 v222, v222\n"
    "v_exp_f32 v223, v223\n"
    "v_mfma_f32_16x16x32_bf16 v[228:231], a[216:219], a[120:123], v[228:231]\n"
    "ds_read_b64_tr_b16 v[212:213], v250 offset:50184\n"
    "ds_read_b64_tr_b16 v[214:215], v250 offset:54280\n"
    "v_cvt_pk_bf16_f32 v232, v216, v217\n"
    "v_cvt_pk_bf16_f32 v233, v218, v219\n"
    "v_cvt_pk_bf16_f32 v234, v220, v221\n"
    "v_cvt_pk_bf16_f32 v235, v222, v223\n"
    "v_mfma_f32_16x16x32_bf16 v[228:231], a[220:223], a[124:127], v[228:231]\n"
    "s_waitcnt lgkmcnt(10)\n"
    "ds_read_b64_tr_b16 a[248:249], v251 offset:50176\n"
    "ds_read_b64_tr_b16 a[250:251], v251 offset:54272\n"
    "v_mfma_f32_16x16x32_bf16 v[128:131], v[192:195], v[232:235], v[128:131]\n"
    "s_waitcnt lgkmcnt(10)\n"
    "ds_read_b64_tr_b16 a[252:253], v251 offset:50184\n"
    "ds_read_b64_tr_b16 a[254:255], v251 offset:54280\n"
    "v_mfma_f32_16x16x32_bf16 v[132:135], v[196:199], v[232:235], v[132:135]\n"
    "s_waitcnt lgkmcnt(10)\n"
    "v_exp_f32 v224, v224\n"
    "v_mfma_f32_16x16x32_bf16 v[136:139], v[200:203], v[232:235], v[136:139]\n"
    "s_waitcnt lgkmcnt(8)\n"
    "v_exp_f32 v225, v225\n"
    "v_mfma_f32_16x16x32_bf16 v[140:143], v[204:207], v[232:235], v[140:143]\n"
    "s_waitcnt lgkmcnt(6)\n"
    "v_exp_f32 v226, v226\n"
    "v_mfma_f32_16x16x32_bf16 v[144:147], v[208:211], v[232:235], v[144:147]\n"
    "s_waitcnt lgkmcnt(4)\n"
    "v_exp_f32 v227, v227\n"
    "v_mfma_f32_16x16x32_bf16 v[148:151], v[212:215], v[232:235], v[148:151]\n"
    "s_waitcnt lgkmcnt(2)\n"
    "v_exp_f32 v228, v228\n"
    "v_exp_f32 v229, v229\n"
    "v_exp_f32 v230, v230\n"
    "v_exp_f32 v231, v231\n"
    "v_mfma_f32_16x16x32_bf16 v[152:155], a[248:251], v[232:235], v[152:155]\n"
    "s_waitcnt lgkmcnt(0)\n"
    "v_cvt_pk_bf16_f32 v236, v224, v225\n"
    "v_cvt_pk_bf16_f32 v237, v226, v227\n"
    "v_cvt_pk_bf16_f32 v238, v228, v229\n"
    "v_cvt_pk_bf16_f32 v239, v230, v231\n"
    "v_mfma_f32_16x16x32_bf16 v[156:159], a[252:255], v[232:235], v[156:159]\n"
    "v_add_f32 v246, v246, v216\n"
    "v_add_f32 v246, v246, v217\n"
    "v_mfma_f32_16x16x32_bf16 v[160:163], v[192:195], v[236:239], v[160:163]\n"
    "v_add_f32 v246, v246, v218\n"
    "v_add_f32 v246, v246, v219\n"
    "v_mfma_f32_16x16x32_bf16 v[164:167], v[196:199], v[236:239], v[164:167]\n"
    "v_add_f32 v246, v246, v220\n"
    "v_add_f32 v246, v246, v221\n"
    "v_mfma_f32_16x16x32_bf16 v[168:171], v[200:203], v[236:239], v[168:171]\n"
    "v_add_f32 v246, v246, v222\n"
    "v_add_f32 v246, v246, v223\n"
    "v_mfma_f32_16x16x32_bf16 v[172:175], v[204:207], v[236:239], v[172:175]\n"
    "v_add_f32 v247, v247, v224\n"
    "v_add_f32 v247, v247, v225\n"
    "v_mfma_f32_16x16x32_bf16 v[176:179], v[208:211], v[236:239], v[176:179]\n"
    "v_add_f32 v247, v247, v226\n"
    "v_add_f32 v247, v247, v227\n"
    "v_mfma_f32_16x16x32_bf16 v[180:183], v[212:215], v[236:239], v[180:183]\n"
    "v_add_f32 v247, v247, v228\n"
    "v_add_f32 v247, v247, v229\n"
    "v_mfma_f32_16x16x32_bf16 v[184:187], a[248:251], v[236:239], v[184:187]\n"
    "v_add_f32 v247, v247, v230\n"
    "v_add_f32 v247, v247, v231\n"
    "v_mfma_f32_16x16x32_bf16 v[188:191], a[252:255], v[236:239], v[188:191]\n"
    "v_mov_b32 v253, v246\n"
    "v_mov_b32 v254, v247\n"
    "s_nop 0\n"
    "v_permlane32_swap_b32 v246, v253\n"
    "v_add_f32 v246, v246, v253\n"
    "v_mov_b32 v253, v246\n"
    "s_nop 1\n"
    "v_permlane16_swap_b32 v246, v253\n"
    "v_add_f32 v246, v246, v253\n"
    "v_rcp_f32 v246, v246\n"
    "s_nop 0\n"
    "v_pk_mul_f32 v[128:129], v[128:129], v[246:247] op_sel_hi:[1,0]\n"
    "v_pk_mul_f32 v[130:131], v[130:131], v[246:247] op_sel_hi:[1,0]\n"
    "v_pk_mul_f32 v[132:133], v[132:133], v[246:247] op_sel_hi:[1,0]\n"
    "v_pk_mul_f32 v[134:135], v[134:135], v[246:247] op_sel_hi:[1,0]\n"
    "v_cvt_pk_bf16_f32 v128, v128, v129\n"
    "v_cvt_pk_bf16_f32 v129, v130, v131\n"
    "v_cvt_pk_bf16_f32 v130, v132, v133\n"
    "v_cvt_pk_bf16_f32 v131, v134, v135\n"
    "buffer_store_dwordx4 v[128:131], v255, s[48:51], s59 offen offset:0 sc1\n"
    "v_pk_mul_f32 v[136:137], v[136:137], v[246:247] op_sel_hi:[1,0]\n"
    "v_pk_mul_f32 v[138:139], v[138:139], v[246:247] op_sel_hi:[1,0]\n"
    "v_pk_mul_f32 v[140:141], v[140:141], v[246:247] op_sel_hi:[1,0]\n"
    "v_pk_mul_f32 v[142:143], v[142:143], v[246:247] op_sel_hi:[1,0]\n"
    "v_cvt_pk_bf16_f32 v136, v136, v137\n"
    "v_cvt_pk_bf16_f32 v137, v138, v139\n"
    "v_cvt_pk_bf16_f32 v138, v140, v141\n"
    "v_cvt_pk_bf16_f32 v139, v142, v143\n"
    "buffer_store_dwordx4 v[136:139], v255, s[48:51], s59 offen offset:64 sc1\n"
    "v_pk_mul_f32 v[144:145], v[144:145], v[246:247] op_sel_hi:[1,0]\n"
    "v_pk_mul_f32 v[146:147], v[146:147], v[246:247] op_sel_hi:[1,0]\n"
    "v_pk_mul_f32 v[148:149], v[148:149], v[246:247] op_sel_hi:[1,0]\n"
    "v_pk_mul_f32 v[150:151], v[150:151], v[246:247] op_sel_hi:[1,0]\n"
    "v_cvt_pk_bf16_f32 v144, v144, v145\n"
    "v_cvt_pk_bf16_f32 v145, v146, v147\n"
    "v_cvt_pk_bf16_f32 v146, v148, v149\n"
    "v_cvt_pk_bf16_f32 v147, v150, v151\n"
    "buffer_store_dwordx4 v[144:147], v255, s[48:51], s59 offen offset:128 sc1\n"
    "v_pk_mul_f32 v[152:153], v[152:153], v[246:247] op_sel_hi:[1,0]\n"
    "v_pk_mul_f32 v[154:155], v[154:155], v[246:247] op_sel_hi:[1,0]\n"
    "v_pk_mul_f32 v[156:157], v[156:157], v[246:247] op_sel_hi:[1,0]\n"
    "v_pk_mul_f32 v[158:159], v[158:159], v[246:247] op_sel_hi:[1,0]\n"
    "v_cvt_pk_bf16_f32 v152, v152, v153\n"
    "v_cvt_pk_bf16_f32 v153, v154, v155\n"
    "v_cvt_pk_bf16_f32 v154, v156, v157\n"
    "v_cvt_pk_bf16_f32 v155, v158, v159\n"
    "buffer_store_dwordx4 v[152:155], v255, s[48:51], s59 offen offset:192 sc1\n"
    "v_permlane32_swap_b32 v247, v254\n"
    "v_add_f32 v247, v247, v254\n"
    "v_mov_b32 v254, v247\n"
    "s_nop 1\n"
    "v_permlane16_swap_b32 v247, v254\n"
    "v_add_f32 v247, v247, v254\n"
    "v_rcp_f32 v247, v247\n"
    "s_nop 0\n"
    "v_pk_mul_f32 v[160:161], v[160:161], v[246:247] op_sel:[0,1] op_sel_hi:[1,1]\n"
    "v_pk_mul_f32 v[162:163], v[162:163], v[246:247] op_sel:[0,1] op_sel_hi:[1,1]\n"
    "v_pk_mul_f32 v[164:165], v[164:165], v[246:247] op_sel:[0,1] op_sel_hi:[1,1]\n"
    "v_pk_mul_f32 v[166:167], v[166:167], v[246:247] op_sel:[0,1] op_sel_hi:[1,1]\n"
    "v_cvt_pk_bf16_f32 v160, v160, v161\n"
    "v_cvt_pk_bf16_f32 v161, v162, v163\n"
    "v_cvt_pk_bf16_f32 v162, v164, v165\n"
    "v_cvt_pk_bf16_f32 v163, v166, v167\n"
    "buffer_store_dwordx4 v[160:163], v255, s[48:51], s60 offen offset:0 sc1\n"
    "v_pk_mul_f32 v[168:169], v[168:169], v[246:247] op_sel:[0,1] op_sel_hi:[1,1]\n"
    "v_pk_mul_f32 v[170:171], v[170:171], v[246:247] op_sel:[0,1] op_sel_hi:[1,1]\n"
    "v_pk_mul_f32 v[172:173], v[172:173], v[246:247] op_sel:[0,1] op_sel_hi:[1,1]\n"
    "v_pk_mul_f32 v[174:175], v[174:175], v[246:247] op_sel:[0,1] op_sel_hi:[1,1]\n"
    "v_cvt_pk_bf16_f32 v168, v168, v169\n"
    "v_cvt_pk_bf16_f32 v169, v170, v171\n"
    "v_cvt_pk_bf16_f32 v170, v172, v173\n"
    "v_cvt_pk_bf16_f32 v171, v174, v175\n"
    "buffer_store_dwordx4 v[168:171], v255, s[48:51], s60 offen offset:64 sc1\n"
    "v_pk_mul_f32 v[176:177], v[176:177], v[246:247] op_sel:[0,1] op_sel_hi:[1,1]\n"
    "v_pk_mul_f32 v[178:179], v[178:179], v[246:247] op_sel:[0,1] op_sel_hi:[1,1]\n"
    "v_pk_mul_f32 v[180:181], v[180:181], v[246:247] op_sel:[0,1] op_sel_hi:[1,1]\n"
    "v_pk_mul_f32 v[182:183], v[182:183], v[246:247] op_sel:[0,1] op_sel_hi:[1,1]\n"
    "v_cvt_pk_bf16_f32 v176, v176, v177\n"
    "v_cvt_pk_bf16_f32 v177, v178, v179\n"
    "v_cvt_pk_bf16_f32 v178, v180, v181\n"
    "v_cvt_pk_bf16_f32 v179, v182, v183\n"
    "buffer_store_dwordx4 v[176:179], v255, s[48:51], s60 offen offset:128 sc1\n"
    "v_pk_mul_f32 v[184:185], v[184:185], v[246:247] op_sel:[0,1] op_sel_hi:[1,1]\n"
    "v_pk_mul_f32 v[186:187], v[186:187], v[246:247] op_sel:[0,1] op_sel_hi:[1,1]\n"
    "v_pk_mul_f32 v[188:189], v[188:189], v[246:247] op_sel:[0,1] op_sel_hi:[1,1]\n"
    "v_pk_mul_f32 v[190:191], v[190:191], v[246:247] op_sel:[0,1] op_sel_hi:[1,1]\n"
    "v_cvt_pk_bf16_f32 v184, v184, v185\n"
    "v_cvt_pk_bf16_f32 v185, v186, v187\n"
    "v_cvt_pk_bf16_f32 v186, v188, v189\n"
    "v_cvt_pk_bf16_f32 v187, v190, v191\n"
    "buffer_store_dwordx4 v[184:187], v255, s[48:51], s60 offen offset:192 sc1\n"
    "v_mfma_f32_16x16x32_bf16 v[224:227], v[0:3], v[8:11], 0\n"
    "v_mfma_f32_16x16x32_bf16 v[224:227], v[16:19], v[24:27], v[224:227]\n"
    "v_mfma_f32_16x16x32_bf16 v[224:227], v[32:35], v[40:43], v[224:227]\n"
    "v_mfma_f32_16x16x32_bf16 v[224:227], v[48:51], v[56:59], v[224:227]\n"
    "v_mfma_f32_16x16x32_bf16 v[224:227], v[64:67], v[72:75], v[224:227]\n"
    "v_mfma_f32_16x16x32_bf16 v[224:227], v[80:83], v[88:91], v[224:227]\n"
    "v_mfma_f32_16x16x32_bf16 v[224:227], v[96:99], v[104:107], v[224:227]\n"
    "v_mfma_f32_16x16x32_bf16 v[224:227], v[112:115], v[120:123], v[224:227]\n"
    "v_mfma_f32_16x16x32_bf16 v[224:227], v[4:7], v[12:15], v[224:227]\n"
    "v_mfma_f32_16x16x32_bf16 v[224:227], v[20:23], v[28:31], v[224:227]\n"
    "v_mfma_f32_16x16x32_bf16 v[224:227], v[36:39], v[44:47], v[224:227]\n"
    "v_mfma_f32_16x16x32_bf16 v[224:227], v[52:55], v[60:63], v[224:227]\n"
    "v_mfma_f32_16x16x32_bf16 v[224:227], v[128:131], v[136:139], v[224:227]\n"
    "v_mfma_f32_16x16x32_bf16 v[224:227], v[144:147], v[152:155], v[224:227]\n"
    "v_mfma_f32_16x16x32_bf16 v[224:227], v[160:163], v[168:171], v[224:227]\n"
    "v_mfma_f32_16x16x32_bf16 v[224:227], v[176:179], v[184:187], v[224:227]\n"
    "v_min3_f32 v216, v240, v241, v242\n"
    "v_min3_f32 v216, v216, v243, v244\n"
    "v_min3_f32 v216, v216, v245, v246\n"
    "v_min3_f32 v216, v216, v247, v247\n"
    "v_max3_f32 v217, v240, v241, v242\n"
    "v_max3_f32 v217, v217, v243, v244\n"
    "v_max3_f32 v217, v217, v245, v246\n"
    "v_max3_f32 v217, v217, v247, v247\n"
    "v_cmp_nle_f32 vcc, s34, v216\n"
    "s_or_b64 s[62:63], s[62:63], vcc\n"
    "v_cmp_nge_f32 vcc, s35, v217\n"
    "s_or_b64 s[62:63], s[62:63], vcc\n"
    "s_nop 7\n"
    "v_add_f32 v224, v224, v225\n"
    "v_add_f32 v226, v226, v227\n"
    "s_movk_i32 s68, 0x207\n"
    "v_add_f32 v224, v224, v226\n"
    "v_cmp_class_f32 vcc, v224, s68\n"
    "s_or_b64 s[62:63], s[62:63], vcc\n"
    "s_branch LEND_%=\n"
    "LBL_%=:\n"
    ".p2align 4\n"
    "LB_%=:\n"
    "v_mfma_f32_16x16x32_bf16 v[216:219], a[192:195], a[0:3], 0\n"
    "s_waitcnt lgkmcnt(6)\n"
    "s_add_u32 s59, s58, 0\n"
    "v_exp_f32 v229, v229\n"
    "v_mfma_f32_16x16x32_bf16 v[216:219], a[196:199], a[4:7], v[216:219]\n"
    "s_waitcnt lgkmcnt(5)\n"
    "s_add_u32 m0, s56, 8192\n"
    "v_exp_f32 v230, v230\n"
    "v_mfma_f32_16x16x32_bf16 v[216:219], a[200:203], a[8:11], v[216:219]\n"
    "s_waitcnt lgkmcnt(4)\n"
    "s_add_u32 s60, s58, 4096\n"
    "v_exp_f32 v231, v231\n"
    "v_mfma_f32_16x16x32_bf16 v[216:219], a[204:207], a[12:15], v[216:219]\n"
    "s_waitcnt lgkmcnt(3)\n"
    "v_cvt_pk_bf16_f32 v236, v224, v225\n"
    "v_cvt_pk_bf16_f32 v237, v226, v227\n"
    "v_mfma_f32_16x16x32_bf16 v[220:223], a[208:211], a[0:3], 0\n"
    "s_waitcnt lgkmcnt(2)\n"
    "v_cvt_pk_bf16_f32 v238, v228, v229\n"
    "v_cvt_pk_bf16_f32 v239, v230, v231\n"
    "v_mfma_f32_16x16x32_bf16 v[220:223], a[212:215], a[4:7], v[220:223]\n"
    "s_waitcnt lgkmcnt(1)\n"
    "ds_read_b64_tr_b16 a[224:225], v250 offset:0\n"
    "ds_read_b64_tr_b16 a[226:227], v250 offset:4096\n"
    "v_mfma_f32_16x16x32_bf16 v[220:223], a[216:219], a[8:11], v[220:223]\n"
    "s_waitcnt lgkmcnt(2)\n"
    "ds_read_b64_tr_b16 a[228:229], v250 offset:8\n"
    "ds_read_b64_tr_b16 a[230:231], v250 offset:4104\n"
    "v_mfma_f32_16x16x32_bf16 v[220:223], a[220:223], a[12:15], v[220:223]\n"
    "ds_read_b64_tr_b16 a[232:233], v251 offset:0\n"
    "ds_read_b64_tr_b16 a[234:235], v251 offset:4096\n"
    "v_mfma_f32_16x16x32_bf16 v[128:131], v[192:195], v[236:239], v[128:131]\n"
    "ds_read_b64_tr_b16 a[236:237], v251 offset:8\n"
    "ds_read_b64_tr_b16 a[238:239], v251 offset:4104\n"
    "v_mfma_f32_16x16x32_bf16 v[132:135], v[196:199], v[236:239], v[132:135]\n"
    "ds_read_b64_tr_b16 a[240:241], v250 offset:1024\n"
    "ds_read_b64_tr_b16 a[242:243], v250 offset:5120\n"
    "v_mfma_f32_16x16x32_bf16 v[136:139], v[200:203], v[236:239], v[136:139]\n"
    "v_exp_f32 v216, v216\n"
    "v_mfma_f32_16x16x32_bf16 v[140:143], v[204:207], v[236:239], v[140:143]\n"
    "v_exp_f32 v217, v217\n"
    "v_mfma_f32_16x16x32_bf16 v[144:147], v[208:211], v[236:239], v[144:147]\n"
    "v_exp_f32 v218, v218\n"
    "v_mfma_f32_16x16x32_bf16 v[148:151], v[212:215], v[236:239], v[148:151]\n"
    "v_exp_f32 v219, v219\n"
    "v_mfma_f32_16x16x32_bf16 v[152:155], a[248:251], v[236:239], v[152:155]\n"
    "v_exp_f32 v220, v220\n"
    "v_mfma_f32_16x16x32_bf16 v[156:159], a[252:255], v[236:239], v[156:159]\n"
    "v_exp_f32 v221, v221\n"
    "v_mfma_f32_16x16x32_bf16 v[188:191], a[112:115], v[236:239], v[188:191]\n"
    "v_exp_f32 v222, v222\n"
    "v_mfma_f32_16x16x32_bf16 v[224:227], a[192:195], a[16:19], 0\n"
    "v_exp_f32 v223, v223\n"
    "v_mfma_f32_16x16x32_bf16 v[224:227], a[196:199], a[20:23], v[224:227]\n"
    "ds_read_b64_tr_b16 a[244:245], v250 offset:1032\n"
    "ds_read_b64_tr_b16 a[246:247], v250 offset:5128\n"
    "v_mfma_f32_16x16x32_bf16 v[224:227], a[200:203], a[24:27], v[224:227]\n"
    "v_cvt_pk_bf16_f32 v232, v216, v217\n"
    "v_cvt_pk_bf16_f32 v233, v218, v219\n"
    "v_mfma_f32_16x16x32_bf16 v[224:227], a[204:207], a[28:31], v[224:227]\n"
    "v_cvt_pk_bf16_f32 v234, v220, v221\n"
    "v_cvt_pk_bf16_f32 v235, v222, v223\n"
    "v_mfma_f32_16x16x32_bf16 v[228:231], a[208:211], a[16:19], 0\n"
    "ds_read_b64_tr_b16 a[248:249], v251 offset:1024\n"
    "ds_read_b64_tr_b16 a[250:251], v251 offset:5120\n"
    "v_mfma_f32_16x16x32_bf16 v[228:231], a[212:215], a[20:23], v[228:231]\n"
    "ds_read_b64_tr_b16 a[252:253], v251 offset:1032\n"
    "ds_read_b64_tr_b16 a[254:255], v251 offset:5128\n"
    "v_mfma_f32_16x16x32_bf16 v[228:231], a[216:219], a[24:27], v[228:231]\n"
    "v_add_f32 v240, v240, v216\n"
    "v_add_f32 v240, v240, v217\n"
    "v_mfma_f32_16x16x32_bf16 v[228:231], a[220:223], a[28:31], v[228:231]\n"
    "s_waitcnt lgkmcnt(14)\n"
    "v_add_f32 v240, v240, v218\n"
    "v_add_f32 v240, v240, v219\n"
    "v_mfma_f32_16x16x32_bf16 a[128:131], a[224:227], v[232:235], a[128:131]\n"
    "s_waitcnt lgkmcnt(12)\n"
    "v_add_f32 v240, v240, v220\n"
    "v_add_f32 v240, v240, v221\n"
    "v_mfma_f32_16x16x32_bf16 a[132:135], a[228:231], v[232:235], a[132:135]\n"
    "s_waitcnt lgkmcnt(10)\n"
    "v_add_f32 v240, v240, v222\n"
    "v_add_f32 v240, v240, v223\n"
    "v_mfma_f32_16x16x32_bf16 a[136:139], a[232:235], v[232:235], a[136:139]\n"
    "s_waitcnt lgkmcnt(8)\n"
    "v_exp_f32 v224, v224\n"
    "v_mfma_f32_16x16x32_bf16 a[140:143], a[236:239], v[232:235], a[140:143]\n"
    "s_waitcnt lgkmcnt(6)\n"
    "v_exp_f32 v225, v225\n"
    "v_mfma_f32_16x16x32_bf16 a[144:147], a[240:243], v[232:235], a[144:147]\n"
    "s_waitcnt lgkmcnt(4)\n"
    "v_exp_f32 v226, v226\n"
    "v_mfma_f32_16x16x32_bf16 a[148:151], a[244:247], v[232:235], a[148:151]\n"
    "s_waitcnt lgkmcnt(2)\n"
    "v_exp_f32 v227, v227\n"
    "v_mfma_f32_16x16x32_bf16 a[152:155], a[248:251], v[232:235], a[152:155]\n"
    "s_waitcnt lgkmcnt(0)\n"
    "v_exp_f32 v228, v228\n"
    "v_mfma_f32_16x16x32_bf16 a[156:159], a[252:255], v[232:235], a[156:159]\n"
    "v_exp_f32 v229, v229\n"
    "v_mfma_f32_16x16x32_bf16 v[216:219], a[192:195], a[32:35], 0\n"
    "v_exp_f32 v230, v230\n"
    "v_mfma_f32_16x16x32_bf16 v[216:219], a[196:199], a[36:39], v[216:219]\n"
    "v_exp_f32 v231, v231\n"
    "v_mfma_f32_16x16x32_bf16 v[216:219], a[200:203], a[40:43], v[216:219]\n"
    "v_cvt_pk_bf16_f32 v236, v224, v225\n"
    "v_cvt_pk_bf16_f32 v237, v226, v227\n"
    "v_mfma_f32_16x16x32_bf16 v[216:219], a[204:207], a[44:47], v[216:219]\n"
    "v_cvt_pk_bf16_f32 v238, v228, v229\n"
    "v_cvt_pk_bf16_f32 v239, v230, v231\n"
    "v_mfma_f32_16x16x32_bf16 v[220:223], a[208:211], a[32:35], 0\n"
    "v_add_f32 v241, v241, v224\n"
    "v_add_f32 v241, v241, v225\n"
    "v_mfma_f32_16x16x32_bf16 v[220:223], a[212:215], a[36:39], v[220:223]\n"
    "v_add_f32 v241, v241, v226\n"
    "v_add_f32 v241, v241, v227\n"
    "v_mfma_f32_16x16x32_bf16 v[220:223], a[216:219], a[40:43], v[220:223]\n"
    "v_add_f32 v241, v241, v228\n"
    "v_add_f32 v241, v241, v229\n"
    "v_mfma_f32_16x16x32_bf16 v[220:223], a[220:223], a[44:47], v[220:223]\n"
    "v_add_f32 v241, v241, v230\n"
    "v_add_f32 v241, v241, v231\n"
    "v_mfma_f32_16x16x32_bf16 a[160:163], a[224:227], v[236:239], a[160:163]\n"
    "v_exp_f32 v216, v216\n"
    "v_mfma_f32_16x16x32_bf16 a[164:167], a[228:231], v[236:239], a[164:167]\n"
    "v_exp_f32 v217, v217\n"
    "v_mfma_f32_16x16x32_bf16 a[168:171], a[232:235], v[236:239], a[168:171]\n"
    "v_exp_f32 v218, v218\n"
    "v_mfma_f32_16x16x32_bf16 a[172:175], a[236:239], v[236:239], a[172:175]\n"
    "v_exp_f32 v219, v219\n"
    "v_mfma_f32_16x16x32_bf16 a[176:179], a[240:243], v[236:239], a[176:179]\n"
    "v_exp_f32 v220, v220\n"
    "v_mfma_f32_16x16x32_bf16 a[180:183], a[244:247], v[236:239], a[180:183]\n"
    "v_exp_f32 v221, v221\n"
    "v_mfma_f32_16x16x32_bf16 a[184:187], a[248:251], v[236:239], a[184:187]\n"
    "v_exp_f32 v222, v222\n"
    "v_mfma_f32_16x16x32_bf16 a[188:191], a[252:255], v[236:239], a[188:191]\n"
    "v_exp_f32 v223, v223\n"
    "v_mfma_f32_16x16x32_bf16 v[224:227], a[192:195], a[48:51], 0\n"
    "v_cvt_pk_bf16_f32 v232, v216, v217\n"
    "v_cvt_pk_bf16_f32 v233, v218, v219\n"
    "v_mfma_f32_16x16x32_bf16 v[224:227], a[196:199], a[52:55], v[224:227]\n"
    "v_cvt_pk_bf16_f32 v234, v220, v221\n"
    "v_cvt_pk_bf16_f32 v235, v222, v223\n"
    "v_mfma_f32_16x16x32_bf16 v[224:227], a[200:203], a[56:59], v[224:227]\n"
    "v_mfma_f32_16x16x32_bf16 v[224:227], a[204:207], a[60:63], v[224:227]\n"
    "v_mfma_f32_16x16x32_bf16 v[228:231], a[208:211], a[48:51], 0\n"
    "v_mfma_f32_16x16x32_bf16 v[228:231], a[212:215], a[52:55], v[228:231]\n"
    "v_mfma_f32_16x16x32_bf16 v[228:231], a[216:219], a[56:59], v[228:231]\n"
    "v_exp_f32 v224, v224\n"
    "v_mfma_f32_16x16x32_bf16 v[228:231], a[220:223], a[60:63], v[228:231]\n"
    "v_exp_f32 v225, v225\n"
    "v_mfma_f32_16x16x32_bf16 v[0:3], a[224:227], v[232:235], v[0:3]\n"
    "v_exp_f32 v226, v226\n"
    "v_mfma_f32_16x16x32_bf16 v[4:7], a[228:231], v[232:235], v[4:7]\n"
    "v_exp_f32 v227, v227\n"
    "v_mfma_f32_16x16x32_bf16 v[8:11], a[232:235], v[232:235], v[8:11]\n"
    "v_exp_f32 v228, v228\n"
    "v_mfma_f32_16x16x32_bf16 v[12:15], a[236:239], v[232:235], v[12:15]\n"
    "v_exp_f32 v229, v229\n"
    "v_mfma_f32_16x16x32_bf16 v[16:19], a[240:243], v[232:235], v[16:19]\n"
    "v_exp_f32 v230, v230\n"
    "v_mfma_f32_16x16x32_bf16 v[20:23], a[244:247], v[232:235], v[20:23]\n"
    "v_exp_f32 v231, v231\n"
    "v_mfma_f32_16x16x32_bf16 v[24:27], a[248:251], v[232:235], v[24:27]\n"
    "v_cvt_pk_bf16_f32 v236, v224, v225\n"
    "v_cvt_pk_bf16_f32 v237, v226, v227\n"
    "v_mfma_f32_16x16x32_bf16 v[28:31], a[252:255], v[232:235], v[28:31]\n"
    "v_cvt_pk_bf16_f32 v238, v228, v229\n"
    "v_cvt_pk_bf16_f32 v239, v230, v231\n"
    "v_mfma_f32_16x16x32_bf16 v[180:183], a[112:115], v[232:235], v[180:183]\n"
    "v_add_f32 v243, v243, v224\n"
    "v_add_f32 v243, v243, v225\n"
    "v_mfma_f32_16x16x32_bf16 v[216:219], a[192:195], a[64:67], 0\n"
    "v_add_f32 v243, v243, v226\n"
    "v_add_f32 v243, v243, v227\n"
    "v_mfma_f32_16x16x32_bf16 v[216:219], a[196:199], a[68:71], v[216:219]\n"
    "v_add_f32 v243, v243, v228\n"
    "v_add_f32 v243, v243, v229\n"
    "v_mfma_f32_16x16x32_bf16 v[216:219], a[200:203], a[72:75], v[216:219]\n"
    "v_add_f32 v243, v243, v230\n"
    "v_add_f32 v243, v243, v231\n"
    "v_mfma_f32_16x16x32_bf16 v[216:219], a[204:207], a[76:79], v[216:219]\n"
    "v_mfma_f32_16x16x32_bf16 v[220:223], a[208:211], a[64:67], 0\n"
    "v_mfma_f32_16x16x32_bf16 v[220:223], a[212:215], a[68:71], v[220:223]\n"
    "v_mfma_f32_16x16x32_bf16 v[220:223], a[216:219], a[72:75], v[220:223]\n"
    "v_exp_f32 v216, v216\n"
    "v_mfma_f32_16x16x32_bf16 v[220:223], a[220:223], a[76:79], v[220:223]\n"
    "v_exp_f32 v217, v217\n"
    "v_mfma_f32_16x16x32_bf16 v[32:35], a[224:227], v[236:239], v[32:35]\n"
    "s_waitcnt vmcnt(16)\n"
    "s_barrier\n"
    "v_mfma_f32_16x16x32_bf16 v[36:39], a[228:231], v[236:239], v[36:39]\n"
    "buffer_load_dwordx4 v252, s[44:47], s59 offen offset:0 nt lds\n"
    "v_mfma_f32_16x16x32_bf16 v[40:43], a[232:235], v[236:239], v[40:43]\n"
    "v_exp_f32 v218, v218\n"
    "v_mfma_f32_16x16x32_bf16 v[44:47], a[236:239], v[236:239], v[44:47]\n"
    "buffer_load_dwordx4 v252, s[44:47], s59 offen offset:2048 nt lds\n"
    "v_mfma_f32_16x16x32_bf16 v[48:51], a[240:243], v[236:239], v[48:51]\n"
    "s_add_u32 m0, s56, 9088\n"
    "v_exp_f32 v219, v219\n"
    "v_mfma_f32_16x16x32_bf16 v[52:55], a[244:247], v[236:239], v[52:55]\n"
    "v_exp_f32 v220, v220\n"
    "v_mfma_f32_16x16x32_bf16 v[56:59], a[248:251], v[236:239], v[56:59]\n"
    "buffer_load_dwordx4 v252, s[44:47], s59 offen offset:128 nt lds\n"
    "v_mfma_f32_16x16x32_bf16 v[60:63], a[252:255], v[236:239], v[60:63]\n"
    "v_exp_f32 v221, v221\n"
    "v_mfma_f32_16x16x32_bf16 v[224:227], a[192:195], a[80:83], 0\n"
    "v_exp_f32 v222, v222\n"
    "v_mfma_f32_16x16x32_bf16 v[224:227], a[196:199], a[84:87], v[224:227]\n"
    "v_exp_f32 v223, v223\n"
    "v_mfma_f32_16x16x32_bf16 v[224:227], a[200:203], a[88:91], v[224:227]\n"
    "v_cvt_pk_bf16_f32 v232, v216, v217\n"
    "v_cvt_pk_bf16_f32 v233, v218, v219\n"
    "v_mfma_f32_16x16x32_bf16 v[224:227], a[204:207], a[92:95], v[224:227]\n"
    "v_cvt_pk_bf16_f32 v234, v220, v221\n"
    "v_cvt_pk_bf16_f32 v235, v222, v223\n"
    "v_mfma_f32_16x16x32_bf16 v[228:231], a[208:211], a[80:83], 0\n"
    "buffer_load_dwordx4 v252, s[44:47], s59 offen offset:2176 nt lds\n"
    "v_mfma_f32_16x16x32_bf16 v[228:231], a[212:215], a[84:87], v[228:231]\n"
    "s_add_u32 m0, s56, 12288\n"
    "ds_read_b64_tr_b16 v[192:193], v250 offset:16384\n"
    "ds_read_b64_tr_b16 v[194:195], v250 offset:20480\n"
    "v_mfma_f32_16x16x32_bf16 v[228:231], a[216:219], a[88:91], v[228:231]\n"
    "s_add_u32 s59, s58, 0\n"
    "buffer_load_dwordx4 v252, s[44:47], s60 offen offset:0 nt lds\n"
    "v_mfma_f32_16x16x32_bf16 v[228:231], a[220:223], a[92:95], v[228:231]\n"
    "buffer_load_dwordx4 v252, s[44:47], s60 offen offset:2048 nt lds\n"
    "v_mfma_f32_16x16x32_bf16 v[64:67], a[224:227], v[232:235], v[64:67]\n"
    "s_add_u32 m0, s56, 13184\n"
    "v_exp_f32 v224, v224\n"
    "v_mfma_f32_16x16x32_bf16 v[68:71], a[228:231], v[232:235], v[68:71]\n"
    "buffer_load_dwordx4 v252, s[44:47], s60 offen offset:128 nt lds\n"
    "v_mfma_f32_16x16x32_bf16 v[72:75], a[232:235], v[232:235], v[72:75]\n"
    "v_exp_f32 v225, v225\n"
    "v_mfma_f32_16x16x32_bf16 v[76:79], a[236:239], v[232:235], v[76:79]\n"
    "v_exp_f32 v226, v226\n"
    "v_mfma_f32_16x16x32_bf16 v[80:83], a[240:243], v[232:235], v[80:83]\n"
    "buffer_load_dwordx4 v252, s[44:47], s60 offen offset:2176 nt lds\n"
    "v_mfma_f32_16x16x32_bf16 v[84:87], a[244:247], v[232:235], v[84:87]\n"
    "s_add_u32 m0, s56, 0\n"
    "v_exp_f32 v227, v227\n"
    "v_mfma_f32_16x16x32_bf16 v[88:91], a[248:251], v[232:235], v[88:91]\n"
    "s_add_u32 s60, s58, 4096\n"
    "v_exp_f32 v228, v228\n"
    "v_mfma_f32_16x16x32_bf16 v[92:95], a[252:255], v[232:235], v[92:95]\n"
    "buffer_load_dwordx4 v252, s[40:43], s59 offen offset:0 nt lds\n"
    "v_mfma_f32_16x16x32_bf16 v[184:187], a[112:115], v[232:235], v[184:187]\n"
    "v_exp_f32 v229, v229\n"
    "v_mfma_f32_16x16x32_bf16 v[216:219], a[192:195], a[96:99], 0\n"
    "v_exp_f32 v230, v230\n"
    "v_mfma_f32_16x16x32_bf16 v[216:219], a[196:199], a[100:103], v[216:219]\n"
    "v_exp_f32 v231, v231\n"
    "v_mfma_f32_16x16x32_bf16 v[216:219], a[200:203], a[104:107], v[216:219]\n"
    "v_cvt_pk_bf16_f32 v236, v224, v225\n"
    "v_cvt_pk_bf16_f32 v237, v226, v227\n"
    "v_mfma_f32_16x16x32_bf16 v[216:219], a[204:207], a[108:111], v[216:219]\n"
    "v_cvt_pk_bf16_f32 v238, v228, v229\n"
    "v_cvt_pk_bf16_f32 v239, v230, v231\n"
    "v_mfma_f32_16x16x32_bf16 v[220:223], a[208:211], a[96:99], 0\n"
    "buffer_load_dwordx4 v252, s[40:43], s59 offen offset:2048 nt lds\n"
    "v_mfma_f32_16x16x32_bf16 v[220:223], a[212:215], a[100:103], v[220:223]\n"
    "s_add_u32 m0, s56, 896\n"
    "ds_read_b128 a[192:195], v248 offset:16384\n"
    "ds_read_b128 a[196:199], v249 offset:16384\n"
    "v_mfma_f32_16x16x32_bf16 v[220:223], a[216:219], a[104:107], v[220:223]\n"
    "buffer_load_dwordx4 v252, s[40:43], s59 offen offset:128 nt lds\n"
    "v_mfma_f32_16x16x32_bf16 v[220:223], a[220:223], a[108:111], v[220:223]\n"
    "ds_read_b128 a[200:203], v248 offset:17408\n"
    "ds_read_b128 a[204:207], v249 offset:17408\n"
    "v_mfma_f32_16x16x32_bf16 v[96:99], a[224:227], v[236:239], v[96:99]\n"
    "buffer_load_dwordx4 v252, s[40:43], s59 offen offset:2176 nt lds\n"
    "v_mfma_f32_16x16x32_bf16 v[100:103], a[228:231], v[236:239], v[100:103]\n"
    "s_add_u32 m0, s56, 4096\n"
    "ds_read_b128 a[208:211], v248 offset:20480\n"
    "ds_read_b128 a[212:215], v249 offset:20480\n"
    "v_mfma_f32_16x16x32_bf16 v[104:107], a[232:235], v[236:239], v[104:107]\n"
    "s_add_u32 s59, s58, 8192\n"
    "buffer_load_dwordx4 v252, s[40:43], s60 offen offset:0 nt lds\n"
    "v_mfma_f32_16x16x32_bf16 v[108:111], a[236:239], v[236:239], v[108:111]\n"
    "v_add_f32 v245, v245, v224\n"
    "v_add_f32 v245, v245, v225\n"
    "v_mfma_f32_16x16x32_bf16 v[112:115], a[240:243], v[236:239], v[112:115]\n"
    "v_add_f32 v245, v245, v226\n"
    "v_add_f32 v245, v245, v227\n"
    "v_mfma_f32_16x16x32_bf16 v[116:119], a[244:247], v[236:239], v[116:119]\n"
    "ds_read_b128 a[216:219], v248 offset:21504\n"
    "ds_read_b128 a[220:223], v249 offset:21504\n"
    "v_mfma_f32_16x16x32_bf16 v[120:123], a[248:251], v[236:239], v[120:123]\n"
    "buffer_load_dwordx4 v252, s[40:43], s60 offen offset:2048 nt lds\n"
    "v_mfma_f32_16x16x32_bf16 v[124:127], a[252:255], v[236:239], v[124:127]\n"
    "s_waitcnt lgkmcnt(7)\n"
    "s_add_u32 m0, s56, 4992\n"
    "v_add_f32 v245, v245, v228\n"
    "v_add_f32 v245, v245, v229\n"
    "v_mfma_f32_16x16x32_bf16 v[224:227], a[192:195], a[0:3], 0\n"
    "s_waitcnt lgkmcnt(6)\n"
    "v_add_f32 v245, v245, v230\n"
    "v_add_f32 v245, v245, v231\n"
    "v_mfma_f32_16x16x32_bf16 v[224:227], a[196:199], a[4:7], v[224:227]\n"
    "s_waitcnt lgkmcnt(5)\n"
    "buffer_load_dwordx4 v252, s[40:43], s60 offen offset:128 nt lds\n"
    "v_mfma_f32_16x16x32_bf16 v[224:227], a[200:203], a[8:11], v[224:227]\n"
    "s_waitcnt lgkmcnt(4)\n"
    "v_exp_f32 v216, v216\n"
    "v_mfma_f32_16x16x32_bf16 v[224:227], a[204:207], a[12:15], v[224:227]\n"
    "s_waitcnt lgkmcnt(3)\n"
    "v_exp_f32 v217, v217\n"
    "v_mfma_f32_16x16x32_bf16 v[228:231], a[208:211], a[0:3], 0\n"
    "s_waitcnt lgkmcnt(2)\n"
    "v_exp_f32 v218, v218\n"
    "v_mfma_f32_16x16x32_bf16 v[228:231], a[212:215], a[4:7], v[228:231]\n"
    "s_waitcnt lgkmcnt(1)\n"
    "v_exp_f32 v219, v219\n"
    "v_exp_f32 v220, v220\n"
    "v_exp_f32 v221, v221\n"
    "v_exp_f32 v222, v222\n"
    "v_exp_f32 v223, v223\n"
    "v_mfma_f32_16x16x32_bf16 v[228:231], a[216:219], a[8:11], v[228:231]\n"
    "s_waitcnt lgkmcnt(0)\n"
    "v_cvt_pk_bf16_f32 v232, v216, v217\n"
    "v_cvt_pk_bf16_f32 v233, v218, v219\n"
    "v_cvt_pk_bf16_f32 v234, v220, v221\n"
    "v_cvt_pk_bf16_f32 v235, v222, v223\n"
    "v_mfma_f32_16x16x32_bf16 v[228:231], a[220:223], a[12:15], v[228:231]\n"
    "buffer_load_dwordx4 v252, s[40:43], s60 offen offset:2176 nt lds\n"
    "v_mfma_f32_16x16x32_bf16 v[128:131], a[224:227], v[232:235], v[128:131]\n"
    "s_add_u32 m0, s56, 24576\n"
    "ds_read_b64_tr_b16 v[196:197], v250 offset:16392\n"
    "ds_read_b64_tr_b16 v[198:199], v250 offset:20488\n"
    "v_mfma_f32_16x16x32_bf16 v[132:135], a[228:231], v[232:235], v[132:135]\n"
    "s_add_u32 s60, s58, 12288\n"
    "buffer_load_dwordx4 v252, s[44:47], s59 offen offset:0 nt lds\n"
    "v_mfma_f32_16x16x32_bf16 v[136:139], a[232:235], v[232:235], v[136:139]\n"
    "ds_read_b64_tr_b16 v[200:201], v251 offset:16384\n"
    "ds_read_b64_tr_b16 v[202:203], v251 offset:20480\n"
    "v_mfma_f32_16x16x32_bf16 v[140:143], a[236:239], v[232:235], v[140:143]\n"
    "buffer_load_dwordx4 v252, s[44:47], s59 offen offset:2048 nt lds\n"
    "v_mfma_f32_16x16x32_bf16 v[144:147], a[240:243], v[232:235], v[144:147]\n"
    "s_add_u32 m0, s56, 25472\n"
    "ds_read_b64_tr_b16 v[204:205], v251 offset:16392\n"
    "ds_read_b64_tr_b16 v[206:207], v251 offset:20488\n"
    "v_mfma_f32_16x16x32_bf16 v[148:151], a[244:247], v[232:235], v[148:151]\n"
    "ds_read_b64_tr_b16 v[208:209], v250 offset:17408\n"
    "ds_read_b64_tr_b16 v[210:211], v250 offset:21504\n"
    "v_mfma_f32_16x16x32_bf16 v[152:155], a[248:251], v[232:235], v[152:155]\n"
    "v_exp_f32 v224, v224\n"
    "v_mfma_f32_16x16x32_bf16 v[156:159], a[252:255], v[232:235], v[156:159]\n"
    "v_exp_f32 v225, v225\n"
    "v_mfma_f32_16x16x32_bf16 v[188:191], a[112:115], v[232:235], v[188:191]\n"
    "v_exp_f32 v226, v226\n"
    "v_mfma_f32_16x16x32_bf16 v[216:219], a[192:195], a[16:19], 0\n"
    "v_exp_f32 v227, v227\n"
    "v_mfma_f32_16x16x32_bf16 v[216:219], a[196:199], a[20:23], v[216:219]\n"
    "v_exp_f32 v228, v228\n"
    "v_mfma_f32_16x16x32_bf16 v[216:219], a[200:203], a[24:27], v[216:219]\n"
    "v_exp_f32 v229, v229\n"
    "v_mfma_f32_16x16x32_bf16 v[216:219], a[204:207], a[28:31], v[216:219]\n"
    "v_exp_f32 v230, v230\n"
    "v_mfma_f32_16x16x32_bf16 v[220:223], a[208:211], a[16:19], 0\n"
    "v_exp_f32 v231, v231\n"
    "v_mfma_f32_16x16x32_bf16 v[220:223], a[212:215], a[20:23], v[220:223]\n"
    "ds_read_b64_tr_b16 v[212:213], v250 offset:17416\n"
    "ds_read_b64_tr_b16 v[214:215], v250 offset:21512\n"
    "v_mfma_f32_16x16x32_bf16 v[220:223], a[216:219], a[24:27], v[220:223]\n"
    "v_cvt_pk_bf16_f32 v236, v224, v225\n"
    "v_cvt_pk_bf16_f32 v237, v226, v227\n"
    "v_cvt_pk_bf16_f32 v238, v228, v229\n"
    "v_cvt_pk_bf16_f32 v239, v230, v231\n"
    "v_mfma_f32_16x16x32_bf16 v[220:223], a[220:223], a[28:31], v[220:223]\n"
    "buffer_load_dwordx4 v252, s[44:47], s59 offen offset:128 nt lds\n"
    "ds_read_b64_tr_b16 a[248:249], v251 offset:17408\n"
    "ds_read_b64_tr_b16 a[250:251], v251 offset:21504\n"
    "v_mfma_f32_16x16x32_bf16 a[128:131], v[192:195], v[236:239], a[128:131]\n"
    "s_waitcnt lgkmcnt(10)\n"
    "ds_read_b64_tr_b16 a[252:253], v251 offset:17416\n"
    "ds_read_b64_tr_b16 a[254:255], v251 offset:21512\n"
    "v_mfma_f32_16x16x32_bf16 a[132:135], v[196:199], v[236:239], a[132:135]\n"
    "s_waitcnt lgkmcnt(10)\n"
    "buffer_load_dwordx4 v252, s[44:47], s59 offen offset:2176 nt lds\n"
    "v_mfma_f32_16x16x32_bf16 a[136:139], v[200:203], v[236:239], a[136:139]\n"
    "s_waitcnt lgkmcnt(8)\n"
    "s_add_u32 m0, s56, 28672\n"
    "v_add_f32 v240, v240, v224\n"
    "v_add_f32 v240, v240, v225\n"
    "v_mfma_f32_16x16x32_bf16 a[140:143], v[204:207], v[236:239], a[140:143]\n"
    "s_waitcnt lgkmcnt(6)\n"
    "s_add_u32 s59, s58, 8192\n"
    "v_add_f32 v240, v240, v226\n"
    "v_add_f32 v240, v240, v227\n"
    "v_mfma_f32_16x16x32_bf16 a[144:147], v[208:211], v[236:239], a[144:147]\n"
    "s_waitcnt lgkmcnt(4)\n"
    "v_add_f32 v240, v240, v228\n"
    "v_add_f32 v240, v240, v229\n"
    "v_mfma_f32_16x16x32_bf16 a[148:151], v[212:215], v[236:239], a[148:151]\n"
    "s_waitcnt lgkmcnt(2)\n"
    "v_add_f32 v240, v240, v230\n"
    "v_add_f32 v240, v240, v231\n"
    "v_mfma_f32_16x16x32_bf16 a[152:155], a[248:251], v[236:239], a[152:155]\n"
    "s_waitcnt lgkmcnt(0)\n"
    "buffer_load_dwordx4 v252, s[44:47], s60 offen offset:0 nt lds\n"
    "v_mfma_f32_16x16x32_bf16 a[156:159], a[252:255], v[236:239], a[156:159]\n"
    "v_exp_f32 v216, v216\n"
    "v_mfma_f32_16x16x32_bf16 v[224:227], a[192:195], a[32:35], 0\n"
    "v_exp_f32 v217, v217\n"
    "v_mfma_f32_16x16x32_bf16 v[224:227], a[196:199], a[36:39], v[224:227]\n"
    "v_exp_f32 v218, v218\n"
    "v_mfma_f32_16x16x32_bf16 v[224:227], a[200:203], a[40:43], v[224:227]\n"
    "v_exp_f32 v219, v219\n"
    "v_mfma_f32_16x16x32_bf16 v[224:227], a[204:207], a[44:47], v[224:227]\n"
    "v_exp_f32 v220, v220\n"
    "v_mfma_f32_16x16x32_bf16 v[228:231], a[208:211], a[32:35], 0\n"
    "v_exp_f32 v221, v221\n"
    "v_mfma_f32_16x16x32_bf16 v[228:231], a[212:215], a[36:39], v[228:231]\n"
    "v_exp_f32 v222, v222\n"
    "v_exp_f32 v223, v223\n"
    "v_mfma_f32_16x16x32_bf16 v[228:231], a[216:219], a[40:43], v[228:231]\n"
    "v_cvt_pk_bf16_f32 v232, v216, v217\n"
    "v_cvt_pk_bf16_f32 v233, v218, v219\n"
    "v_cvt_pk_bf16_f32 v234, v220, v221\n"
    "v_cvt_pk_bf16_f32 v235, v222, v223\n"
    "v_mfma_f32_16x16x32_bf16 v[228:231], a[220:223], a[44:47], v[228:231]\n"
    "buffer_load_dwordx4 v252, s[44:47], s60 offen offset:2048 nt lds\n"
    "v_mfma_f32_16x16x32_bf16 a[160:163], v[192:195], v[232:235], a[160:163]\n"
    "s_add_u32 m0, s56, 29568\n"
    "v_add_f32 v241, v241, v216\n"
    "v_add_f32 v241, v241, v217\n"
    "v_mfma_f32_16x16x32_bf16 a[164:167], v[196:199], v[232:235], a[164:167]\n"
    "v_add_f32 v241, v241, v218\n"
    "v_add_f32 v241, v241, v219\n"
    "v_mfma_f32_16x16x32_bf16 a[168:171], v[200:203], v[232:235], a[168:171]\n"
    "v_add_f32 v241, v241, v220\n"
    "v_add_f32 v241, v241, v221\n"
    "v_mfma_f32_16x16x32_bf16 a[172:175], v[204:207], v[232:235], a[172:175]\n"
    "buffer_load_dwordx4 v252, s[44:47], s60 offen offset:128 nt lds\n"
    "v_mfma_f32_16x16x32_bf16 a[176:179], v[208:211], v[232:235], a[176:179]\n"
    "v_add_f32 v241, v241, v222\n"
    "v_add_f32 v241, v241, v223\n"
    "v_mfma_f32_16x16x32_bf16 a[180:183], v[212:215], v[232:235], a[180:183]\n"
    "v_exp_f32 v224, v224\n"
    "v_mfma_f32_16x16x32_bf16 a[184:187], a[248:251], v[232:235], a[184:187]\n"
    "v_exp_f32 v225, v225\n"
    "v_mfma_f32_16x16x32_bf16 a[188:191], a[252:255], v[232:235], a[188:191]\n"
    "v_exp_f32 v226, v226\n"
    "v_mfma_f32_16x16x32_bf16 v[216:219], a[192:195], a[48:51], 0\n"
    "v_exp_f32 v227, v227\n"
    "v_mfma_f32_16x16x32_bf16 v[216:219], a[196:199], a[52:55], v[216:219]\n"
    "v_exp_f32 v228, v228\n"
    "v_mfma_f32_16x16x32_bf16 v[216:219], a[200:203], a[56:59], v[216:219]\n"
    "v_exp_f32 v229, v229\n"
    "v_mfma_f32_16x16x32_bf16 v[216:219], a[204:207], a[60:63], v[216:219]\n"
    "v_exp_f32 v230, v230\n"
    "v_mfma_f32_16x16x32_bf16 v[220:223], a[208:211], a[48:51], 0\n"
    "v_exp_f32 v231, v231\n"
    "v_mfma_f32_16x16x32_bf16 v[220:223], a[212:215], a[52:55], v[220:223]\n"
    "v_cvt_pk_bf16_f32 v236, v224, v225\n"
    "v_cvt_pk_bf16_f32 v237, v226, v227\n"
    "v_mfma_f32_16x16x32_bf16 v[220:223], a[216:219], a[56:59], v[220:223]\n"
    "v_cvt_pk_bf16_f32 v238, v228, v229\n"
    "v_cvt_pk_bf16_f32 v239, v230, v231\n"
    "v_mfma_f32_16x16x32_bf16 v[220:223], a[220:223], a[60:63], v[220:223]\n"
    "buffer_load_dwordx4 v252, s[44:47], s60 offen offset:2176 nt lds\n"
    "v_mfma_f32_16x16x32_bf16 v[0:3], v[192:195], v[236:239], v[0:3]\n"
    "s_add_u32 m0, s56, 16384\n"
    "v_exp_f32 v216, v216\n"
    "v_mfma_f32_16x16x32_bf16 v[4:7], v[196:199], v[236:239], v[4:7]\n"
    "s_add_u32 s60, s58, 12288\n"
    "buffer_load_dwordx4 v252, s[40:43], s59 offen offset:0 nt lds\n"
    "v_mfma_f32_16x16x32_bf16 v[8:11], v[200:203], v[236:239], v[8:11]\n"
    "s_add_u32 s58, s58, 16384\n"
    "v_exp_f32 v217, v217\n"
    "v_mfma_f32_16x16x32_bf16 v[12:15], v[204:207], v[236:239], v[12:15]\n"
    "v_exp_f32 v218, v218\n"
    "v_mfma_f32_16x16x32_bf16 v[16:19], v[208:211], v[236:239], v[16:19]\n"
    "v_exp_f32 v219, v219\n"
    "v_mfma_f32_16x16x32_bf16 v[20:23], v[212:215], v[236:239], v[20:23]\n"
    "v_exp_f32 v220, v220\n"
    "v_mfma_f32_16x16x32_bf16 v[24:27], a[248:251], v[236:239], v[24:27]\n"
    "v_exp_f32 v221, v221\n"
    "v_mfma_f32_16x16x32_bf16 v[28:31], a[252:255], v[236:239], v[28:31]\n"
    "buffer_load_dwordx4 v252, s[40:43], s59 offen offset:2048 nt lds\n"
    "v_mfma_f32_16x16x32_bf16 v[180:183], a[112:115], v[236:239], v[180:183]\n"
    "s_add_u32 m0, s56, 17280\n"
    "v_exp_f32 v222, v222\n"
    "v_mfma_f32_16x16x32_bf16 v[224:227], a[192:195], a[64:67], 0\n"
    "v_exp_f32 v223, v223\n"
    "v_mfma_f32_16x16x32_bf16 v[224:227], a[196:199], a[68:71], v[224:227]\n"
    "v_cvt_pk_bf16_f32 v232, v216, v217\n"
    "v_cvt_pk_bf16_f32 v233, v218, v219\n"
    "v_mfma_f32_16x16x32_bf16 v[224:227], a[200:203], a[72:75], v[224:227]\n"
    "v_cvt_pk_bf16_f32 v234, v220, v221\n"
    "v_cvt_pk_bf16_f32 v235, v222, v223\n"
    "v_mfma_f32_16x16x32_bf16 v[224:227], a[204:207], a[76:79], v[224:227]\n"
    "v_add_f32 v243, v243, v216\n"
    "v_add_f32 v243, v243, v217\n"
    "v_mfma_f32_16x16x32_bf16 v[228:231], a[208:211], a[64:67], 0\n"
    "v_add_f32 v243, v243, v218\n"
    "v_add_f32 v243, v243, v219\n"
    "v_mfma_f32_16x16x32_bf16 v[228:231], a[212:215], a[68:71], v[228:231]\n"
    "buffer_load_dwordx4 v252, s[40:43], s59 offen offset:128 nt lds\n"
    "v_mfma_f32_16x16x32_bf16 v[228:231], a[216:219], a[72:75], v[228:231]\n"
    "v_add_f32 v243, v243, v220\n"
    "v_add_f32 v243, v243, v221\n"
    "v_mfma_f32_16x16x32_bf16 v[228:231], a[220:223], a[76:79], v[228:231]\n"
    "v_add_f32 v243, v243, v222\n"
    "v_add_f32 v243, v243, v223\n"
    "v_mfma_f32_16x16x32_bf16 v[32:35], v[192:195], v[232:235], v[32:35]\n"
    "v_exp_f32 v224, v224\n"
    "v_mfma_f32_16x16x32_bf16 v[36:39], v[196:199], v[232:235], v[36:39]\n"
    "v_exp_f32 v225, v225\n"
    "v_mfma_f32_16x16x32_bf16 v[40:43], v[200:203], v[232:235], v[40:43]\n"
    "v_exp_f32 v226, v226\n"
    "v_mfma_f32_16x16x32_bf16 v[44:47], v[204:207], v[232:235], v[44:47]\n"
    "buffer_load_dwordx4 v252, s[40:43], s59 offen offset:2176 nt lds\n"
    "v_mfma_f32_16x16x32_bf16 v[48:51], v[208:211], v[232:235], v[48:51]\n"
    "s_add_u32 m0, s56, 20480\n"
    "v_exp_f32 v227, v227\n"
    "v_mfma_f32_16x16x32_bf16 v[52:55], v[212:215], v[232:235], v[52:55]\n"
    "v_exp_f32 v228, v228\n"
    "v_mfma_f32_16x16x32_bf16 v[56:59], a[248:251], v[232:235], v[56:59]\n"
    "v_exp_f32 v229, v229\n"
    "v_mfma_f32_16x16x32_bf16 v[60:63], a[252:255], v[232:235], v[60:63]\n"
    "v_exp_f32 v230, v230\n"
    "v_mfma_f32_16x16x32_bf16 v[216:219], a[192:195], a[80:83], 0\n"
    "v_exp_f32 v231, v231\n"
    "v_mfma_f32_16x16x32_bf16 v[216:219], a[196:199], a[84:87], v[216:219]\n"
    "v_cvt_pk_bf16_f32 v236, v224, v225\n"
    "v_cvt_pk_bf16_f32 v237, v226, v227\n"
    "v_mfma_f32_16x16x32_bf16 v[216:219], a[200:203], a[88:91], v[216:219]\n"
    "v_cvt_pk_bf16_f32 v238, v228, v229\n"
    "v_cvt_pk_bf16_f32 v239, v230, v231\n"
    "v_mfma_f32_16x16x32_bf16 v[216:219], a[204:207], a[92:95], v[216:219]\n"
    "buffer_load_dwordx4 v252, s[40:43], s60 offen offset:0 nt lds\n"
    "v_mfma_f32_16x16x32_bf16 v[220:223], a[208:211], a[80:83], 0\n"
    "v_xor_b32 v248, s55, v248\n"
    "v_xor_b32 v249, s55, v249\n"
    "v_mfma_f32_16x16x32_bf16 v[220:223], a[212:215], a[84:87], v[220:223]\n"
    "v_xor_b32 v250, s55, v250\n"
    "v_xor_b32 v251, s55, v251\n"
    "v_mfma_f32_16x16x32_bf16 v[220:223], a[216:219], a[88:91], v[220:223]\n"
    "s_xor_b32 s55, s55, 0x18000\n"
    "v_exp_f32 v216, v216\n"
    "v_mfma_f32_16x16x32_bf16 v[220:223], a[220:223], a[92:95], v[220:223]\n"
    "v_exp_f32 v217, v217\n"
    "v_mfma_f32_16x16x32_bf16 v[64:67], v[192:195], v[236:239], v[64:67]\n"
    "v_exp_f32 v218, v218\n"
    "v_mfma_f32_16x16x32_bf16 v[68:71], v[196:199], v[236:239], v[68:71]\n"
    "buffer_load_dwordx4 v252, s[40:43], s60 offen offset:2048 nt lds\n"
    "v_mfma_f32_16x16x32_bf16 v[72:75], v[200:203], v[236:239], v[72:75]\n"
    "s_add_u32 m0, s56, 21376\n"
    "v_exp_f32 v219, v219\n"
    "v_mfma_f32_16x16x32_bf16 v[76:79], v[204:207], v[236:239], v[76:79]\n"
    "s_xor_b32 s56, s56, s57\n"
    "v_exp_f32 v220, v220\n"
    "v_mfma_f32_16x16x32_bf16 v[80:83], v[208:211], v[236:239], v[80:83]\n"
    "s_xor_b32 s57, s57, 0x18000\n"
    "v_exp_f32 v221, v221\n"
    "v_mfma_f32_16x16x32_bf16 v[84:87], v[212:215], v[236:239], v[84:87]\n"
    "v_exp_f32 v222, v222\n"
    "v_mfma_f32_16x16x32_bf16 v[88:91], a[248:251], v[236:239], v[88:91]\n"
    "v_exp_f32 v223, v223\n"
    "v_mfma_f32_16x16x32_bf16 v[92:95], a[252:255], v[236:239], v[92:95]\n"
    "buffer_load_dwordx4 v252, s[40:43], s60 offen offset:128 nt lds\n"
    "v_mfma_f32_16x16x32_bf16 v[184:187], a[112:115], v[236:239], v[184:187]\n"
    "v_cvt_pk_bf16_f32 v232, v216, v217\n"
    "v_cvt_pk_bf16_f32 v233, v218, v219\n"
    "v_mfma_f32_16x16x32_bf16 v[224:227], a[192:195], a[96:99], 0\n"
    "v_cvt_pk_bf16_f32 v234, v220, v221\n"
    "v_cvt_pk_bf16_f32 v235, v222, v223\n"
    "v_mfma_f32_16x16x32_bf16 v[224:227], a[196:199], a[100:103], v[224:227]\n"
    "ds_read_b128 a[192:195], v248 offset:0\n"
    "ds_read_b128 a[196:199], v249 offset:0\n"
    "v_mfma_f32_16x16x32_bf16 v[224:227], a[200:203], a[104:107], v[224:227]\n"
    "ds_read_b128 a[200:203], v248 offset:1024\n"
    "v_add_f32 v245, v245, v216\n"
    "v_mfma_f32_16x16x32_bf16 v[224:227], a[204:207], a[108:111], v[224:227]\n"
    "ds_read_b128 a[204:207], v249 offset:1024\n"
    "v_add_f32 v245, v245, v217\n"
    "v_mfma_f32_16x16x32_bf16 v[228:231], a[208:211], a[96:99], 0\n"
    "ds_read_b128 a[208:211], v248 offset:4096\n"
    "v_add_f32 v245, v245, v218\n"
    "v_mfma_f32_16x16x32_bf16 v[228:231], a[212:215], a[100:103], v[228:231]\n"
    "buffer_load_dwordx4 v252, s[40:43], s60 offen offset:2176 nt lds\n"
    "v_mfma_f32_16x16x32_bf16 v[228:231], a[216:219], a[104:107], v[228:231]\n"
    "ds_read_b128 a[212:215], v249 offset:4096\n"
    "v_add_f32 v245, v245, v219\n"
    "v_mfma_f32_16x16x32_bf16 v[228:231], a[220:223], a[108:111], v[228:231]\n"
    "ds_read_b128 a[216:219], v248 offset:5120\n"
    "ds_read_b128 a[220:223], v249 offset:5120\n"
    "v_mfma_f32_16x16x32_bf16 v[96:99], v[192:195], v[232:235], v[96:99]\n"
    "v_add_f32 v245, v245, v220\n"
    "v_add_f32 v245, v245, v221\n"
    "v_mfma_f32_16x16x32_bf16 v[100:103], v[196:199], v[232:235], v[100:103]\n"
    "v_add_f32 v245, v245, v222\n"
    "v_add_f32 v245, v245, v223\n"
    "v_mfma_f32_16x16x32_bf16 v[104:107], v[200:203], v[232:235], v[104:107]\n"
    "v_exp_f32 v224, v224\n"
    "v_mfma_f32_16x16x32_bf16 v[108:111], v[204:207], v[232:235], v[108:111]\n"
    "v_exp_f32 v225, v225\n"
    "v_mfma_f32_16x16x32_bf16 v[112:115], v[208:211], v[232:235], v[112:115]\n"
    "s_sub_u32 s54, s54, 1\n"
    "v_exp_f32 v226, v226\n"
    "v_mfma_f32_16x16x32_bf16 v[116:119], v[212:215], v[232:235], v[116:119]\n"
    "v_exp_f32 v227, v227\n"
    "v_mfma_f32_16x16x32_bf16 v[120:123], a[248:251], v[232:235], v[120:123]\n"
    "s_cmp_lg_u32 s54, 0\n"
    "v_exp_f32 v228, v228\n"
    "v_mfma_f32_16x16x32_bf16 v[124:127], a[252:255], v[232:235], v[124:127]\n"
    "s_waitcnt lgkmcnt(7)\n"
    "s_cbranch_scc1 LB_%=\n"
    "v_mfma_f32_16x16x32_bf16 v[216:219], a[192:195], a[32:35], 0\n"
    "s_waitcnt lgkmcnt(6)\n"
    "s_add_u32 s59, s67, 32768\n"
    "v_exp_f32 v229, v229\n"
    "v_mfma_f32_16x16x32_bf16 v[216:219], a[196:199], a[36:39], v[216:219]\n"
    "s_waitcnt lgkmcnt(5)\n"
    "s_add_u32 s60, s67, 49152\n"
    "v_exp_f32 v230, v230\n"
    "v_mfma_f32_16x16x32_bf16 v[216:219], a[200:203], a[40:43], v[216:219]\n"
    "s_waitcnt lgkmcnt(4)\n"
    "v_exp_f32 v231, v231\n"
    "v_mfma_f32_16x16x32_bf16 v[216:219], a[204:207], a[44:47], v[216:219]\n"
    "s_waitcnt lgkmcnt(3)\n"
    "v_cvt_pk_bf16_f32 v236, v224, v225\n"
    "v_cvt_pk_bf16_f32 v237, v226, v227\n"
    "v_mfma_f32_16x16x32_bf16 v[220:223], a[208:211], a[32:35], 0\n"
    "s_waitcnt lgkmcnt(2)\n"
    "v_cvt_pk_bf16_f32 v238, v228, v229\n"
    "v_cvt_pk_bf16_f32 v239, v230, v231\n"
    "v_mfma_f32_16x16x32_bf16 v[220:223], a[212:215], a[36:39], v[220:223]\n"
    "s_waitcnt lgkmcnt(1)\n"
    "ds_read_b64_tr_b16 a[224:225], v250 offset:0\n"
    "ds_read_b64_tr_b16 a[226:227], v250 offset:4096\n"
    "v_mfma_f32_16x16x32_bf16 v[220:223], a[216:219], a[40:43], v[220:223]\n"
    "s_waitcnt lgkmcnt(2)\n"
    "ds_read_b64_tr_b16 a[228:229], v250 offset:8\n"
    "ds_read_b64_tr_b16 a[230:231], v250 offset:4104\n"
    "v_mfma_f32_16x16x32_bf16 v[220:223], a[220:223], a[44:47], v[220:223]\n"
    "ds_read_b64_tr_b16 a[232:233], v251 offset:0\n"
    "ds_read_b64_tr_b16 a[234:235], v251 offset:4096\n"
    "v_mfma_f32_16x16x32_bf16 v[128:131], v[192:195], v[236:239], v[128:131]\n"
    "ds_read_b64_tr_b16 a[236:237], v251 offset:8\n"
    "ds_read_b64_tr_b16 a[238:239], v251 offset:4104\n"
    "v_mfma_f32_16x16x32_bf16 v[132:135], v[196:199], v[236:239], v[132:135]\n"
    "ds_read_b64_tr_b16 a[240:241], v250 offset:1024\n"
    "ds_read_b64_tr_b16 a[242:243], v250 offset:5120\n"
    "v_mfma_f32_16x16x32_bf16 v[136:139], v[200:203], v[236:239], v[136:139]\n"
    "v_exp_f32 v216, v216\n"
    "v_mfma_f32_16x16x32_bf16 v[140:143], v[204:207], v[236:239], v[140:143]\n"
    "v_exp_f32 v217, v217\n"
    "v_mfma_f32_16x16x32_bf16 v[144:147], v[208:211], v[236:239], v[144:147]\n"
    "v_exp_f32 v218, v218\n"
    "v_mfma_f32_16x16x32_bf16 v[148:151], v[212:215], v[236:239], v[148:151]\n"
    "v_exp_f32 v219, v219\n"
    "v_mfma_f32_16x16x32_bf16 v[152:155], a[248:251], v[236:239], v[152:155]\n"
    "v_exp_f32 v220, v220\n"
    "v_mfma_f32_16x16x32_bf16 v[156:159], a[252:255], v[236:239], v[156:159]\n"
    "v_exp_f32 v221, v221\n"
    "v_mfma_f32_16x16x32_bf16 v[188:191], a[112:115], v[236:239], v[188:191]\n"
    "v_exp_f32 v222, v222\n"
    "v_mfma_f32_16x16x32_bf16 v[224:227], a[192:195], a[48:51], 0\n"
    "v_exp_f32 v223, v223\n"
    "v_mfma_f32_16x16x32_bf16 v[224:227], a[196:199], a[52:55], v[224:227]\n"
    "ds_read_b64_tr_b16 a[244:245], v250 offset:1032\n"
    "ds_read_b64_tr_b16 a[246:247], v250 offset:5128\n"
    "v_mfma_f32_16x16x32_bf16 v[224:227], a[200:203], a[56:59], v[224:227]\n"
    "v_cvt_pk_bf16_f32 v232, v216, v217\n"
    "v_cvt_pk_bf16_f32 v233, v218, v219\n"
    "v_mfma_f32_16x16x32_bf16 v[224:227], a[204:207], a[60:63], v[224:227]\n"
    "v_cvt_pk_bf16_f32 v234, v220, v221\n"
    "v_cvt_pk_bf16_f32 v235, v222, v223\n"
    "v_mfma_f32_16x16x32_bf16 v[228:231], a[208:211], a[48:51], 0\n"
    "ds_read_b64_tr_b16 a[248:249], v251 offset:1024\n"
    "ds_read_b64_tr_b16 a[250:251], v251 offset:5120\n"
    "v_mfma_f32_16x16x32_bf16 v[228:231], a[212:215], a[52:55], v[228:231]\n"
    "ds_read_b64_tr_b16 a[252:253], v251 offset:1032\n"
    "ds_read_b64_tr_b16 a[254:255], v251 offset:5128\n"
    "v_mfma_f32_16x16x32_bf16 v[228:231], a[216:219], a[56:59], v[228:231]\n"
    "v_exp_f32 v224, v224\n"
    "v_mfma_f32_16x16x32_bf16 v[228:231], a[220:223], a[60:63], v[228:231]\n"
    "s_waitcnt lgkmcnt(14)\n"
    "v_exp_f32 v225, v225\n"
    "v_mfma_f32_16x16x32_bf16 v[0:3], a[224:227], v[232:235], v[0:3]\n"
    "s_waitcnt lgkmcnt(12)\n"
    "v_exp_f32 v226, v226\n"
    "v_mfma_f32_16x16x32_bf16 v[4:7], a[228:231], v[232:235], v[4:7]\n"
    "s_waitcnt lgkmcnt(10)\n"
    "v_exp_f32 v227, v227\n"
    "v_mfma_f32_16x16x32_bf16 v[8:11], a[232:235], v[232:235], v[8:11]\n"
    "s_waitcnt lgkmcnt(8)\n"
    "v_exp_f32 v228, v228\n"
    "v_mfma_f32_16x16x32_bf16 v[12:15], a[236:239], v[232:235], v[12:15]\n"
    "s_waitcnt lgkmcnt(6)\n"
    "v_exp_f32 v229, v229\n"
    "v_mfma_f32_16x16x32_bf16 v[16:19], a[240:243], v[232:235], v[16:19]\n"
    "s_waitcnt lgkmcnt(4)\n"
    "v_exp_f32 v230, v230\n"
    "v_mfma_f32_16x16x32_bf16 v[20:23], a[244:247], v[232:235], v[20:23]\n"
    "s_waitcnt lgkmcnt(2)\n"
    "v_exp_f32 v231, v231\n"
    "v_mfma_f32_16x16x32_bf16 v[24:27], a[248:251], v[232:235], v[24:27]\n"
    "s_waitcnt lgkmcnt(0)\n"
    "v_cvt_pk_bf16_f32 v236, v224, v225\n"
    "v_cvt_pk_bf16_f32 v237, v226, v227\n"
    "v_mfma_f32_16x16x32_bf16 v[28:31], a[252:255], v[232:235], v[28:31]\n"
    "v_cvt_pk_bf16_f32 v238, v228, v229\n"
    "v_cvt_pk_bf16_f32 v239, v230, v231\n"
    "v_mfma_f32_16x16x32_bf16 v[180:183], a[112:115], v[232:235], v[180:183]\n"
    "v_add_f32 v243, v243, v224\n"
    "v_add_f32 v243, v243, v225\n"
    "v_mfma_f32_16x16x32_bf16 v[216:219], a[192:195], a[64:67], 0\n"
    "v_add_f32 v243, v243, v226\n"
    "v_add_f32 v243, v243, v227\n"
    "v_mfma_f32_16x16x32_bf16 v[216:219], a[196:199], a[68:71], v[216:219]\n"
    "v_add_f32 v243, v243, v228\n"
    "v_add_f32 v243, v243, v229\n"
    "v_mfma_f32_16x16x32_bf16 v[216:219], a[200:203], a[72:75], v[216:219]\n"
    "v_add_f32 v243, v243, v230\n"
    "v_add_f32 v243, v243, v231\n"
    "v_mfma_f32_16x16x32_bf16 v[216:219], a[204:207], a[76:79], v[216:219]\n"
    "v_mfma_f32_16x16x32_bf16 v[220:223], a[208:211], a[64:67], 0\n"
    "v_mfma_f32_16x16x32_bf16 v[220:223], a[212:215], a[68:71], v[220:223]\n"
    "v_mfma_f32_16x16x32_bf16 v[220:223], a[216:219], a[72:75], v[220:223]\n"
    "v_exp_f32 v216, v216\n"
    "v_mfma_f32_16x16x32_bf16 v[220:223], a[220:223], a[76:79], v[220:223]\n"
    "v_exp_f32 v217, v217\n"
    "v_mfma_f32_16x16x32_bf16 v[32:35], a[224:227], v[236:239], v[32:35]\n"
    "v_exp_f32 v218, v218\n"
    "v_mfma_f32_16x16x32_bf16 v[36:39], a[228:231], v[236:239], v[36:39]\n"
    "v_exp_f32 v219, v219\n"
    "v_mfma_f32_16x16x32_bf16 v[40:43], a[232:235], v[236:239], v[40:43]\n"
    "v_exp_f32 v220, v220\n"
    "v_mfma_f32_16x16x32_bf16 v[44:47], a[236:239], v[236:239], v[44:47]\n"
    "v_exp_f32 v221, v221\n"
    "v_mfma_f32_16x16x32_bf16 v[48:51], a[240:243], v[236:239], v[48:51]\n"
    "v_exp_f32 v222, v222\n"
    "v_mfma_f32_16x16x32_bf16 v[52:55], a[244:247], v[236:239], v[52:55]\n"
    "v_exp_f32 v223, v223\n"
    "v_mfma_f32_16x16x32_bf16 v[56:59], a[248:251], v[236:239], v[56:59]\n"
    "v_cvt_pk_bf16_f32 v232, v216, v217\n"
    "v_cvt_pk_bf16_f32 v233, v218, v219\n"
    "v_mfma_f32_16x16x32_bf16 v[60:63], a[252:255], v[236:239], v[60:63]\n"
    "v_cvt_pk_bf16_f32 v234, v220, v221\n"
    "v_cvt_pk_bf16_f32 v235, v222, v223\n"
    "v_mfma_f32_16x16x32_bf16 v[224:227], a[192:195], a[80:83], 0\n"
    "v_mfma_f32_16x16x32_bf16 v[224:227], a[196:199], a[84:87], v[224:227]\n"
    "v_mfma_f32_16x16x32_bf16 v[224:227], a[200:203], a[88:91], v[224:227]\n"
    "v_mfma_f32_16x16x32_bf16 v[224:227], a[204:207], a[92:95], v[224:227]\n"
    "v_mfma_f32_16x16x32_bf16 v[228:231], a[208:211], a[80:83], 0\n"
    "v_mfma_f32_16x16x32_bf16 v[228:231], a[212:215], a[84:87], v[228:231]\n"
    "v_mfma_f32_16x16x32_bf16 v[228:231], a[216:219], a[88:91], v[228:231]\n"
    "v_exp_f32 v224, v224\n"
    "v_mfma_f32_16x16x32_bf16 v[228:231], a[220:223], a[92:95], v[228:231]\n"
    "v_exp_f32 v225, v225\n"
    "v_mfma_f32_16x16x32_bf16 v[64:67], a[224:227], v[232:235], v[64:67]\n"
    "s_waitcnt vmcnt(16)\n"
    "s_barrier\n"
    "ds_read_b128 a[192:195], v248 offset:16384\n"
    "ds_read_b128 a[196:199], v249 offset:16384\n"
    "v_mfma_f32_16x16x32_bf16 v[68:71], a[228:231], v[232:235], v[68:71]\n"
    "ds_read_b128 a[200:203], v248 offset:17408\n"
    "ds_read_b128 a[204:207], v249 offset:17408\n"
    "v_mfma_f32_16x16x32_bf16 v[72:75], a[232:235], v[232:235], v[72:75]\n"
    "ds_read_b128 a[208:211], v248 offset:20480\n"
    "ds_read_b128 a[212:215], v249 offset:20480\n"
    "v_mfma_f32_16x16x32_bf16 v[76:79], a[236:239], v[232:235], v[76:79]\n"
    "ds_read_b128 a[216:219], v248 offset:21504\n"
    "ds_read_b128 a[220:223], v249 offset:21504\n"
    "v_mfma_f32_16x16x32_bf16 v[80:83], a[240:243], v[232:235], v[80:83]\n"
    "v_exp_f32 v226, v226\n"
    "v_mfma_f32_16x16x32_bf16 v[84:87], a[244:247], v[232:235], v[84:87]\n"
    "v_exp_f32 v227, v227\n"
    "v_mfma_f32_16x16x32_bf16 v[88:91], a[248:251], v[232:235], v[88:91]\n"
    "v_exp_f32 v228, v228\n"
    "v_mfma_f32_16x16x32_bf16 v[92:95], a[252:255], v[232:235], v[92:95]\n"
    "v_exp_f32 v229, v229\n"
    "v_mfma_f32_16x16x32_bf16 v[184:187], a[112:115], v[232:235], v[184:187]\n"
    "s_waitcnt lgkmcnt(7)\n"
    "v_exp_f32 v230, v230\n"
    "v_mfma_f32_16x16x32_bf16 v[216:219], a[192:195], a[32:35], 0\n"
    "s_waitcnt lgkmcnt(6)\n"
    "v_exp_f32 v231, v231\n"
    "v_mfma_f32_16x16x32_bf16 v[216:219], a[196:199], a[36:39], v[216:219]\n"
    "s_waitcnt lgkmcnt(5)\n"
    "v_cvt_pk_bf16_f32 v236, v224, v225\n"
    "v_cvt_pk_bf16_f32 v237, v226, v227\n"
    "v_mfma_f32_16x16x32_bf16 v[216:219], a[200:203], a[40:43], v[216:219]\n"
    "s_waitcnt lgkmcnt(4)\n"
    "v_cvt_pk_bf16_f32 v238, v228, v229\n"
    "v_cvt_pk_bf16_f32 v239, v230, v231\n"
    "v_mfma_f32_16x16x32_bf16 v[216:219], a[204:207], a[44:47], v[216:219]\n"
    "s_waitcnt lgkmcnt(3)\n"
    "v_add_f32 v245, v245, v224\n"
    "v_add_f32 v245, v245, v225\n"
    "v_mfma_f32_16x16x32_bf16 v[220:223], a[208:211], a[32:35], 0\n"
    "s_waitcnt lgkmcnt(2)\n"
    "v_add_f32 v245, v245, v226\n"
    "v_add_f32 v245, v245, v227\n"
    "v_mfma_f32_16x16x32_bf16 v[220:223], a[212:215], a[36:39], v[220:223]\n"
    "s_waitcnt lgkmcnt(1)\n"
    "ds_read_b64_tr_b16 v[192:193], v250 offset:16384\n"
    "ds_read_b64_tr_b16 v[194:195], v250 offset:20480\n"
    "v_mfma_f32_16x16x32_bf16 v[220:223], a[216:219], a[40:43], v[220:223]\n"
    "s_waitcnt lgkmcnt(2)\n"
    "ds_read_b64_tr_b16 v[196:197], v250 offset:16392\n"
    "ds_read_b64_tr_b16 v[198:199], v250 offset:20488\n"
    "v_mfma_f32_16x16x32_bf16 v[220:223], a[220:223], a[44:47], v[220:223]\n"
    "ds_read_b64_tr_b16 v[200:201], v251 offset:16384\n"
    "ds_read_b64_tr_b16 v[202:203], v251 offset:20480\n"
    "v_mfma_f32_16x16x32_bf16 v[96:99], a[224:227], v[236:239], v[96:99]\n"
    "v_add_f32 v245, v245, v228\n"
    "v_add_f32 v245, v245, v229\n"
    "v_mfma_f32_16x16x32_bf16 v[100:103], a[228:231], v[236:239], v[100:103]\n"
    "v_add_f32 v245, v245, v230\n"
    "v_add_f32 v245, v245, v231\n"
    "v_mfma_f32_16x16x32_bf16 v[104:107], a[232:235], v[236:239], v[104:107]\n"
    "ds_read_b64_tr_b16 v[204:205], v251 offset:16392\n"
    "ds_read_b64_tr_b16 v[206:207], v251 offset:20488\n"
    "v_mfma_f32_16x16x32_bf16 v[108:111], a[236:239], v[236:239], v[108:111]\n"
    "ds_read_b64_tr_b16 v[208:209], v250 offset:17408\n"
    "ds_read_b64_tr_b16 v[210:211], v250 offset:21504\n"
    "v_mfma_f32_16x16x32_bf16 v[112:115], a[240:243], v[236:239], v[112:115]\n"
    "v_exp_f32 v216, v216\n"
    "v_mfma_f32_16x16x32_bf16 v[116:119], a[244:247], v[236:239], v[116:119]\n"
    "v_exp_f32 v217, v217\n"
    "v_mfma_f32_16x16x32_bf16 v[120:123], a[248:251], v[236:239], v[120:123]\n"
    "v_exp_f32 v218, v218\n"
    "v_mfma_f32_16x16x32_bf16 v[124:127], a[252:255], v[236:239], v[124:127]\n"
    "v_exp_f32 v219, v219\n"
    "v_mfma_f32_16x16x32_bf16 v[224:227], a[192:195], a[48:51], 0\n"
    "v_exp_f32 v220, v220\n"
    "v_mfma_f32_16x16x32_bf16 v[224:227], a[196:199], a[52:55], v[224:227]\n"
    "v_exp_f32 v221, v221\n"
    "v_mfma_f32_16x16x32_bf16 v[224:227], a[200:203], a[56:59], v[224:227]\n"
    "v_exp_f32 v222, v222\n"
    "v_mfma_f32_16x16x32_bf16 v[224:227], a[204:207], a[60:63], v[224:227]\n"
    "v_exp_f32 v223, v223\n"
    "v_mfma_f32_16x16x32_bf16 v[228:231], a[208:211], a[48:51], 0\n"
    "ds_read_b64_tr_b16 v[212:213], v250 offset:17416\n"
    "ds_read_b64_tr_b16 v[214:215], v250 offset:21512\n"
    "v_mfma_f32_16x16x32_bf16 v[228:231], a[212:215], a[52:55], v[228:231]\n"
    "v_cvt_pk_bf16_f32 v232, v216, v217\n"
    "v_cvt_pk_bf16_f32 v233, v218, v219\n"
    "v_mfma_f32_16x16x32_bf16 v[228:231], a[216:219], a[56:59], v[228:231]\n"
    "v_cvt_pk_bf16_f32 v234, v220, v221\n"
    "v_cvt_pk_bf16_f32 v235, v222, v223\n"
    "v_mfma_f32_16x16x32_bf16 v[228:231], a[220:223], a[60:63], v[228:231]\n"
    "s_waitcnt lgkmcnt(10)\n"
    "ds_read_b64_tr_b16 a[248:249], v251 offset:17408\n"
    "ds_read_b64_tr_b16 a[250:251], v251 offset:21504\n"
    "v_mfma_f32_16x16x32_bf16 v[0:3], v[192:195], v[232:235], v[0:3]\n"
    "s_waitcnt lgkmcnt(10)\n"
    "ds_read_b64_tr_b16 a[252:253], v251 offset:17416\n"
    "ds_read_b64_tr_b16 a[254:255], v251 offset:21512\n"
    "v_mfma_f32_16x16x32_bf16 v[4:7], v[196:199], v[232:235], v[4:7]\n"
    "s_waitcnt lgkmcnt(10)\n"
    "v_exp_f32 v224, v224\n"
    "v_mfma_f32_16x16x32_bf16 v[8:11], v[200:203], v[232:235], v[8:11]\n"
    "s_waitcnt lgkmcnt(8)\n"
    "v_exp_f32 v225, v225\n"
    "v_mfma_f32_16x16x32_bf16 v[12:15], v[204:207], v[232:235], v[12:15]\n"
    "s_waitcnt lgkmcnt(6)\n"
    "v_exp_f32 v226, v226\n"
    "v_mfma_f32_16x16x32_bf16 v[16:19], v[208:211], v[232:235], v[16:19]\n"
    "s_waitcnt lgkmcnt(4)\n"
    "v_exp_f32 v227, v227\n"
    "v_mfma_f32_16x16x32_bf16 v[20:23], v[212:215], v[232:235], v[20:23]\n"
    "s_waitcnt lgkmcnt(2)\n"
    "v_exp_f32 v228, v228\n"
    "v_mfma_f32_16x16x32_bf16 v[24:27], a[248:251], v[232:235], v[24:27]\n"
    "s_waitcnt lgkmcnt(0)\n"
    "v_exp_f32 v229, v229\n"
    "v_mfma_f32_16x16x32_bf16 v[28:31], a[252:255], v[232:235], v[28:31]\n"
    "v_exp_f32 v230, v230\n"
    "v_mfma_f32_16x16x32_bf16 v[180:183], a[112:115], v[232:235], v[180:183]\n"
    "v_exp_f32 v231, v231\n"
    "v_mfma_f32_16x16x32_bf16 v[216:219], a[192:195], a[64:67], 0\n"
    "v_cvt_pk_bf16_f32 v236, v224, v225\n"
    "v_cvt_pk_bf16_f32 v237, v226, v227\n"
    "v_mfma_f32_16x16x32_bf16 v[216:219], a[196:199], a[68:71], v[216:219]\n"
    "v_cvt_pk_bf16_f32 v238, v228, v229\n"
    "v_cvt_pk_bf16_f32 v239, v230, v231\n"
    "v_mfma_f32_16x16x32_bf16 v[216:219], a[200:203], a[72:75], v[216:219]\n"
    "v_add_f32 v243, v243, v224\n"
    "v_add_f32 v243, v243, v225\n"
    "v_mfma_f32_16x16x32_bf16 v[216:219], a[204:207], a[76:79], v[216:219]\n"
    "v_add_f32 v243, v243, v226\n"
    "v_add_f32 v243, v243, v227\n"
    "v_mfma_f32_16x16x32_bf16 v[220:223], a[208:211], a[64:67], 0\n"
    "v_add_f32 v243, v243, v228\n"
    "v_add_f32 v243, v243, v229\n"
    "v_mfma_f32_16x16x32_bf16 v[220:223], a[212:215], a[68:71], v[220:223]\n"
    "v_add_f32 v243, v243, v230\n"
    "v_add_f32 v243, v243, v231\n"
    "v_mfma_f32_16x16x32_bf16 v[220:223], a[216:219], a[72:75], v[220:223]\n"
    "v_exp_f32 v216, v216\n"
    "v_mfma_f32_16x16x32_bf16 v[220:223], a[220:223], a[76:79], v[220:223]\n"
    "v_exp_f32 v217, v217\n"
    "v_mfma_f32_16x16x32_bf16 v[32:35], v[192:195], v[236:239], v[32:35]\n"
    "v_exp_f32 v218, v218\n"
    "v_mfma_f32_16x16x32_bf16 v[36:39], v[196:199], v[236:239], v[36:39]\n"
    "v_exp_f32 v219, v219\n"
    "v_mfma_f32_16x16x32_bf16 v[40:43], v[200:203], v[236:239], v[40:43]\n"
    "v_exp_f32 v220, v220\n"
    "v_mfma_f32_16x16x32_bf16 v[44:47], v[204:207], v[236:239], v[44:47]\n"
    "v_exp_f32 v221, v221\n"
    "v_mfma_f32_16x16x32_bf16 v[48:51], v[208:211], v[236:239], v[48:51]\n"
    "v_exp_f32 v222, v222\n"
    "v_mfma_f32_16x16x32_bf16 v[52:55], v[212:215], v[236:239], v[52:55]\n"
    "v_exp_f32 v223, v223\n"
    "v_mfma_f32_16x16x32_bf16 v[56:59], a[248:251], v[236:239], v[56:59]\n"
    "v_cvt_pk_bf16_f32 v232, v216, v217\n"
    "v_cvt_pk_bf16_f32 v233, v218, v219\n"
    "v_mfma_f32_16x16x32_bf16 v[60:63], a[252:255], v[236:239], v[60:63]\n"
    "v_cvt_pk_bf16_f32 v234, v220, v221\n"
    "v_cvt_pk_bf16_f32 v235, v222, v223\n"
    "v_mfma_f32_16x16x32_bf16 v[224:227], a[192:195], a[80:83], 0\n"
    "v_xor_b32 v248, 32768, v248\n"
    "v_xor_b32 v249, 32768, v249\n"
    "v_mfma_f32_16x16x32_bf16 v[224:227], a[196:199], a[84:87], v[224:227]\n"
    "ds_read_b128 a[192:195], v248 offset:0\n"
    "ds_read_b128 a[196:199], v249 offset:0\n"
    "v_mfma_f32_16x16x32_bf16 v[224:227], a[200:203], a[88:91], v[224:227]\n"
    "ds_read_b128 a[200:203], v248 offset:1024\n"
    "v_xor_b32 v250, 32768, v250\n"
    "v_mfma_f32_16x16x32_bf16 v[224:227], a[204:207], a[92:95], v[224:227]\n"
    "ds_read_b128 a[204:207], v249 offset:1024\n"
    "ds_read_b64_tr_b16 a[224:225], v250 offset:0\n"
    "v_mfma_f32_16x16x32_bf16 v[228:231], a[208:211], a[80:83], 0\n"
    "ds_read_b128 a[208:211], v248 offset:4096\n"
    "ds_read_b64_tr_b16 a[226:227], v250 offset:4096\n"
    "v_mfma_f32_16x16x32_bf16 v[228:231], a[212:215], a[84:87], v[228:231]\n"
    "ds_read_b128 a[212:215], v249 offset:4096\n"
    "ds_read_b64_tr_b16 a[228:229], v250 offset:8\n"
    "v_mfma_f32_16x16x32_bf16 v[228:231], a[216:219], a[88:91], v[228:231]\n"
    "ds_read_b128 a[216:219], v248 offset:5120\n"
    "ds_read_b64_tr_b16 a[230:231], v250 offset:4104\n"
    "v_mfma_f32_16x16x32_bf16 v[228:231], a[220:223], a[92:95], v[228:231]\n"
    "ds_read_b128 a[220:223], v249 offset:5120\n"
    "v_xor_b32 v251, 32768, v251\n"
    "v_mfma_f32_16x16x32_bf16 v[64:67], v[192:195], v[232:235], v[64:67]\n"
    "v_exp_f32 v224, v224\n"
    "v_mfma_f32_16x16x32_bf16 v[68:71], v[196:199], v[232:235], v[68:71]\n"
    "v_exp_f32 v225, v225\n"
    "v_mfma_f32_16x16x32_bf16 v[72:75], v[200:203], v[232:235], v[72:75]\n"
    "v_exp_f32 v226, v226\n"
    "v_mfma_f32_16x16x32_bf16 v[76:79], v[204:207], v[232:235], v[76:79]\n"
    "v_exp_f32 v227, v227\n"
    "v_mfma_f32_16x16x32_bf16 v[80:83], v[208:211], v[232:235], v[80:83]\n"
    "v_exp_f32 v228, v228\n"
    "v_mfma_f32_16x16x32_bf16 v[84:87], v[212:215], v[232:235], v[84:87]\n"
    "v_exp_f32 v229, v229\n"
    "v_mfma_f32_16x16x32_bf16 v[88:91], a[248:251], v[232:235], v[88:91]\n"
    "v_exp_f32 v230, v230\n"
    "v_mfma_f32_16x16x32_bf16 v[92:95], a[252:255], v[232:235], v[92:95]\n"
    "v_exp_f32 v231, v231\n"
    "v_mfma_f32_16x16x32_bf16 v[184:187], a[112:115], v[232:235], v[184:187]\n"
    "s_waitcnt lgkmcnt(11)\n"
    "v_cvt_pk_bf16_f32 v236, v224, v225\n"
    "v_cvt_pk_bf16_f32 v237, v226, v227\n"
    "v_mfma_f32_16x16x32_bf16 v[216:219], a[192:195], a[32:35], 0\n"
    "s_waitcnt lgkmcnt(10)\n"
    "v_cvt_pk_bf16_f32 v238, v228, v229\n"
    "v_cvt_pk_bf16_f32 v239, v230, v231\n"
    "v_mfma_f32_16x16x32_bf16 v[216:219], a[196:199], a[36:39], v[216:219]\n"
    "s_waitcnt lgkmcnt(9)\n"
    "v_add_f32 v245, v245, v224\n"
    "v_add_f32 v245, v245, v225\n"
    "v_mfma_f32_16x16x32_bf16 v[216:219], a[200:203], a[40:43], v[216:219]\n"
    "s_waitcnt lgkmcnt(8)\n"
    "v_add_f32 v245, v245, v226\n"
    "v_add_f32 v245, v245, v227\n"
    "v_mfma_f32_16x16x32_bf16 v[216:219], a[204:207], a[44:47], v[216:219]\n"
    "s_waitcnt lgkmcnt(6)\n"
    "ds_read_b64_tr_b16 a[232:233], v251 offset:0\n"
    "ds_read_b64_tr_b16 a[234:235], v251 offset:4096\n"
    "v_mfma_f32_16x16x32_bf16 v[220:223], a[208:211], a[32:35], 0\n"
    "s_waitcnt lgkmcnt(6)\n"
    "v_add_f32 v245, v245, v228\n"
    "v_add_f32 v245, v245, v229\n"
    "v_mfma_f32_16x16x32_bf16 v[220:223], a[212:215], a[36:39], v[220:223]\n"
    "s_waitcnt lgkmcnt(4)\n"
    "v_add_f32 v245, v245, v230\n"
    "v_add_f32 v245, v245, v231\n"
    "v_mfma_f32_16x16x32_bf16 v[220:223], a[216:219], a[40:43], v[220:223]\n"
    "s_waitcnt lgkmcnt(2)\n"
    "ds_read_b64_tr_b16 a[236:237], v251 offset:8\n"
    "ds_read_b64_tr_b16 a[238:239], v251 offset:4104\n"
    "v_mfma_f32_16x16x32_bf16 v[220:223], a[220:223], a[44:47], v[220:223]\n"
    "ds_read_b64_tr_b16 a[240:241], v250 offset:1024\n"
    "ds_read_b64_tr_b16 a[242:243], v250 offset:5120\n"
    "v_mfma_f32_16x16x32_bf16 v[96:99], v[192:195], v[236:239], v[96:99]\n"
    "v_exp_f32 v216, v216\n"
    "v_mfma_f32_16x16x32_bf16 v[100:103], v[196:199], v[236:239], v[100:103]\n"
    "v_exp_f32 v217, v217\n"
    "v_mfma_f32_16x16x32_bf16 v[104:107], v[200:203], v[236:239], v[104:107]\n"
    "v_exp_f32 v218, v218\n"
    "v_mfma_f32_16x16x32_bf16 v[108:111], v[204:207], v[236:239], v[108:111]\n"
    "v_exp_f32 v219, v219\n"
    "v_mfma_f32_16x16x32_bf16 v[112:115], v[208:211], v[236:239], v[112:115]\n"
    "v_exp_f32 v220, v220\n"
    "v_mfma_f32_16x16x32_bf16 v[116:119], v[212:215], v[236:239], v[116:119]\n"
    "v_exp_f32 v221, v221\n"
    "v_mfma_f32_16x16x32_bf16 v[120:123], a[248:251], v[236:239], v[120:123]\n"
    "v_exp_f32 v222, v222\n"
    "v_mfma_f32_16x16x32_bf16 v[124:127], a[252:255], v[236:239], v[124:127]\n"
    "v_exp_f32 v223, v223\n"
    "v_mfma_f32_16x16x32_bf16 v[224:227], a[192:195], a[48:51], 0\n"
    "ds_read_b64_tr_b16 a[244:245], v250 offset:1032\n"
    "ds_read_b64_tr_b16 a[246:247], v250 offset:5128\n"
    "v_mfma_f32_16x16x32_bf16 v[224:227], a[196:199], a[52:55], v[224:227]\n"
    "v_cvt_pk_bf16_f32 v232, v216, v217\n"
    "v_cvt_pk_bf16_f32 v233, v218, v219\n"
    "v_mfma_f32_16x16x32_bf16 v[224:227], a[200:203], a[56:59], v[224:227]\n"
    "v_cvt_pk_bf16_f32 v234, v220, v221\n"
    "v_cvt_pk_bf16_f32 v235, v222, v223\n"
    "v_mfma_f32_16x16x32_bf16 v[224:227], a[204:207], a[60:63], v[224:227]\n"
    "ds_read_b64_tr_b16 a[248:249], v251 offset:1024\n"
    "ds_read_b64_tr_b16 a[250:251], v251 offset:5120\n"
    "v_mfma_f32_16x16x32_bf16 v[228:231], a[208:211], a[48:51], 0\n"
    "ds_read_b64_tr_b16 a[252:253], v251 offset:1032\n"
    "ds_read_b64_tr_b16 a[254:255], v251 offset:5128\n"
    "v_mfma_f32_16x16x32_bf16 v[228:231], a[212:215], a[52:55], v[228:231]\n"
    "v_mfma_f32_16x16x32_bf16 v[228:231], a[216:219], a[56:59], v[228:231]\n"
    "v_exp_f32 v224, v224\n"
    "v_mfma_f32_16x16x32_bf16 v[228:231], a[220:223], a[60:63], v[228:231]\n"
    "v_exp_f32 v225, v225\n"
    "v_mfma_f32_16x16x32_bf16 v[0:3], a[224:227], v[232:235], v[0:3]\n"
    "v_exp_f32 v226, v226\n"
    "v_mfma_f32_16x16x32_bf16 v[4:7], a[228:231], v[232:235], v[4:7]\n"
    "s_waitcnt lgkmcnt(10)\n"
    "v_exp_f32 v227, v227\n"
    "v_mfma_f32_16x16x32_bf16 v[8:11], a[232:235], v[232:235], v[8:11]\n"
    "s_waitcnt lgkmcnt(8)\n"
    "v_exp_f32 v228, v228\n"
    "v_mfma_f32_16x16x32_bf16 v[12:15], a[236:239], v[232:235], v[12:15]\n"
    "s_waitcnt lgkmcnt(6)\n"
    "v_exp_f32 v229, v229\n"
    "v_mfma_f32_16x16x32_bf16 v[16:19], a[240:243], v[232:235], v[16:19]\n"
    "s_waitcnt lgkmcnt(4)\n"
    "v_exp_f32 v230, v230\n"
    "v_mfma_f32_16x16x32_bf16 v[20:23], a[244:247], v[232:235], v[20:23]\n"
    "s_waitcnt lgkmcnt(2)\n"
    "v_exp_f32 v231, v231\n"
    "v_mfma_f32_16x16x32_bf16 v[24:27], a[248:251], v[232:235], v[24:27]\n"
    "s_waitcnt lgkmcnt(0)\n"
    "v_cvt_pk_bf16_f32 v236, v224, v225\n"
    "v_cvt_pk_bf16_f32 v237, v226, v227\n"
    "v_mfma_f32_16x16x32_bf16 v[28:31], a[252:255], v[232:235], v[28:31]\n"
    "v_cvt_pk_bf16_f32 v238, v228, v229\n"
    "v_cvt_pk_bf16_f32 v239, v230, v231\n"
    "v_mfma_f32_16x16x32_bf16 v[180:183], a[112:115], v[232:235], v[180:183]\n"
    "v_add_f32 v243, v243, v224\n"
    "v_add_f32 v243, v243, v225\n"
    "v_mfma_f32_16x16x32_bf16 v[216:219], a[192:195], a[64:67], 0\n"
    "v_add_f32 v243, v243, v226\n"
    "v_add_f32 v243, v243, v227\n"
    "v_mfma_f32_16x16x32_bf16 v[216:219], a[196:199], a[68:71], v[216:219]\n"
    "v_add_f32 v243, v243, v228\n"
    "v_add_f32 v243, v243, v229\n"
    "v_mfma_f32_16x16x32_bf16 v[216:219], a[200:203], a[72:75], v[216:219]\n"
    "v_add_f32 v243, v243, v230\n"
    "v_add_f32 v243, v243, v231\n"
    "v_mfma_f32_16x16x32_bf16 v[216:219], a[204:207], a[76:79], v[216:219]\n"
    "v_mfma_f32_16x16x32_bf16 v[220:223], a[208:211], a[64:67], 0\n"
    "v_mfma_f32_16x16x32_bf16 v[220:223], a[212:215], a[68:71], v[220:223]\n"
    "v_mfma_f32_16x16x32_bf16 v[220:223], a[216:219], a[72:75], v[220:223]\n"
    "v_exp_f32 v216, v216\n"
    "v_mfma_f32_16x16x32_bf16 v[220:223], a[220:223], a[76:79], v[220:223]\n"
    "v_exp_f32 v217, v217\n"
    "v_mfma_f32_16x16x32_bf16 v[32:35], a[224:227], v[236:239], v[32:35]\n"
    "v_exp_f32 v218, v218\n"
    "v_mfma_f32_16x16x32_bf16 v[36:39], a[228:231], v[236:239], v[36:39]\n"
    "v_exp_f32 v219, v219\n"
    "v_mfma_f32_16x16x32_bf16 v[40:43], a[232:235], v[236:239], v[40:43]\n"
    "v_exp_f32 v220, v220\n"
    "v_mfma_f32_16x16x32_bf16 v[44:47], a[236:239], v[236:239], v[44:47]\n"
    "v_exp_f32 v221, v221\n"
    "v_mfma_f32_16x16x32_bf16 v[48:51], a[240:243], v[236:239], v[48:51]\n"
    "v_exp_f32 v222, v222\n"
    "v_mfma_f32_16x16x32_bf16 v[52:55], a[244:247], v[236:239], v[52:55]\n"
    "v_exp_f32 v223, v223\n"
    "v_mfma_f32_16x16x32_bf16 v[56:59], a[248:251], v[236:239], v[56:59]\n"
    "v_cvt_pk_bf16_f32 v232, v216, v217\n"
    "v_cvt_pk_bf16_f32 v233, v218, v219\n"
    "v_mfma_f32_16x16x32_bf16 v[60:63], a[252:255], v[236:239], v[60:63]\n"
    "v_cvt_pk_bf16_f32 v234, v220, v221\n"
    "v_cvt_pk_bf16_f32 v235, v222, v223\n"
    "v_mfma_f32_16x16x32_bf16 v[224:227], a[192:195], a[80:83], 0\n"
    "v_mfma_f32_16x16x32_bf16 v[224:227], a[196:199], a[84:87], v[224:227]\n"
    "v_mfma_f32_16x16x32_bf16 v[224:227], a[200:203], a[88:91], v[224:227]\n"
    "v_mfma_f32_16x16x32_bf16 v[224:227], a[204:207], a[92:95], v[224:227]\n"
    "v_mfma_f32_16x16x32_bf16 v[228:231], a[208:211], a[80:83], 0\n"
    "v_mfma_f32_16x16x32_bf16 v[228:231], a[212:215], a[84:87], v[228:231]\n"
    "v_mfma_f32_16x16x32_bf16 v[228:231], a[216:219], a[88:91], v[228:231]\n"
    "v_exp_f32 v224, v224\n"
    "v_mfma_f32_16x16x32_bf16 v[228:231], a[220:223], a[92:95], v[228:231]\n"
    "v_exp_f32 v225, v225\n"
    "v_mfma_f32_16x16x32_bf16 v[64:67], a[224:227], v[232:235], v[64:67]\n"
    "s_waitcnt vmcnt(0)\n"
    "s_barrier\n"
    "ds_read_b128 a[192:195], v248 offset:16384\n"
    "ds_read_b128 a[196:199], v249 offset:16384\n"
    "v_mfma_f32_16x16x32_bf16 v[68:71], a[228:231], v[232:235], v[68:71]\n"
    "ds_read_b128 a[200:203], v248 offset:17408\n"
    "ds_read_b128 a[204:207], v249 offset:17408\n"
    "v_mfma_f32_16x16x32_bf16 v[72:75], a[232:235], v[232:235], v[72:75]\n"
    "ds_read_b128 a[208:211], v248 offset:20480\n"
    "ds_read_b128 a[212:215], v249 offset:20480\n"
    "v_mfma_f32_16x16x32_bf16 v[76:79], a[236:239], v[232:235], v[76:79]\n"
    "ds_read_b128 a[216:219], v248 offset:21504\n"
    "ds_read_b128 a[220:223], v249 offset:21504\n"
    "v_mfma_f32_16x16x32_bf16 v[80:83], a[240:243], v[232:235], v[80:83]\n"
    "v_exp_f32 v226, v226\n"
    "v_mfma_f32_16x16x32_bf16 v[84:87], a[244:247], v[232:235], v[84:87]\n"
    "v_exp_f32 v227, v227\n"
    "v_mfma_f32_16x16x32_bf16 v[88:91], a[248:251], v[232:235], v[88:91]\n"
    "v_exp_f32 v228, v228\n"
    "v_mfma_f32_16x16x32_bf16 v[92:95], a[252:255], v[232:235], v[92:95]\n"
    "v_exp_f32 v229, v229\n"
    "v_mfma_f32_16x16x32_bf16 v[184:187], a[112:115], v[232:235], v[184:187]\n"
    "s_waitcnt lgkmcnt(7)\n"
    "v_exp_f32 v230, v230\n"
    "v_mfma_f32_16x16x32_bf16 v[216:219], a[192:195], a[32:35], 0\n"
    "s_waitcnt lgkmcnt(6)\n"
    "v_exp_f32 v231, v231\n"
    "v_mfma_f32_16x16x32_bf16 v[216:219], a[196:199], a[36:39], v[216:219]\n"
    "s_waitcnt lgkmcnt(5)\n"
    "v_cvt_pk_bf16_f32 v236, v224, v225\n"
    "v_cvt_pk_bf16_f32 v237, v226, v227\n"
    "v_mfma_f32_16x16x32_bf16 v[216:219], a[200:203], a[40:43], v[216:219]\n"
    "s_waitcnt lgkmcnt(4)\n"
    "v_cvt_pk_bf16_f32 v238, v228, v229\n"
    "v_cvt_pk_bf16_f32 v239, v230, v231\n"
    "v_mfma_f32_16x16x32_bf16 v[216:219], a[204:207], a[44:47], v[216:219]\n"
    "s_waitcnt lgkmcnt(3)\n"
    "v_add_f32 v245, v245, v224\n"
    "v_add_f32 v245, v245, v225\n"
    "v_mfma_f32_16x16x32_bf16 v[220:223], a[208:211], a[32:35], 0\n"
    "s_waitcnt lgkmcnt(2)\n"
    "v_add_f32 v245, v245, v226\n"
    "v_add_f32 v245, v245, v227\n"
    "v_mfma_f32_16x16x32_bf16 v[220:223], a[212:215], a[36:39], v[220:223]\n"
    "s_waitcnt lgkmcnt(1)\n"
    "ds_read_b64_tr_b16 v[192:193], v250 offset:16384\n"
    "ds_read_b64_tr_b16 v[194:195], v250 offset:20480\n"
    "v_mfma_f32_16x16x32_bf16 v[220:223], a[216:219], a[40:43], v[220:223]\n"
    "s_waitcnt lgkmcnt(2)\n"
    "ds_read_b64_tr_b16 v[196:197], v250 offset:16392\n"
    "ds_read_b64_tr_b16 v[198:199], v250 offset:20488\n"
    "v_mfma_f32_16x16x32_bf16 v[220:223], a[220:223], a[44:47], v[220:223]\n"
    "ds_read_b64_tr_b16 v[200:201], v251 offset:16384\n"
    "ds_read_b64_tr_b16 v[202:203], v251 offset:20480\n"
    "v_mfma_f32_16x16x32_bf16 v[96:99], a[224:227], v[236:239], v[96:99]\n"
    "v_add_f32 v245, v245, v228\n"
    "v_add_f32 v245, v245, v229\n"
    "v_mfma_f32_16x16x32_bf16 v[100:103], a[228:231], v[236:239], v[100:103]\n"
    "v_add_f32 v245, v245, v230\n"
    "v_add_f32 v245, v245, v231\n"
    "v_mfma_f32_16x16x32_bf16 v[104:107], a[232:235], v[236:239], v[104:107]\n"
    "ds_read_b64_tr_b16 v[204:205], v251 offset:16392\n"
    "ds_read_b64_tr_b16 v[206:207], v251 offset:20488\n"
    "v_mfma_f32_16x16x32_bf16 v[108:111], a[236:239], v[236:239], v[108:111]\n"
    "ds_read_b64_tr_b16 v[208:209], v250 offset:17408\n"
    "ds_read_b64_tr_b16 v[210:211], v250 offset:21504\n"
    "v_mfma_f32_16x16x32_bf16 v[112:115], a[240:243], v[236:239], v[112:115]\n"
    "v_exp_f32 v216, v216\n"
    "v_mfma_f32_16x16x32_bf16 v[116:119], a[244:247], v[236:239], v[116:119]\n"
    "v_exp_f32 v217, v217\n"
    "v_mfma_f32_16x16x32_bf16 v[120:123], a[248:251], v[236:239], v[120:123]\n"
    "v_exp_f32 v218, v218\n"
    "v_mfma_f32_16x16x32_bf16 v[124:127], a[252:255], v[236:239], v[124:127]\n"
    "v_exp_f32 v219, v219\n"
    "v_mfma_f32_16x16x32_bf16 v[224:227], a[192:195], a[48:51], 0\n"
    "v_exp_f32 v220, v220\n"
    "v_mfma_f32_16x16x32_bf16 v[224:227], a[196:199], a[52:55], v[224:227]\n"
    "v_exp_f32 v221, v221\n"
    "v_mfma_f32_16x16x32_bf16 v[224:227], a[200:203], a[56:59], v[224:227]\n"
    "v_exp_f32 v222, v222\n"
    "v_mfma_f32_16x16x32_bf16 v[224:227], a[204:207], a[60:63], v[224:227]\n"
    "v_exp_f32 v223, v223\n"
    "v_mfma_f32_16x16x32_bf16 v[228:231], a[208:211], a[48:51], 0\n"
    "ds_read_b64_tr_b16 v[212:213], v250 offset:17416\n"
    "ds_read_b64_tr_b16 v[214:215], v250 offset:21512\n"
    "v_mfma_f32_16x16x32_bf16 v[228:231], a[212:215], a[52:55], v[228:231]\n"
    "v_cvt_pk_bf16_f32 v232, v216, v217\n"
    "v_cvt_pk_bf16_f32 v233, v218, v219\n"
    "v_mfma_f32_16x16x32_bf16 v[228:231], a[216:219], a[56:59], v[228:231]\n"
    "v_cvt_pk_bf16_f32 v234, v220, v221\n"
    "v_cvt_pk_bf16_f32 v235, v222, v223\n"
    "v_mfma_f32_16x16x32_bf16 v[228:231], a[220:223], a[60:63], v[228:231]\n"
    "s_waitcnt lgkmcnt(10)\n"
    "ds_read_b64_tr_b16 a[248:249], v251 offset:17408\n"
    "ds_read_b64_tr_b16 a[250:251], v251 offset:21504\n"
    "v_mfma_f32_16x16x32_bf16 v[0:3], v[192:195], v[232:235], v[0:3]\n"
    "s_waitcnt lgkmcnt(10)\n"
    "ds_read_b64_tr_b16 a[252:253], v251 offset:17416\n"
    "ds_read_b64_tr_b16 a[254:255], v251 offset:21512\n"
    "v_mfma_f32_16x16x32_bf16 v[4:7], v[196:199], v[232:235], v[4:7]\n"
    "s_waitcnt lgkmcnt(10)\n"
    "v_exp_f32 v224, v224\n"
    "v_mfma_f32_16x16x32_bf16 v[8:11], v[200:203], v[232:235], v[8:11]\n"
    "s_waitcnt lgkmcnt(8)\n"
    "v_exp_f32 v225, v225\n"
    "v_mfma_f32_16x16x32_bf16 v[12:15], v[204:207], v[232:235], v[12:15]\n"
    "s_waitcnt lgkmcnt(6)\n"
    "v_exp_f32 v226, v226\n"
    "v_mfma_f32_16x16x32_bf16 v[16:19], v[208:211], v[232:235], v[16:19]\n"
    "s_waitcnt lgkmcnt(4)\n"
    "v_exp_f32 v227, v227\n"
    "v_mfma_f32_16x16x32_bf16 v[20:23], v[212:215], v[232:235], v[20:23]\n"
    "s_waitcnt lgkmcnt(2)\n"
    "v_exp_f32 v228, v228\n"
    "v_mfma_f32_16x16x32_bf16 v[24:27], a[248:251], v[232:235], v[24:27]\n"
    "s_waitcnt lgkmcnt(0)\n"
    "v_exp_f32 v229, v229\n"
    "v_mfma_f32_16x16x32_bf16 v[28:31], a[252:255], v[232:235], v[28:31]\n"
    "v_exp_f32 v230, v230\n"
    "v_mfma_f32_16x16x32_bf16 v[180:183], a[112:115], v[232:235], v[180:183]\n"
    "v_exp_f32 v231, v231\n"
    "v_mfma_f32_16x16x32_bf16 v[216:219], a[192:195], a[64:67], 0\n"
    "v_cvt_pk_bf16_f32 v236, v224, v225\n"
    "v_cvt_pk_bf16_f32 v237, v226, v227\n"
    "v_mfma_f32_16x16x32_bf16 v[216:219], a[196:199], a[68:71], v[216:219]\n"
    "v_cvt_pk_bf16_f32 v238, v228, v229\n"
    "v_cvt_pk_bf16_f32 v239, v230, v231\n"
    "v_mfma_f32_16x16x32_bf16 v[216:219], a[200:203], a[72:75], v[216:219]\n"
    "v_mov_b32 v242, v180\n"
    "v_add_f32 v243, v243, v224\n"
    "v_mfma_f32_16x16x32_bf16 v[216:219], a[204:207], a[76:79], v[216:219]\n"
    "v_rcp_f32 v242, v242\n"
    "v_mfma_f32_16x16x32_bf16 v[220:223], a[208:211], a[64:67], 0\n"
    "v_pk_mul_f32 v[0:1], v[0:1], v[242:243] op_sel_hi:[1,0]\n"
    "v_pk_mul_f32 v[2:3], v[2:3], v[242:243] op_sel_hi:[1,0]\n"
    "v_pk_mul_f32 v[4:5], v[4:5], v[242:243] op_sel_hi:[1,0]\n"
    "v_pk_mul_f32 v[6:7], v[6:7], v[242:243] op_sel_hi:[1,0]\n"
    "v_pk_mul_f32 v[8:9], v[8:9], v[242:243] op_sel_hi:[1,0]\n"
    "v_pk_mul_f32 v[10:11], v[10:11], v[242:243] op_sel_hi:[1,0]\n"
    "v_pk_mul_f32 v[12:13], v[12:13], v[242:243] op_sel_hi:[1,0]\n"
    "v_pk_mul_f32 v[14:15], v[14:15], v[242:243] op_sel_hi:[1,0]\n"
    "v_pk_mul_f32 v[16:17], v[16:17], v[242:243] op_sel_hi:[1,0]\n"
    "v_pk_mul_f32 v[18:19], v[18:19], v[242:243] op_sel_hi:[1,0]\n"
    "v_pk_mul_f32 v[20:21], v[20:21], v[242:243] op_sel_hi:[1,0]\n"
    "v_pk_mul_f32 v[22:23], v[22:23], v[242:243] op_sel_hi:[1,0]\n"
    "v_pk_mul_f32 v[24:25], v[24:25], v[242:243] op_sel_hi:[1,0]\n"
    "v_pk_mul_f32 v[26:27], v[26:27], v[242:243] op_sel_hi:[1,0]\n"
    "v_pk_mul_f32 v[28:29], v[28:29], v[242:243] op_sel_hi:[1,0]\n"
    "v_pk_mul_f32 v[30:31], v[30:31], v[242:243] op_sel_hi:[1,0]\n"
    "v_cvt_pk_bf16_f32 v0, v0, v1\n"
    "v_mfma_f32_16x16x32_bf16 v[220:223], a[212:215], a[68:71], v[220:223]\n"
    "v_cvt_pk_bf16_f32 v1, v2, v3\n"
    "v_cvt_pk_bf16_f32 v2, v4, v5\n"
    "v_mfma_f32_16x16x32_bf16 v[220:223], a[216:219], a[72:75], v[220:223]\n"
    "v_cvt_pk_bf16_f32 v3, v6, v7\n"
    "v_cvt_pk_bf16_f32 v8, v8, v9\n"
    "v_mfma_f32_16x16x32_bf16 v[220:223], a[220:223], a[76:79], v[220:223]\n"
    "v_cvt_pk_bf16_f32 v9, v10, v11\n"
    "v_cvt_pk_bf16_f32 v10, v12, v13\n"
    "v_mfma_f32_16x16x32_bf16 v[32:35], v[192:195], v[236:239], v[32:35]\n"
    "v_cvt_pk_bf16_f32 v11, v14, v15\n"
    "v_add_f32 v243, v243, v225\n"
    "v_mfma_f32_16x16x32_bf16 v[36:39], v[196:199], v[236:239], v[36:39]\n"
    "v_add_f32 v243, v243, v226\n"
    "v_add_f32 v243, v243, v227\n"
    "v_mfma_f32_16x16x32_bf16 v[40:43], v[200:203], v[236:239], v[40:43]\n"
    "v_add_f32 v243, v243, v228\n"
    "v_add_f32 v243, v243, v229\n"
    "v_mfma_f32_16x16x32_bf16 v[44:47], v[204:207], v[236:239], v[44:47]\n"
    "v_add_f32 v243, v243, v230\n"
    "v_add_f32 v243, v243, v231\n"
    "v_mfma_f32_16x16x32_bf16 v[48:51], v[208:211], v[236:239], v[48:51]\n"
    "buffer_store_dwordx4 v[0:3], v255, s[48:51], s59 offen offset:0 sc1\n"
    "v_mfma_f32_16x16x32_bf16 v[52:55], v[212:215], v[236:239], v[52:55]\n"
    "buffer_store_dwordx4 v[8:11], v255, s[48:51], s59 offen offset:64 sc1\n"
    "v_mfma_f32_16x16x32_bf16 v[56:59], a[248:251], v[236:239], v[56:59]\n"
    "v_exp_f32 v216, v216\n"
    "v_mfma_f32_16x16x32_bf16 v[60:63], a[252:255], v[236:239], v[60:63]\n"
    "v_exp_f32 v217, v217\n"
    "v_mfma_f32_16x16x32_bf16 v[224:227], a[192:195], a[80:83], 0\n"
    "v_exp_f32 v218, v218\n"
    "v_mfma_f32_16x16x32_bf16 v[224:227], a[196:199], a[84:87], v[224:227]\n"
    "v_exp_f32 v219, v219\n"
    "v_mfma_f32_16x16x32_bf16 v[224:227], a[200:203], a[88:91], v[224:227]\n"
    "v_exp_f32 v220, v220\n"
    "v_mfma_f32_16x16x32_bf16 v[224:227], a[204:207], a[92:95], v[224:227]\n"
    "v_exp_f32 v221, v221\n"
    "v_mfma_f32_16x16x32_bf16 v[228:231], a[208:211], a[80:83], 0\n"
    "v_exp_f32 v222, v222\n"
    "v_mfma_f32_16x16x32_bf16 v[228:231], a[212:215], a[84:87], v[228:231]\n"
    "v_exp_f32 v223, v223\n"
    "v_mfma_f32_16x16x32_bf16 v[228:231], a[216:219], a[88:91], v[228:231]\n"
    "v_cvt_pk_bf16_f32 v232, v216, v217\n"
    "v_cvt_pk_bf16_f32 v233, v218, v219\n"
    "v_cvt_pk_bf16_f32 v234, v220, v221\n"
    "v_cvt_pk_bf16_f32 v235, v222, v223\n"
    "v_mfma_f32_16x16x32_bf16 v[228:231], a[220:223], a[92:95], v[228:231]\n"
    "v_cvt_pk_bf16_f32 v16, v16, v17\n"
    "v_cvt_pk_bf16_f32 v17, v18, v19\n"
    "v_mfma_f32_16x16x32_bf16 v[64:67], v[192:195], v[232:235], v[64:67]\n"
    "v_cvt_pk_bf16_f32 v18, v20, v21\n"
    "v_cvt_pk_bf16_f32 v19, v22, v23\n"
    "v_mfma_f32_16x16x32_bf16 v[68:71], v[196:199], v[232:235], v[68:71]\n"
    "v_cvt_pk_bf16_f32 v24, v24, v25\n"
    "v_cvt_pk_bf16_f32 v25, v26, v27\n"
    "v_mfma_f32_16x16x32_bf16 v[72:75], v[200:203], v[232:235], v[72:75]\n"
    "v_cvt_pk_bf16_f32 v26, v28, v29\n"
    "v_cvt_pk_bf16_f32 v27, v30, v31\n"
    "v_mfma_f32_16x16x32_bf16 v[76:79], v[204:207], v[232:235], v[76:79]\n"
    "v_exp_f32 v224, v224\n"
    "v_mfma_f32_16x16x32_bf16 v[80:83], v[208:211], v[232:235], v[80:83]\n"
    "v_exp_f32 v225, v225\n"
    "v_mfma_f32_16x16x32_bf16 v[84:87], v[212:215], v[232:235], v[84:87]\n"
    "v_exp_f32 v226, v226\n"
    "v_mfma_f32_16x16x32_bf16 v[88:91], a[248:251], v[232:235], v[88:91]\n"
    "v_exp_f32 v227, v227\n"
    "v_mfma_f32_16x16x32_bf16 v[92:95], a[252:255], v[232:235], v[92:95]\n"
    "v_exp_f32 v228, v228\n"
    "v_mfma_f32_16x16x32_bf16 v[184:187], a[112:115], v[232:235], v[184:187]\n"
    "v_exp_f32 v229, v229\n"
    "v_mfma_f32_16x16x32_bf16 v[216:219], a[192:195], a[0:3], 0\n"
    "v_exp_f32 v230, v230\n"
    "v_mfma_f32_16x16x32_bf16 v[216:219], a[196:199], a[4:7], v[216:219]\n"
    "v_exp_f32 v231, v231\n"
    "v_mfma_f32_16x16x32_bf16 v[216:219], a[200:203], a[8:11], v[216:219]\n"
    "v_cvt_pk_bf16_f32 v236, v224, v225\n"
    "v_cvt_pk_bf16_f32 v237, v226, v227\n"
    "v_mfma_f32_16x16x32_bf16 v[216:219], a[204:207], a[12:15], v[216:219]\n"
    "buffer_store_dwordx4 v[16:19], v255, s[48:51], s59 offen offset:128 sc1\n"
    "v_mfma_f32_16x16x32_bf16 v[220:223], a[208:211], a[0:3], 0\n"
    "buffer_store_dwordx4 v[24:27], v255, s[48:51], s59 offen offset:192 sc1\n"
    "v_mfma_f32_16x16x32_bf16 v[220:223], a[212:215], a[4:7], v[220:223]\n"
    "s_add_u32 s59, s67, 65536\n"
    "v_cvt_pk_bf16_f32 v238, v228, v229\n"
    "v_cvt_pk_bf16_f32 v239, v230, v231\n"
    "v_mfma_f32_16x16x32_bf16 v[220:223], a[216:219], a[8:11], v[220:223]\n"
    "v_add_f32 v245, v245, v224\n"
    "v_add_f32 v245, v245, v225\n"
    "v_mfma_f32_16x16x32_bf16 v[220:223], a[220:223], a[12:15], v[220:223]\n"
    "v_add_f32 v245, v245, v226\n"
    "v_add_f32 v245, v245, v227\n"
    "v_mfma_f32_16x16x32_bf16 v[96:99], v[192:195], v[236:239], v[96:99]\n"
    "v_add_f32 v245, v245, v228\n"
    "v_add_f32 v245, v245, v229\n"
    "v_mfma_f32_16x16x32_bf16 v[100:103], v[196:199], v[236:239], v[100:103]\n"
    "v_add_f32 v245, v245, v230\n"
    "v_add_f32 v245, v245, v231\n"
    "v_mfma_f32_16x16x32_bf16 v[104:107], v[200:203], v[236:239], v[104:107]\n"
    "v_exp_f32 v216, v216\n"
    "v_mfma_f32_16x16x32_bf16 v[108:111], v[204:207], v[236:239], v[108:111]\n"
    "v_exp_f32 v217, v217\n"
    "v_mfma_f32_16x16x32_bf16 v[112:115], v[208:211], v[236:239], v[112:115]\n"
    "v_exp_f32 v218, v218\n"
    "v_mfma_f32_16x16x32_bf16 v[116:119], v[212:215], v[236:239], v[116:119]\n"
    "v_exp_f32 v219, v219\n"
    "v_mfma_f32_16x16x32_bf16 v[120:123], a[248:251], v[236:239], v[120:123]\n"
    "v_exp_f32 v220, v220\n"
    "v_mfma_f32_16x16x32_bf16 v[124:127], a[252:255], v[236:239], v[124:127]\n"
    "v_exp_f32 v221, v221\n"
    "v_mfma_f32_16x16x32_bf16 v[224:227], a[192:195], a[16:19], 0\n"
    "v_exp_f32 v222, v222\n"
    "v_mfma_f32_16x16x32_bf16 v[224:227], a[196:199], a[20:23], v[224:227]\n"
    "v_exp_f32 v223, v223\n"
    "v_mfma_f32_16x16x32_bf16 v[224:227], a[200:203], a[24:27], v[224:227]\n"
    "v_cvt_pk_bf16_f32 v232, v216, v217\n"
    "v_cvt_pk_bf16_f32 v233, v218, v219\n"
    "v_mfma_f32_16x16x32_bf16 v[224:227], a[204:207], a[28:31], v[224:227]\n"
    "v_cvt_pk_bf16_f32 v234, v220, v221\n"
    "v_cvt_pk_bf16_f32 v235, v222, v223\n"
    "v_mfma_f32_16x16x32_bf16 v[228:231], a[208:211], a[16:19], 0\n"
    "v_mov_b32 v254, v243\n"
    "v_add_f32 v240, v240, v216\n"
    "v_mfma_f32_16x16x32_bf16 v[228:231], a[212:215], a[20:23], v[228:231]\n"
    "v_permlane32_swap_b32 v243, v254\n"
    "v_add_f32 v243, v243, v254\n"
    "v_mfma_f32_16x16x32_bf16 v[228:231], a[216:219], a[24:27], v[228:231]\n"
    "v_mov_b32 v254, v243\n"
    "v_add_f32 v240, v240, v217\n"
    "v_mfma_f32_16x16x32_bf16 v[228:231], a[220:223], a[28:31], v[228:231]\n"
    "v_permlane16_swap_b32 v243, v254\n"
    "v_add_f32 v243, v243, v254\n"
    "v_mfma_f32_16x16x32_bf16 a[128:131], v[192:195], v[232:235], a[128:131]\n"
    "v_rcp_f32 v243, v243\n"
    "v_mfma_f32_16x16x32_bf16 a[132:135], v[196:199], v[232:235], a[132:135]\n"
    "v_pk_mul_f32 v[32:33], v[32:33], v[242:243] op_sel:[0,1] op_sel_hi:[1,1]\n"
    "v_pk_mul_f32 v[34:35], v[34:35], v[242:243] op_sel:[0,1] op_sel_hi:[1,1]\n"
    "v_pk_mul_f32 v[36:37], v[36:37], v[242:243] op_sel:[0,1] op_sel_hi:[1,1]\n"
    "v_pk_mul_f32 v[38:39], v[38:39], v[242:243] op_sel:[0,1] op_sel_hi:[1,1]\n"
    "v_pk_mul_f32 v[40:41], v[40:41], v[242:243] op_sel:[0,1] op_sel_hi:[1,1]\n"
    "v_pk_mul_f32 v[42:43], v[42:43], v[242:243] op_sel:[0,1] op_sel_hi:[1,1]\n"
    "v_pk_mul_f32 v[44:45], v[44:45], v[242:243] op_sel:[0,1] op_sel_hi:[1,1]\n"
    "v_pk_mul_f32 v[46:47], v[46:47], v[242:243] op_sel:[0,1] op_sel_hi:[1,1]\n"
    "v_pk_mul_f32 v[48:49], v[48:49], v[242:243] op_sel:[0,1] op_sel_hi:[1,1]\n"
    "v_pk_mul_f32 v[50:51], v[50:51], v[242:243] op_sel:[0,1] op_sel_hi:[1,1]\n"
    "v_pk_mul_f32 v[52:53], v[52:53], v[242:243] op_sel:[0,1] op_sel_hi:[1,1]\n"
    "v_pk_mul_f32 v[54:55], v[54:55], v[242:243] op_sel:[0,1] op_sel_hi:[1,1]\n"
    "v_pk_mul_f32 v[56:57], v[56:57], v[242:243] op_sel:[0,1] op_sel_hi:[1,1]\n"
    "v_pk_mul_f32 v[58:59], v[58:59], v[242:243] op_sel:[0,1] op_sel_hi:[1,1]\n"
    "v_pk_mul_f32 v[60:61], v[60:61], v[242:243] op_sel:[0,1] op_sel_hi:[1,1]\n"
    "v_pk_mul_f32 v[62:63], v[62:63], v[242:243] op_sel:[0,1] op_sel_hi:[1,1]\n"
    "v_cvt_pk_bf16_f32 v32, v32, v33\n"
    "v_mfma_f32_16x16x32_bf16 a[136:139], v[200:203], v[232:235], a[136:139]\n"
    "v_cvt_pk_bf16_f32 v33, v34, v35\n"
    "v_cvt_pk_bf16_f32 v34, v36, v37\n"
    "v_cvt_pk_bf16_f32 v35, v38, v39\n"
    "v_mfma_f32_16x16x32_bf16 a[140:143], v[204:207], v[232:235], a[140:143]\n"
    "buffer_store_dwordx4 v[32:35], v255, s[48:51], s60 offen offset:0 sc1\n"
    "v_cvt_pk_bf16_f32 v40, v40, v41\n"
    "v_cvt_pk_bf16_f32 v41, v42, v43\n"
    "v_cvt_pk_bf16_f32 v42, v44, v45\n"
    "v_cvt_pk_bf16_f32 v43, v46, v47\n"
    "v_mfma_f32_16x16x32_bf16 a[144:147], v[208:211], v[232:235], a[144:147]\n"
    "buffer_store_dwordx4 v[40:43], v255, s[48:51], s60 offen offset:64 sc1\n"
    "v_mfma_f32_16x16x32_bf16 a[148:151], v[212:215], v[232:235], a[148:151]\n"
    "v_add_f32 v240, v240, v218\n"
    "v_add_f32 v240, v240, v219\n"
    "v_mfma_f32_16x16x32_bf16 a[152:155], a[248:251], v[232:235], a[152:155]\n"
    "v_add_f32 v240, v240, v220\n"
    "v_add_f32 v240, v240, v221\n"
    "v_mfma_f32_16x16x32_bf16 a[156:159], a[252:255], v[232:235], a[156:159]\n"
    "v_add_f32 v240, v240, v222\n"
    "v_add_f32 v240, v240, v223\n"
    "v_mfma_f32_16x16x32_bf16 v[216:219], a[192:195], a[96:99], 0\n"
    "v_exp_f32 v224, v224\n"
    "v_mfma_f32_16x16x32_bf16 v[216:219], a[196:199], a[100:103], v[216:219]\n"
    "v_exp_f32 v225, v225\n"
    "v_mfma_f32_16x16x32_bf16 v[216:219], a[200:203], a[104:107], v[216:219]\n"
    "v_exp_f32 v226, v226\n"
    "v_mfma_f32_16x16x32_bf16 v[216:219], a[204:207], a[108:111], v[216:219]\n"
    "v_exp_f32 v227, v227\n"
    "v_mfma_f32_16x16x32_bf16 v[220:223], a[208:211], a[96:99], 0\n"
    "v_exp_f32 v228, v228\n"
    "v_mfma_f32_16x16x32_bf16 v[220:223], a[212:215], a[100:103], v[220:223]\n"
    "v_exp_f32 v229, v229\n"
    "v_exp_f32 v230, v230\n"
    "v_exp_f32 v231, v231\n"
    "v_mfma_f32_16x16x32_bf16 v[220:223], a[216:219], a[104:107], v[220:223]\n"
    "v_cvt_pk_bf16_f32 v236, v224, v225\n"
    "v_cvt_pk_bf16_f32 v237, v226, v227\n"
    "v_cvt_pk_bf16_f32 v238, v228, v229\n"
    "v_cvt_pk_bf16_f32 v239, v230, v231\n"
    "v_mfma_f32_16x16x32_bf16 v[220:223], a[220:223], a[108:111], v[220:223]\n"
    "ds_read_b128 a[192:195], v248 offset:0\n"
    "ds_read_b128 a[196:199], v249 offset:0\n"
    "v_mfma_f32_16x16x32_bf16 a[160:163], v[192:195], v[236:239], a[160:163]\n"
    "ds_read_b128 a[200:203], v248 offset:1024\n"
    "ds_read_b128 a[204:207], v249 offset:1024\n"
    "v_mfma_f32_16x16x32_bf16 a[164:167], v[196:199], v[236:239], a[164:167]\n"
    "ds_read_b128 a[208:211], v248 offset:4096\n"
    "ds_read_b128 a[212:215], v249 offset:4096\n"
    "v_mfma_f32_16x16x32_bf16 a[168:171], v[200:203], v[236:239], a[168:171]\n"
    "v_add_f32 v241, v241, v224\n"
    "v_add_f32 v241, v241, v225\n"
    "v_mfma_f32_16x16x32_bf16 a[172:175], v[204:207], v[236:239], a[172:175]\n"
    "v_add_f32 v241, v241, v226\n"
    "v_add_f32 v241, v241, v227\n"
    "v_mfma_f32_16x16x32_bf16 a[176:179], v[208:211], v[236:239], a[176:179]\n"
    "ds_read_b128 a[216:219], v248 offset:5120\n"
    "ds_read_b128 a[220:223], v249 offset:5120\n"
    "v_mfma_f32_16x16x32_bf16 a[180:183], v[212:215], v[236:239], a[180:183]\n"
    "v_cvt_pk_bf16_f32 v48, v48, v49\n"
    "v_cvt_pk_bf16_f32 v49, v50, v51\n"
    "v_mfma_f32_16x16x32_bf16 a[184:187], a[248:251], v[236:239], a[184:187]\n"
    "v_cvt_pk_bf16_f32 v50, v52, v53\n"
    "v_cvt_pk_bf16_f32 v51, v54, v55\n"
    "v_mfma_f32_16x16x32_bf16 a[188:191], a[252:255], v[236:239], a[188:191]\n"
    "s_waitcnt lgkmcnt(7)\n"
    "v_cvt_pk_bf16_f32 v56, v56, v57\n"
    "v_cvt_pk_bf16_f32 v57, v58, v59\n"
    "v_mfma_f32_16x16x32_bf16 v[224:227], a[192:195], a[0:3], 0\n"
    "s_waitcnt lgkmcnt(6)\n"
    "v_cvt_pk_bf16_f32 v58, v60, v61\n"
    "v_cvt_pk_bf16_f32 v59, v62, v63\n"
    "v_mfma_f32_16x16x32_bf16 v[224:227], a[196:199], a[4:7], v[224:227]\n"
    "s_waitcnt lgkmcnt(5)\n"
    "v_add_f32 v241, v241, v228\n"
    "v_add_f32 v241, v241, v229\n"
    "v_mfma_f32_16x16x32_bf16 v[224:227], a[200:203], a[8:11], v[224:227]\n"
    "s_waitcnt lgkmcnt(4)\n"
    "v_add_f32 v241, v241, v230\n"
    "v_add_f32 v241, v241, v231\n"
    "v_mfma_f32_16x16x32_bf16 v[224:227], a[204:207], a[12:15], v[224:227]\n"
    "s_waitcnt lgkmcnt(3)\n"
    "buffer_store_dwordx4 v[48:51], v255, s[48:51], s60 offen offset:128 sc1\n"
    "v_mfma_f32_16x16x32_bf16 v[228:231], a[208:211], a[0:3], 0\n"
    "s_waitcnt lgkmcnt(2)\n"
    "buffer_store_dwordx4 v[56:59], v255, s[48:51], s60 offen offset:192 sc1\n"
    "v_mfma_f32_16x16x32_bf16 v[228:231], a[212:215], a[4:7], v[228:231]\n"
    "s_waitcnt lgkmcnt(1)\n"
    "s_add_u32 s60, s67, 81920\n"
    "v_exp_f32 v216, v216\n"
    "v_exp_f32 v217, v217\n"
    "v_exp_f32 v218, v218\n"
    "v_exp_f32 v219, v219\n"
    "v_exp_f32 v220, v220\n"
    "v_exp_f32 v221, v221\n"
    "v_exp_f32 v222, v222\n"
    "v_exp_f32 v223, v223\n"
    "v_mfma_f32_16x16x32_bf16 v[228:231], a[216:219], a[8:11], v[228:231]\n"
    "s_waitcnt lgkmcnt(0)\n"
    "v_cvt_pk_bf16_f32 v232, v216, v217\n"
    "v_cvt_pk_bf16_f32 v233, v218, v219\n"
    "v_cvt_pk_bf16_f32 v234, v220, v221\n"
    "v_cvt_pk_bf16_f32 v235, v222, v223\n"
    "v_mfma_f32_16x16x32_bf16 v[228:231], a[220:223], a[12:15], v[228:231]\n"
    "ds_read_b64_tr_b16 a[224:225], v250 offset:0\n"
    "ds_read_b64_tr_b16 a[226:227], v250 offset:4096\n"
    "v_mfma_f32_16x16x32_bf16 v[128:131], v[192:195], v[232:235], v[128:131]\n"
    "ds_read_b64_tr_b16 a[228:229], v250 offset:8\n"
    "ds_read_b64_tr_b16 a[230:231], v250 offset:4104\n"
    "v_mfma_f32_16x16x32_bf16 v[132:135], v[196:199], v[232:235], v[132:135]\n"
    "ds_read_b64_tr_b16 a[232:233], v251 offset:0\n"
    "ds_read_b64_tr_b16 a[234:235], v251 offset:4096\n"
    "v_mfma_f32_16x16x32_bf16 v[136:139], v[200:203], v[232:235], v[136:139]\n"
    "ds_read_b64_tr_b16 a[236:237], v251 offset:8\n"
    "ds_read_b64_tr_b16 a[238:239], v251 offset:4104\n"
    "v_mfma_f32_16x16x32_bf16 v[140:143], v[204:207], v[232:235], v[140:143]\n"
    "ds_read_b64_tr_b16 a[240:241], v250 offset:1024\n"
    "ds_read_b64_tr_b16 a[242:243], v250 offset:5120\n"
    "v_mfma_f32_16x16x32_bf16 v[144:147], v[208:211], v[232:235], v[144:147]\n"
    "v_exp_f32 v224, v224\n"
    "v_mfma_f32_16x16x32_bf16 v[148:151], v[212:215], v[232:235], v[148:151]\n"
    "v_exp_f32 v225, v225\n"
    "v_mfma_f32_16x16x32_bf16 v[152:155], a[248:251], v[232:235], v[152:155]\n"
    "v_exp_f32 v226, v226\n"
    "v_mfma_f32_16x16x32_bf16 v[156:159], a[252:255], v[232:235], v[156:159]\n"
    "v_exp_f32 v227, v227\n"
    "v_mfma_f32_16x16x32_bf16 v[188:191], a[112:115], v[232:235], v[188:191]\n"
    "v_exp_f32 v228, v228\n"
    "v_mfma_f32_16x16x32_bf16 v[216:219], a[192:195], a[16:19], 0\n"
    "v_exp_f32 v229, v229\n"
    "v_mfma_f32_16x16x32_bf16 v[216:219], a[196:199], a[20:23], v[216:219]\n"
    "v_exp_f32 v230, v230\n"
    "v_mfma_f32_16x16x32_bf16 v[216:219], a[200:203], a[24:27], v[216:219]\n"
    "v_exp_f32 v231, v231\n"
    "v_mfma_f32_16x16x32_bf16 v[216:219], a[204:207], a[28:31], v[216:219]\n"
    "ds_read_b64_tr_b16 a[244:245], v250 offset:1032\n"
    "ds_read_b64_tr_b16 a[246:247], v250 offset:5128\n"
    "v_mfma_f32_16x16x32_bf16 v[220:223], a[208:211], a[16:19], 0\n"
    "v_cvt_pk_bf16_f32 v236, v224, v225\n"
    "v_cvt_pk_bf16_f32 v237, v226, v227\n"
    "v_mfma_f32_16x16x32_bf16 v[220:223], a[212:215], a[20:23], v[220:223]\n"
    "v_cvt_pk_bf16_f32 v238, v228, v229\n"
    "v_cvt_pk_bf16_f32 v239, v230, v231\n"
    "v_mfma_f32_16x16x32_bf16 v[220:223], a[216:219], a[24:27], v[220:223]\n"
    "ds_read_b64_tr_b16 a[248:249], v251 offset:1024\n"
    "ds_read_b64_tr_b16 a[250:251], v251 offset:5120\n"
    "v_mfma_f32_16x16x32_bf16 v[220:223], a[220:223], a[28:31], v[220:223]\n"
    "s_waitcnt lgkmcnt(12)\n"
    "ds_read_b64_tr_b16 a[252:253], v251 offset:1032\n"
    "v_mov_b32 v244, v184\n"
    "v_mfma_f32_16x16x32_bf16 a[128:131], a[224:227], v[236:239], a[128:131]\n"
    "s_waitcnt lgkmcnt(11)\n"
    "ds_read_b64_tr_b16 a[254:255], v251 offset:5128\n"
    "v_rcp_f32 v244, v244\n"
    "v_mfma_f32_16x16x32_bf16 a[132:135], a[228:231], v[236:239], a[132:135]\n"
    "s_waitcnt lgkmcnt(10)\n"
    "v_pk_mul_f32 v[64:65], v[64:65], v[244:245] op_sel_hi:[1,0]\n"
    "v_pk_mul_f32 v[66:67], v[66:67], v[244:245] op_sel_hi:[1,0]\n"
    "v_pk_mul_f32 v[68:69], v[68:69], v[244:245] op_sel_hi:[1,0]\n"
    "v_pk_mul_f32 v[70:71], v[70:71], v[244:245] op_sel_hi:[1,0]\n"
    "v_pk_mul_f32 v[72:73], v[72:73], v[244:245] op_sel_hi:[1,0]\n"
    "v_pk_mul_f32 v[74:75], v[74:75], v[244:245] op_sel_hi:[1,0]\n"
    "v_pk_mul_f32 v[76:77], v[76:77], v[244:245] op_sel_hi:[1,0]\n"
    "v_pk_mul_f32 v[78:79], v[78:79], v[244:245] op_sel_hi:[1,0]\n"
    "v_pk_mul_f32 v[80:81], v[80:81], v[244:245] op_sel_hi:[1,0]\n"
    "v_pk_mul_f32 v[82:83], v[82:83], v[244:245] op_sel_hi:[1,0]\n"
    "v_pk_mul_f32 v[84:85], v[84:85], v[244:245] op_sel_hi:[1,0]\n"
    "v_pk_mul_f32 v[86:87], v[86:87], v[244:245] op_sel_hi:[1,0]\n"
    "v_pk_mul_f32 v[88:89], v[88:89], v[244:245] op_sel_hi:[1,0]\n"
    "v_pk_mul_f32 v[90:91], v[90:91], v[244:245] op_sel_hi:[1,0]\n"
    "v_pk_mul_f32 v[92:93], v[92:93], v[244:245] op_sel_hi:[1,0]\n"
    "v_pk_mul_f32 v[94:95], v[94:95], v[244:245] op_sel_hi:[1,0]\n"
    "v_cvt_pk_bf16_f32 v64, v64, v65\n"
    "v_cvt_pk_bf16_f32 v65, v66, v67\n"
    "v_cvt_pk_bf16_f32 v66, v68, v69\n"
    "v_cvt_pk_bf16_f32 v67, v70, v71\n"
    "v_mfma_f32_16x16x32_bf16 a[136:139], a[232:235], v[236:239], a[136:139]\n"
    "s_waitcnt lgkmcnt(8)\n"
    "buffer_store_dwordx4 v[64:67], v255, s[48:51], s59 offen offset:0 sc1\n"
    "v_cvt_pk_bf16_f32 v72, v72, v73\n"
    "v_cvt_pk_bf16_f32 v73, v74, v75\n"
    "v_cvt_pk_bf16_f32 v74, v76, v77\n"
    "v_cvt_pk_bf16_f32 v75, v78, v79\n"
    "v_mfma_f32_16x16x32_bf16 a[140:143], a[236:239], v[236:239], a[140:143]\n"
    "s_waitcnt lgkmcnt(6)\n"
    "buffer_store_dwordx4 v[72:75], v255, s[48:51], s59 offen offset:64 sc1\n"
    "v_mfma_f32_16x16x32_bf16 a[144:147], a[240:243], v[236:239], a[144:147]\n"
    "s_waitcnt lgkmcnt(4)\n"
    "v_add_f32 v240, v240, v224\n"
    "v_add_f32 v240, v240, v225\n"
    "v_mfma_f32_16x16x32_bf16 a[148:151], a[244:247], v[236:239], a[148:151]\n"
    "s_waitcnt lgkmcnt(2)\n"
    "v_add_f32 v240, v240, v226\n"
    "v_add_f32 v240, v240, v227\n"
    "v_mfma_f32_16x16x32_bf16 a[152:155], a[248:251], v[236:239], a[152:155]\n"
    "s_waitcnt lgkmcnt(0)\n"
    "v_add_f32 v240, v240, v228\n"
    "v_add_f32 v240, v240, v229\n"
    "v_mfma_f32_16x16x32_bf16 a[156:159], a[252:255], v[236:239], a[156:159]\n"
    "v_add_f32 v240, v240, v230\n"
    "v_add_f32 v240, v240, v231\n"
    "v_mfma_f32_16x16x32_bf16 v[224:227], a[192:195], a[96:99], 0\n"
    "v_exp_f32 v216, v216\n"
    "v_mfma_f32_16x16x32_bf16 v[224:227], a[196:199], a[100:103], v[224:227]\n"
    "v_exp_f32 v217, v217\n"
    "v_mfma_f32_16x16x32_bf16 v[224:227], a[200:203], a[104:107], v[224:227]\n"
    "v_exp_f32 v218, v218\n"
    "v_mfma_f32_16x16x32_bf16 v[224:227], a[204:207], a[108:111], v[224:227]\n"
    "v_exp_f32 v219, v219\n"
    "v_mfma_f32_16x16x32_bf16 v[228:231], a[208:211], a[96:99], 0\n"
    "v_exp_f32 v220, v220\n"
    "v_mfma_f32_16x16x32_bf16 v[228:231], a[212:215], a[100:103], v[228:231]\n"
    "v_exp_f32 v221, v221\n"
    "v_exp_f32 v222, v222\n"
    "v_exp_f32 v223, v223\n"
    "v_mfma_f32_16x16x32_bf16 v[228:231], a[216:219], a[104:107], v[228:231]\n"
    "v_cvt_pk_bf16_f32 v232, v216, v217\n"
    "v_cvt_pk_bf16_f32 v233, v218, v219\n"
    "v_cvt_pk_bf16_f32 v234, v220, v221\n"
    "v_cvt_pk_bf16_f32 v235, v222, v223\n"
    "v_mfma_f32_16x16x32_bf16 v[228:231], a[220:223], a[108:111], v[228:231]\n"
    "ds_read_b128 a[192:195], v248 offset:49152\n"
    "ds_read_b128 a[196:199], v249 offset:49152\n"
    "v_mfma_f32_16x16x32_bf16 a[160:163], a[224:227], v[232:235], a[160:163]\n"
    "ds_read_b128 a[200:203], v248 offset:50176\n"
    "ds_read_b128 a[204:207], v249 offset:50176\n"
    "v_mfma_f32_16x16x32_bf16 a[164:167], a[228:231], v[232:235], a[164:167]\n"
    "ds_read_b128 a[208:211], v248 offset:53248\n"
    "ds_read_b128 a[212:215], v249 offset:53248\n"
    "v_mfma_f32_16x16x32_bf16 a[168:171], a[232:235], v[232:235], a[168:171]\n"
    "v_add_f32 v241, v241, v216\n"
    "v_add_f32 v241, v241, v217\n"
    "v_mfma_f32_16x16x32_bf16 a[172:175], a[236:239], v[232:235], a[172:175]\n"
    "v_add_f32 v241, v241, v218\n"
    "v_add_f32 v241, v241, v219\n"
    "v_mfma_f32_16x16x32_bf16 a[176:179], a[240:243], v[232:235], a[176:179]\n"
    "ds_read_b128 a[216:219], v248 offset:54272\n"
    "ds_read_b128 a[220:223], v249 offset:54272\n"
    "v_mfma_f32_16x16x32_bf16 a[180:183], a[244:247], v[232:235], a[180:183]\n"
    "v_cvt_pk_bf16_f32 v80, v80, v81\n"
    "v_cvt_pk_bf16_f32 v81, v82, v83\n"
    "v_mfma_f32_16x16x32_bf16 a[184:187], a[248:251], v[232:235], a[184:187]\n"
    "v_cvt_pk_bf16_f32 v82, v84, v85\n"
    "v_cvt_pk_bf16_f32 v83, v86, v87\n"
    "v_mfma_f32_16x16x32_bf16 a[188:191], a[252:255], v[232:235], a[188:191]\n"
    "s_waitcnt lgkmcnt(7)\n"
    "v_cvt_pk_bf16_f32 v88, v88, v89\n"
    "v_cvt_pk_bf16_f32 v89, v90, v91\n"
    "v_mfma_f32_16x16x32_bf16 v[216:219], a[192:195], a[0:3], 0\n"
    "s_waitcnt lgkmcnt(6)\n"
    "v_cvt_pk_bf16_f32 v90, v92, v93\n"
    "v_cvt_pk_bf16_f32 v91, v94, v95\n"
    "v_mfma_f32_16x16x32_bf16 v[216:219], a[196:199], a[4:7], v[216:219]\n"
    "s_waitcnt lgkmcnt(5)\n"
    "v_add_f32 v241, v241, v220\n"
    "v_add_f32 v241, v241, v221\n"
    "v_mfma_f32_16x16x32_bf16 v[216:219], a[200:203], a[8:11], v[216:219]\n"
    "s_waitcnt lgkmcnt(4)\n"
    "buffer_store_dwordx4 v[80:83], v255, s[48:51], s59 offen offset:128 sc1\n"
    "v_mfma_f32_16x16x32_bf16 v[216:219], a[204:207], a[12:15], v[216:219]\n"
    "s_waitcnt lgkmcnt(3)\n"
    "buffer_store_dwordx4 v[88:91], v255, s[48:51], s59 offen offset:192 sc1\n"
    "v_add_f32 v241, v241, v222\n"
    "v_add_f32 v241, v241, v223\n"
    "v_mfma_f32_16x16x32_bf16 v[220:223], a[208:211], a[0:3], 0\n"
    "s_waitcnt lgkmcnt(2)\n"
    "s_add_u32 s59, s67, 0\n"
    "v_exp_f32 v224, v224\n"
    "v_mfma_f32_16x16x32_bf16 v[220:223], a[212:215], a[4:7], v[220:223]\n"
    "s_waitcnt lgkmcnt(1)\n"
    "v_exp_f32 v225, v225\n"
    "v_exp_f32 v226, v226\n"
    "v_exp_f32 v227, v227\n"
    "v_exp_f32 v228, v228\n"
    "v_exp_f32 v229, v229\n"
    "v_exp_f32 v230, v230\n"
    "v_exp_f32 v231, v231\n"
    "v_mfma_f32_16x16x32_bf16 v[220:223], a[216:219], a[8:11], v[220:223]\n"
    "s_waitcnt lgkmcnt(0)\n"
    "v_cvt_pk_bf16_f32 v236, v224, v225\n"
    "v_cvt_pk_bf16_f32 v237, v226, v227\n"
    "v_cvt_pk_bf16_f32 v238, v228, v229\n"
    "v_cvt_pk_bf16_f32 v239, v230, v231\n"
    "v_mfma_f32_16x16x32_bf16 v[220:223], a[220:223], a[12:15], v[220:223]\n"
    "ds_read_b64_tr_b16 v[192:193], v250 offset:49152\n"
    "ds_read_b64_tr_b16 v[194:195], v250 offset:53248\n"
    "v_mfma_f32_16x16x32_bf16 v[128:131], a[224:227], v[236:239], v[128:131]\n"
    "ds_read_b64_tr_b16 v[196:197], v250 offset:49160\n"
    "ds_read_b64_tr_b16 v[198:199], v250 offset:53256\n"
    "v_mfma_f32_16x16x32_bf16 v[132:135], a[228:231], v[236:239], v[132:135]\n"
    "ds_read_b64_tr_b16 v[200:201], v251 offset:49152\n"
    "ds_read_b64_tr_b16 v[202:203], v251 offset:53248\n"
    "v_mfma_f32_16x16x32_bf16 v[136:139], a[232:235], v[236:239], v[136:139]\n"
    "ds_read_b64_tr_b16 v[204:205], v251 offset:49160\n"
    "ds_read_b64_tr_b16 v[206:207], v251 offset:53256\n"
    "v_mfma_f32_16x16x32_bf16 v[140:143], a[236:239], v[236:239], v[140:143]\n"
    "ds_read_b64_tr_b16 v[208:209], v250 offset:50176\n"
    "v_mov_b32 v254, v245\n"
    "v_mfma_f32_16x16x32_bf16 v[144:147], a[240:243], v[236:239], v[144:147]\n"
    "ds_read_b64_tr_b16 v[210:211], v250 offset:54272\n"
    "v_permlane32_swap_b32 v245, v254\n"
    "v_mfma_f32_16x16x32_bf16 v[148:151], a[244:247], v[236:239], v[148:151]\n"
    "v_exp_f32 v216, v216\n"
    "v_mfma_f32_16x16x32_bf16 v[152:155], a[248:251], v[236:239], v[152:155]\n"
    "v_exp_f32 v217, v217\n"
    "v_mfma_f32_16x16x32_bf16 v[156:159], a[252:255], v[236:239], v[156:159]\n"
    "v_exp_f32 v218, v218\n"
    "v_mfma_f32_16x16x32_bf16 v[188:191], a[112:115], v[236:239], v[188:191]\n"
    "v_exp_f32 v219, v219\n"
    "v_mfma_f32_16x16x32_bf16 v[224:227], a[192:195], a[16:19], 0\n"
    "v_exp_f32 v220, v220\n"
    "v_mfma_f32_16x16x32_bf16 v[224:227], a[196:199], a[20:23], v[224:227]\n"
    "v_exp_f32 v221, v221\n"
    "v_mfma_f32_16x16x32_bf16 v[224:227], a[200:203], a[24:27], v[224:227]\n"
    "v_exp_f32 v222, v222\n"
    "v_mfma_f32_16x16x32_bf16 v[224:227], a[204:207], a[28:31], v[224:227]\n"
    "v_exp_f32 v223, v223\n"
    "v_mfma_f32_16x16x32_bf16 v[228:231], a[208:211], a[16:19], 0\n"
    "v_add_f32 v245, v245, v254\n"
    "v_mov_b32 v254, v245\n"
    "v_mfma_f32_16x16x32_bf16 v[228:231], a[212:215], a[20:23], v[228:231]\n"
    "ds_read_b64_tr_b16 v[212:213], v250 offset:50184\n"
    "ds_read_b64_tr_b16 v[214:215], v250 offset:54280\n"
    "v_mfma_f32_16x16x32_bf16 v[228:231], a[216:219], a[24:27], v[228:231]\n"
    "v_cvt_pk_bf16_f32 v232, v216, v217\n"
    "v_cvt_pk_bf16_f32 v233, v218, v219\n"
    "v_cvt_pk_bf16_f32 v234, v220, v221\n"
    "v_cvt_pk_bf16_f32 v235, v222, v223\n"
    "v_mfma_f32_16x16x32_bf16 v[228:231], a[220:223], a[28:31], v[228:231]\n"
    "s_waitcnt lgkmcnt(10)\n"
    "ds_read_b64_tr_b16 a[248:249], v251 offset:50176\n"
    "ds_read_b64_tr_b16 a[250:251], v251 offset:54272\n"
    "v_permlane16_swap_b32 v245, v254\n"
    "v_add_f32 v245, v245, v254\n"
    "v_rcp_f32 v245, v245\n"
    "v_mfma_f32_16x16x32_bf16 a[128:131], v[192:195], v[232:235], a[128:131]\n"
    "s_waitcnt lgkmcnt(10)\n"
    "ds_read_b64_tr_b16 a[252:253], v251 offset:50184\n"
    "ds_read_b64_tr_b16 a[254:255], v251 offset:54280\n"
    "v_pk_mul_f32 v[96:97], v[96:97], v[244:245] op_sel:[0,1] op_sel_hi:[1,1]\n"
    "v_pk_mul_f32 v[98:99], v[98:99], v[244:245] op_sel:[0,1] op_sel_hi:[1,1]\n"
    "v_pk_mul_f32 v[100:101], v[100:101], v[244:245] op_sel:[0,1] op_sel_hi:[1,1]\n"
    "v_pk_mul_f32 v[102:103], v[102:103], v[244:245] op_sel:[0,1] op_sel_hi:[1,1]\n"
    "v_pk_mul_f32 v[104:105], v[104:105], v[244:245] op_sel:[0,1] op_sel_hi:[1,1]\n"
    "v_pk_mul_f32 v[106:107], v[106:107], v[244:245] op_sel:[0,1] op_sel_hi:[1,1]\n"
    "v_pk_mul_f32 v[108:109], v[108:109], v[244:245] op_sel:[0,1] op_sel_hi:[1,1]\n"
    "v_pk_mul_f32 v[110:111], v[110:111], v[244:245] op_sel:[0,1] op_sel_hi:[1,1]\n"
    "v_pk_mul_f32 v[112:113], v[112:113], v[244:245] op_sel:[0,1] op_sel_hi:[1,1]\n"
    "v_pk_mul_f32 v[114:115], v[114:115], v[244:245] op_sel:[0,1] op_sel_hi:[1,1]\n"
    "v_pk_mul_f32 v[116:117], v[116:117], v[244:245] op_sel:[0,1] op_sel_hi:[1,1]\n"
    "v_pk_mul_f32 v[118:119], v[118:119], v[244:245] op_sel:[0,1] op_sel_hi:[1,1]\n"
    "v_pk_mul_f32 v[120:121], v[120:121], v[244:245] op_sel:[0,1] op_sel_hi:[1,1]\n"
    "v_pk_mul_f32 v[122:123], v[122:123], v[244:245] op_sel:[0,1] op_sel_hi:[1,1]\n"
    "v_pk_mul_f32 v[124:125], v[124:125], v[244:245] op_sel:[0,1] op_sel_hi:[1,1]\n"
    "v_pk_mul_f32 v[126:127], v[126:127], v[244:245] op_sel:[0,1] op_sel_hi:[1,1]\n"
    "v_cvt_pk_bf16_f32 v96, v96, v97\n"
    "v_cvt_pk_bf16_f32 v97, v98, v99\n"
    "v_cvt_pk_bf16_f32 v98, v100, v101\n"
    "v_cvt_pk_bf16_f32 v99, v102, v103\n"
    "v_mfma_f32_16x16x32_bf16 a[132:135], v[196:199], v[232:235], a[132:135]\n"
    "s_waitcnt lgkmcnt(10)\n"
    "buffer_store_dwordx4 v[96:99], v255, s[48:51], s60 offen offset:0 sc1\n"
    "ds_read_b128 a[192:195], v248 offset:32768\n"
    "v_cvt_pk_bf16_f32 v104, v104, v105\n"
    "v_cvt_pk_bf16_f32 v105, v106, v107\n"
    "v_cvt_pk_bf16_f32 v106, v108, v109\n"
    "v_cvt_pk_bf16_f32 v107, v110, v111\n"
    "v_mfma_f32_16x16x32_bf16 a[136:139], v[200:203], v[232:235], a[136:139]\n"
    "s_waitcnt lgkmcnt(9)\n"
    "buffer_store_dwordx4 v[104:107], v255, s[48:51], s60 offen offset:64 sc1\n"
    "ds_read_b128 a[196:199], v249 offset:32768\n"
    "v_mfma_f32_16x16x32_bf16 a[140:143], v[204:207], v[232:235], a[140:143]\n"
    "s_waitcnt lgkmcnt(8)\n"
    "ds_read_b128 a[200:203], v248 offset:33792\n"
    "ds_read_b128 a[204:207], v249 offset:33792\n"
    "v_mfma_f32_16x16x32_bf16 a[144:147], v[208:211], v[232:235], a[144:147]\n"
    "s_waitcnt lgkmcnt(8)\n"
    "ds_read_b128 a[208:211], v248 offset:36864\n"
    "ds_read_b128 a[212:215], v249 offset:36864\n"
    "v_mfma_f32_16x16x32_bf16 a[148:151], v[212:215], v[232:235], a[148:151]\n"
    "s_waitcnt lgkmcnt(8)\n"
    "v_add_f32 v240, v240, v216\n"
    "v_add_f32 v240, v240, v217\n"
    "v_mfma_f32_16x16x32_bf16 a[152:155], a[248:251], v[232:235], a[152:155]\n"
    "s_waitcnt lgkmcnt(6)\n"
    "v_add_f32 v240, v240, v218\n"
    "v_add_f32 v240, v240, v219\n"
    "v_mfma_f32_16x16x32_bf16 a[156:159], a[252:255], v[232:235], a[156:159]\n"
    "s_waitcnt lgkmcnt(5)\n"
    "ds_read_b128 a[216:219], v248 offset:37888\n"
    "ds_read_b128 a[220:223], v249 offset:37888\n"
    "v_mfma_f32_16x16x32_bf16 v[216:219], a[192:195], a[0:3], 0\n"
    "s_waitcnt lgkmcnt(6)\n"
    "v_add_f32 v240, v240, v220\n"
    "v_add_f32 v240, v240, v221\n"
    "v_mfma_f32_16x16x32_bf16 v[216:219], a[196:199], a[4:7], v[216:219]\n"
    "s_waitcnt lgkmcnt(5)\n"
    "v_add_f32 v240, v240, v222\n"
    "v_add_f32 v240, v240, v223\n"
    "v_mfma_f32_16x16x32_bf16 v[216:219], a[200:203], a[8:11], v[216:219]\n"
    "s_waitcnt lgkmcnt(4)\n"
    "v_exp_f32 v224, v224\n"
    "v_mfma_f32_16x16x32_bf16 v[216:219], a[204:207], a[12:15], v[216:219]\n"
    "s_waitcnt lgkmcnt(3)\n"
    "v_exp_f32 v225, v225\n"
    "v_mfma_f32_16x16x32_bf16 v[220:223], a[208:211], a[0:3], 0\n"
    "s_waitcnt lgkmcnt(2)\n"
    "v_exp_f32 v226, v226\n"
    "v_mfma_f32_16x16x32_bf16 v[220:223], a[212:215], a[4:7], v[220:223]\n"
    "s_waitcnt lgkmcnt(1)\n"
    "v_exp_f32 v227, v227\n"
    "v_exp_f32 v228, v228\n"
    "v_exp_f32 v229, v229\n"
    "v_exp_f32 v230, v230\n"
    "v_exp_f32 v231, v231\n"
    "v_mfma_f32_16x16x32_bf16 v[220:223], a[216:219], a[8:11], v[220:223]\n"
    "s_waitcnt lgkmcnt(0)\n"
    "v_cvt_pk_bf16_f32 v236, v224, v225\n"
    "v_cvt_pk_bf16_f32 v237, v226, v227\n"
    "v_cvt_pk_bf16_f32 v238, v228, v229\n"
    "v_cvt_pk_bf16_f32 v239, v230, v231\n"
    "v_mfma_f32_16x16x32_bf16 v[220:223], a[220:223], a[12:15], v[220:223]\n"
    "v_add_f32 v241, v241, v224\n"
    "v_add_f32 v241, v241, v225\n"
    "v_mfma_f32_16x16x32_bf16 a[160:163], v[192:195], v[236:239], a[160:163]\n"
    "v_add_f32 v241, v241, v226\n"
    "v_add_f32 v241, v241, v227\n"
    "v_mfma_f32_16x16x32_bf16 a[164:167], v[196:199], v[236:239], a[164:167]\n"
    "v_cvt_pk_bf16_f32 v112, v112, v113\n"
    "v_cvt_pk_bf16_f32 v113, v114, v115\n"
    "v_mfma_f32_16x16x32_bf16 a[168:171], v[200:203], v[236:239], a[168:171]\n"
    "v_cvt_pk_bf16_f32 v114, v116, v117\n"
    "v_cvt_pk_bf16_f32 v115, v118, v119\n"
    "v_mfma_f32_16x16x32_bf16 a[172:175], v[204:207], v[236:239], a[172:175]\n"
    "v_cvt_pk_bf16_f32 v120, v120, v121\n"
    "v_cvt_pk_bf16_f32 v121, v122, v123\n"
    "v_mfma_f32_16x16x32_bf16 a[176:179], v[208:211], v[236:239], a[176:179]\n"
    "v_cvt_pk_bf16_f32 v122, v124, v125\n"
    "v_cvt_pk_bf16_f32 v123, v126, v127\n"
    "v_mfma_f32_16x16x32_bf16 a[180:183], v[212:215], v[236:239], a[180:183]\n"
    "ds_read_b64_tr_b16 a[224:225], v250 offset:32768\n"
    "ds_read_b64_tr_b16 a[226:227], v250 offset:36864\n"
    "v_mfma_f32_16x16x32_bf16 a[184:187], a[248:251], v[236:239], a[184:187]\n"
    "ds_read_b64_tr_b16 a[228:229], v250 offset:32776\n"
    "ds_read_b64_tr_b16 a[230:231], v250 offset:36872\n"
    "v_mfma_f32_16x16x32_bf16 a[188:191], a[252:255], v[236:239], a[188:191]\n"
    "ds_read_b64_tr_b16 a[232:233], v251 offset:32768\n"
    "ds_read_b64_tr_b16 a[234:235], v251 offset:36864\n"
    "v_mfma_f32_16x16x32_bf16 v[224:227], a[192:195], a[16:19], 0\n"
    "v_add_f32 v241, v241, v228\n"
    "v_add_f32 v241, v241, v229\n"
    "v_mfma_f32_16x16x32_bf16 v[224:227], a[196:199], a[20:23], v[224:227]\n"
    "buffer_store_dwordx4 v[112:115], v255, s[48:51], s60 offen offset:128 sc1\n"
    "v_mfma_f32_16x16x32_bf16 v[224:227], a[200:203], a[24:27], v[224:227]\n"
    "buffer_store_dwordx4 v[120:123], v255, s[48:51], s60 offen offset:192 sc1\n"
    "v_mfma_f32_16x16x32_bf16 v[224:227], a[204:207], a[28:31], v[224:227]\n"
    "s_add_u32 s60, s67, 16384\n"
    "v_add_f32 v241, v241, v230\n"
    "v_add_f32 v241, v241, v231\n"
    "v_mfma_f32_16x16x32_bf16 v[228:231], a[208:211], a[16:19], 0\n"
    "ds_read_b64_tr_b16 a[236:237], v251 offset:32776\n"
    "ds_read_b64_tr_b16 a[238:239], v251 offset:36872\n"
    "v_mfma_f32_16x16x32_bf16 v[228:231], a[212:215], a[20:23], v[228:231]\n"
    "ds_read_b64_tr_b16 a[240:241], v250 offset:33792\n"
    "ds_read_b64_tr_b16 a[242:243], v250 offset:37888\n"
    "v_exp_f32 v216, v216\n"
    "v_exp_f32 v217, v217\n"
    "v_exp_f32 v218, v218\n"
    "v_exp_f32 v219, v219\n"
    "v_exp_f32 v220, v220\n"
    "v_exp_f32 v221, v221\n"
    "v_exp_f32 v222, v222\n"
    "v_exp_f32 v223, v223\n"
    "v_mfma_f32_16x16x32_bf16 v[228:231], a[216:219], a[24:27], v[228:231]\n"
    "ds_read_b64_tr_b16 a[244:245], v250 offset:33800\n"
    "ds_read_b64_tr_b16 a[246:247], v250 offset:37896\n"
    "v_cvt_pk_bf16_f32 v232, v216, v217\n"
    "v_cvt_pk_bf16_f32 v233, v218, v219\n"
    "v_cvt_pk_bf16_f32 v234, v220, v221\n"
    "v_cvt_pk_bf16_f32 v235, v222, v223\n"
    "v_mfma_f32_16x16x32_bf16 v[228:231], a[220:223], a[28:31], v[228:231]\n"
    "s_waitcnt lgkmcnt(10)\n"
    "ds_read_b64_tr_b16 a[248:249], v251 offset:33792\n"
    "ds_read_b64_tr_b16 a[250:251], v251 offset:37888\n"
    "v_mfma_f32_16x16x32_bf16 a[128:131], a[224:227], v[232:235], a[128:131]\n"
    "s_waitcnt lgkmcnt(10)\n"
    "ds_read_b64_tr_b16 a[252:253], v251 offset:33800\n"
    "ds_read_b64_tr_b16 a[254:255], v251 offset:37896\n"
    "v_mfma_f32_16x16x32_bf16 a[132:135], a[228:231], v[232:235], a[132:135]\n"
    "s_waitcnt lgkmcnt(10)\n"
    "v_add_f32 v240, v240, v216\n"
    "v_add_f32 v240, v240, v217\n"
    "v_mfma_f32_16x16x32_bf16 a[136:139], a[232:235], v[232:235], a[136:139]\n"
    "s_waitcnt lgkmcnt(8)\n"
    "v_add_f32 v240, v240, v218\n"
    "v_add_f32 v240, v240, v219\n"
    "v_mfma_f32_16x16x32_bf16 a[140:143], a[236:239], v[232:235], a[140:143]\n"
    "s_waitcnt lgkmcnt(6)\n"
    "v_add_f32 v240, v240, v220\n"
    "v_add_f32 v240, v240, v221\n"
    "v_mfma_f32_16x16x32_bf16 a[144:147], a[240:243], v[232:235], a[144:147]\n"
    "s_waitcnt lgkmcnt(4)\n"
    "v_add_f32 v240, v240, v222\n"
    "v_add_f32 v240, v240, v223\n"
    "v_mfma_f32_16x16x32_bf16 a[148:151], a[244:247], v[232:235], a[148:151]\n"
    "s_waitcnt lgkmcnt(2)\n"
    "v_exp_f32 v224, v224\n"
    "v_mfma_f32_16x16x32_bf16 a[152:155], a[248:251], v[232:235], a[152:155]\n"
    "s_waitcnt lgkmcnt(0)\n"
    "v_exp_f32 v225, v225\n"
    "v_mfma_f32_16x16x32_bf16 a[156:159], a[252:255], v[232:235], a[156:159]\n"
    "v_exp_f32 v226, v226\n"
    "v_mfma_f32_16x16x32_bf16 v[216:219], a[192:195], a[96:99], 0\n"
    "v_exp_f32 v227, v227\n"
    "v_mfma_f32_16x16x32_bf16 v[216:219], a[196:199], a[100:103], v[216:219]\n"
    "v_exp_f32 v228, v228\n"
    "v_mfma_f32_16x16x32_bf16 v[216:219], a[200:203], a[104:107], v[216:219]\n"
    "v_exp_f32 v229, v229\n"
    "v_mfma_f32_16x16x32_bf16 v[216:219], a[204:207], a[108:111], v[216:219]\n"
    "v_exp_f32 v230, v230\n"
    "v_mfma_f32_16x16x32_bf16 v[220:223], a[208:211], a[96:99], 0\n"
    "v_exp_f32 v231, v231\n"
    "v_mfma_f32_16x16x32_bf16 v[220:223], a[212:215], a[100:103], v[220:223]\n"
    "v_cvt_pk_bf16_f32 v236, v224, v225\n"
    "v_cvt_pk_bf16_f32 v237, v226, v227\n"
    "v_mfma_f32_16x16x32_bf16 v[220:223], a[216:219], a[104:107], v[220:223]\n"
    "v_cvt_pk_bf16_f32 v238, v228, v229\n"
    "v_cvt_pk_bf16_f32 v239, v230, v231\n"
    "v_mfma_f32_16x16x32_bf16 v[220:223], a[220:223], a[108:111], v[220:223]\n"
    "ds_read_b128 a[192:195], v248 offset:49152\n"
    "v_mov_b32 v253, v240\n"
    "v_mfma_f32_16x16x32_bf16 a[160:163], a[224:227], v[236:239], a[160:163]\n"
    "s_nop 0\n"
    "v_permlane32_swap_b32 v240, v253\n"
    "v_add_f32 v240, v240, v253\n"
    "v_mfma_f32_16x16x32_bf16 a[164:167], a[228:231], v[236:239], a[164:167]\n"
    "ds_read_b128 a[196:199], v249 offset:49152\n"
    "v_mov_b32 v253, v240\n"
    "v_mfma_f32_16x16x32_bf16 a[168:171], a[232:235], v[236:239], a[168:171]\n"
    "s_nop 0\n"
    "v_permlane16_swap_b32 v240, v253\n"
    "v_add_f32 v240, v240, v253\n"
    "v_rcp_f32 v240, v240\n"
    "v_mfma_f32_16x16x32_bf16 a[172:175], a[236:239], v[236:239], a[172:175]\n"
    "ds_read_b128 a[200:203], v248 offset:50176\n"
    "v_accvgpr_read_b32 v68, a128\n"
    "v_accvgpr_read_b32 v69, a129\n"
    "v_accvgpr_read_b32 v70, a130\n"
    "v_accvgpr_read_b32 v71, a131\n"
    "v_accvgpr_read_b32 v76, a132\n"
    "v_accvgpr_read_b32 v77, a133\n"
    "v_accvgpr_read_b32 v78, a134\n"
    "v_accvgpr_read_b32 v79, a135\n"
    "v_mul_f32 v68, v68, v240\n"
    "v_mul_f32 v69, v69, v240\n"
    "v_mul_f32 v70, v70, v240\n"
    "v_mul_f32 v71, v71, v240\n"
    "v_mul_f32 v76, v76, v240\n"
    "v_mul_f32 v77, v77, v240\n"
    "v_mul_f32 v78, v78, v240\n"
    "v_mul_f32 v79, v79, v240\n"
    "v_cvt_pk_bf16_f32 v4, v68, v69\n"
    "v_cvt_pk_bf16_f32 v5, v70, v71\n"
    "v_cvt_pk_bf16_f32 v6, v76, v77\n"
    "v_cvt_pk_bf16_f32 v7, v78, v79\n"
    "v_mfma_f32_16x16x32_bf16 a[176:179], a[240:243], v[236:239], a[176:179]\n"
    "buffer_store_dwordx4 v[4:7], v255, s[48:51], s59 offen offset:0 sc1\n"
    "ds_read_b128 a[204:207], v249 offset:50176\n"
    "v_accvgpr_read_b32 v84, a136\n"
    "v_accvgpr_read_b32 v85, a137\n"
    "v_accvgpr_read_b32 v86, a138\n"
    "v_accvgpr_read_b32 v87, a139\n"
    "v_accvgpr_read_b32 v92, a140\n"
    "v_accvgpr_read_b32 v93, a141\n"
    "v_accvgpr_read_b32 v94, a142\n"
    "v_accvgpr_read_b32 v95, a143\n"
    "v_mul_f32 v84, v84, v240\n"
    "v_mul_f32 v85, v85, v240\n"
    "v_mul_f32 v86, v86, v240\n"
    "v_mul_f32 v87, v87, v240\n"
    "v_mul_f32 v92, v92, v240\n"
    "v_mul_f32 v93, v93, v240\n"
    "v_mul_f32 v94, v94, v240\n"
    "v_mul_f32 v95, v95, v240\n"
    "v_cvt_pk_bf16_f32 v12, v84, v85\n"
    "v_cvt_pk_bf16_f32 v13, v86, v87\n"
    "v_cvt_pk_bf16_f32 v14, v92, v93\n"
    "v_cvt_pk_bf16_f32 v15, v94, v95\n"
    "v_mfma_f32_16x16x32_bf16 a[180:183], a[244:247], v[236:239], a[180:183]\n"
    "buffer_store_dwordx4 v[12:15], v255, s[48:51], s59 offen offset:64 sc1\n"
    "ds_read_b128 a[208:211], v248 offset:53248\n"
    "v_mfma_f32_16x16x32_bf16 a[184:187], a[248:251], v[236:239], a[184:187]\n"
    "ds_read_b128 a[212:215], v249 offset:53248\n"
    "v_add_f32 v241, v241, v224\n"
    "v_mfma_f32_16x16x32_bf16 a[188:191], a[252:255], v[236:239], a[188:191]\n"
    "s_waitcnt lgkmcnt(5)\n"
    "ds_read_b128 a[216:219], v248 offset:54272\n"
    "v_add_f32 v241, v241, v225\n"
    "v_add_f32 v241, v241, v226\n"
    "v_add_f32 v241, v241, v227\n"
    "v_mfma_f32_16x16x32_bf16 v[224:227], a[192:195], a[96:99], 0\n"
    "s_waitcnt lgkmcnt(5)\n"
    "ds_read_b128 a[220:223], v249 offset:54272\n"
    "v_add_f32 v241, v241, v228\n"
    "v_mfma_f32_16x16x32_bf16 v[224:227], a[196:199], a[100:103], v[224:227]\n"
    "s_waitcnt lgkmcnt(5)\n"
    "v_add_f32 v241, v241, v229\n"
    "v_add_f32 v241, v241, v230\n"
    "v_mfma_f32_16x16x32_bf16 v[224:227], a[200:203], a[104:107], v[224:227]\n"
    "s_waitcnt lgkmcnt(4)\n"
    "v_add_f32 v241, v241, v231\n"
    "v_accvgpr_read_b32 v68, a144\n"
    "v_mfma_f32_16x16x32_bf16 v[224:227], a[204:207], a[108:111], v[224:227]\n"
    "s_waitcnt lgkmcnt(3)\n"
    "v_accvgpr_read_b32 v69, a145\n"
    "v_accvgpr_read_b32 v70, a146\n"
    "v_mfma_f32_16x16x32_bf16 v[228:231], a[208:211], a[96:99], 0\n"
    "s_waitcnt lgkmcnt(2)\n"
    "v_accvgpr_read_b32 v71, a147\n"
    "v_accvgpr_read_b32 v76, a148\n"
    "v_mfma_f32_16x16x32_bf16 v[228:231], a[212:215], a[100:103], v[228:231]\n"
    "s_waitcnt lgkmcnt(1)\n"
    "v_accvgpr_read_b32 v77, a149\n"
    "v_accvgpr_read_b32 v78, a150\n"
    "v_accvgpr_read_b32 v79, a151\n"
    "v_mul_f32 v68, v68, v240\n"
    "v_mul_f32 v69, v69, v240\n"
    "v_mul_f32 v70, v70, v240\n"
    "v_mul_f32 v71, v71, v240\n"
    "v_mul_f32 v76, v76, v240\n"
    "v_mul_f32 v77, v77, v240\n"
    "v_mul_f32 v78, v78, v240\n"
    "v_mul_f32 v79, v79, v240\n"
    "v_cvt_pk_bf16_f32 v20, v68, v69\n"
    "v_cvt_pk_bf16_f32 v21, v70, v71\n"
    "v_cvt_pk_bf16_f32 v22, v76, v77\n"
    "v_cvt_pk_bf16_f32 v23, v78, v79\n"
    "v_exp_f32 v216, v216\n"
    "v_exp_f32 v217, v217\n"
    "v_exp_f32 v218, v218\n"
    "v_exp_f32 v219, v219\n"
    "v_exp_f32 v220, v220\n"
    "v_exp_f32 v221, v221\n"
    "v_exp_f32 v222, v222\n"
    "v_exp_f32 v223, v223\n"
    "v_mfma_f32_16x16x32_bf16 v[228:231], a[216:219], a[104:107], v[228:231]\n"
    "s_waitcnt lgkmcnt(0)\n"
    "buffer_store_dwordx4 v[20:23], v255, s[48:51], s59 offen offset:128 sc1\n"
    "v_accvgpr_read_b32 v84, a152\n"
    "v_accvgpr_read_b32 v85, a153\n"
    "v_accvgpr_read_b32 v86, a154\n"
    "v_accvgpr_read_b32 v87, a155\n"
    "v_accvgpr_read_b32 v92, a156\n"
    "v_accvgpr_read_b32 v93, a157\n"
    "v_accvgpr_read_b32 v94, a158\n"
    "v_accvgpr_read_b32 v95, a159\n"
    "v_mul_f32 v84, v84, v240\n"
    "v_mul_f32 v85, v85, v240\n"
    "v_mul_f32 v86, v86, v240\n"
    "v_mul_f32 v87, v87, v240\n"
    "v_mul_f32 v92, v92, v240\n"
    "v_mul_f32 v93, v93, v240\n"
    "v_mul_f32 v94, v94, v240\n"
    "v_mul_f32 v95, v95, v240\n"
    "v_cvt_pk_bf16_f32 v28, v84, v85\n"
    "v_cvt_pk_bf16_f32 v29, v86, v87\n"
    "v_cvt_pk_bf16_f32 v30, v92, v93\n"
    "v_cvt_pk_bf16_f32 v31, v94, v95\n"
    "v_cvt_pk_bf16_f32 v232, v216, v217\n"
    "v_cvt_pk_bf16_f32 v233, v218, v219\n"
    "v_cvt_pk_bf16_f32 v234, v220, v221\n"
    "v_cvt_pk_bf16_f32 v235, v222, v223\n"
    "v_mfma_f32_16x16x32_bf16 v[228:231], a[220:223], a[108:111], v[228:231]\n"
    "buffer_store_dwordx4 v[28:31], v255, s[48:51], s59 offen offset:192 sc1\n"
    "v_mfma_f32_16x16x32_bf16 v[128:131], a[224:227], v[232:235], v[128:131]\n"
    "s_add_u32 s59, s67, 98304\n"
    "ds_read_b64_tr_b16 v[192:193], v250 offset:49152\n"
    "ds_read_b64_tr_b16 v[194:195], v250 offset:53248\n"
    "v_mfma_f32_16x16x32_bf16 v[132:135], a[228:231], v[232:235], v[132:135]\n"
    "ds_read_b64_tr_b16 v[196:197], v250 offset:49160\n"
    "ds_read_b64_tr_b16 v[198:199], v250 offset:53256\n"
    "v_mfma_f32_16x16x32_bf16 v[136:139], a[232:235], v[232:235], v[136:139]\n"
    "ds_read_b64_tr_b16 v[200:201], v251 offset:49152\n"
    "ds_read_b64_tr_b16 v[202:203], v251 offset:53248\n"
    "v_mfma_f32_16x16x32_bf16 v[140:143], a[236:239], v[232:235], v[140:143]\n"
    "ds_read_b64_tr_b16 v[204:205], v251 offset:49160\n"
    "v_mov_b32 v254, v241\n"
    "v_mfma_f32_16x16x32_bf16 v[144:147], a[240:243], v[232:235], v[144:147]\n"
    "ds_read_b64_tr_b16 v[206:207], v251 offset:53256\n"
    "v_permlane32_swap_b32 v241, v254\n"
    "v_mfma_f32_16x16x32_bf16 v[148:151], a[244:247], v[232:235], v[148:151]\n"
    "v_add_f32 v241, v241, v254\n"
    "v_mov_b32 v254, v241\n"
    "v_mfma_f32_16x16x32_bf16 v[152:155], a[248:251], v[232:235], v[152:155]\n"
    "ds_read_b64_tr_b16 v[208:209], v250 offset:50176\n"
    "ds_read_b64_tr_b16 v[210:211], v250 offset:54272\n"
    "v_permlane16_swap_b32 v241, v254\n"
    "v_add_f32 v241, v241, v254\n"
    "v_rcp_f32 v241, v241\n"
    "v_exp_f32 v224, v224\n"
    "v_exp_f32 v225, v225\n"
    "v_exp_f32 v226, v226\n"
    "v_exp_f32 v227, v227\n"
    "v_exp_f32 v228, v228\n"
    "v_exp_f32 v229, v229\n"
    "v_exp_f32 v230, v230\n"
    "v_exp_f32 v231, v231\n"
    "v_mfma_f32_16x16x32_bf16 v[156:159], a[252:255], v[232:235], v[156:159]\n"
    "ds_read_b64_tr_b16 v[212:213], v250 offset:50184\n"
    "ds_read_b64_tr_b16 v[214:215], v250 offset:54280\n"
    "v_accvgpr_read_b32 v100, a160\n"
    "v_accvgpr_read_b32 v101, a161\n"
    "v_accvgpr_read_b32 v102, a162\n"
    "v_accvgpr_read_b32 v103, a163\n"
    "v_accvgpr_read_b32 v108, a164\n"
    "v_accvgpr_read_b32 v109, a165\n"
    "v_accvgpr_read_b32 v110, a166\n"
    "v_accvgpr_read_b32 v111, a167\n"
    "v_mul_f32 v100, v100, v241\n"
    "v_mul_f32 v101, v101, v241\n"
    "v_mul_f32 v102, v102, v241\n"
    "v_mul_f32 v103, v103, v241\n"
    "v_mul_f32 v108, v108, v241\n"
    "v_mul_f32 v109, v109, v241\n"
    "v_mul_f32 v110, v110, v241\n"
    "v_mul_f32 v111, v111, v241\n"
    "v_cvt_pk_bf16_f32 v36, v100, v101\n"
    "v_cvt_pk_bf16_f32 v37, v102, v103\n"
    "v_cvt_pk_bf16_f32 v38, v108, v109\n"
    "v_cvt_pk_bf16_f32 v39, v110, v111\n"
    "v_cvt_pk_bf16_f32 v236, v224, v225\n"
    "v_cvt_pk_bf16_f32 v237, v226, v227\n"
    "v_cvt_pk_bf16_f32 v238, v228, v229\n"
    "v_cvt_pk_bf16_f32 v239, v230, v231\n"
    "v_mfma_f32_16x16x32_bf16 v[188:191], a[112:115], v[232:235], v[188:191]\n"
    "s_waitcnt lgkmcnt(10)\n"
    "buffer_store_dwordx4 v[36:39], v255, s[48:51], s60 offen offset:0 sc1\n"
    "ds_read_b64_tr_b16 a[248:249], v251 offset:50176\n"
    "ds_read_b64_tr_b16 a[250:251], v251 offset:54272\n"
    "v_accvgpr_read_b32 v116, a168\n"
    "v_accvgpr_read_b32 v117, a169\n"
    "v_accvgpr_read_b32 v118, a170\n"
    "v_accvgpr_read_b32 v119, a171\n"
    "v_accvgpr_read_b32 v124, a172\n"
    "v_accvgpr_read_b32 v125, a173\n"
    "v_accvgpr_read_b32 v126, a174\n"
    "v_accvgpr_read_b32 v127, a175\n"
    "v_mul_f32 v116, v116, v241\n"
    "v_mul_f32 v117, v117, v241\n"
    "v_mul_f32 v118, v118, v241\n"
    "v_mul_f32 v119, v119, v241\n"
    "v_mul_f32 v124, v124, v241\n"
    "v_mul_f32 v125, v125, v241\n"
    "v_mul_f32 v126, v126, v241\n"
    "v_mul_f32 v127, v127, v241\n"
    "v_cvt_pk_bf16_f32 v44, v116, v117\n"
    "v_cvt_pk_bf16_f32 v45, v118, v119\n"
    "v_cvt_pk_bf16_f32 v46, v124, v125\n"
    "v_cvt_pk_bf16_f32 v47, v126, v127\n"
    "v_mfma_f32_16x16x32_bf16 v[128:131], v[192:195], v[236:239], v[128:131]\n"
    "s_waitcnt lgkmcnt(10)\n"
    "buffer_store_dwordx4 v[44:47], v255, s[48:51], s60 offen offset:64 sc1\n"
    "ds_read_b64_tr_b16 a[252:253], v251 offset:50184\n"
    "ds_read_b64_tr_b16 a[254:255], v251 offset:54280\n"
    "v_mfma_f32_16x16x32_bf16 v[132:135], v[196:199], v[236:239], v[132:135]\n"
    "s_waitcnt lgkmcnt(10)\n"
    "v_accvgpr_read_b32 v100, a176\n"
    "v_accvgpr_read_b32 v101, a177\n"
    "v_mfma_f32_16x16x32_bf16 v[136:139], v[200:203], v[236:239], v[136:139]\n"
    "s_waitcnt lgkmcnt(8)\n"
    "v_accvgpr_read_b32 v102, a178\n"
    "v_accvgpr_read_b32 v103, a179\n"
    "v_mfma_f32_16x16x32_bf16 v[140:143], v[204:207], v[236:239], v[140:143]\n"
    "s_waitcnt lgkmcnt(6)\n"
    "v_accvgpr_read_b32 v108, a180\n"
    "v_accvgpr_read_b32 v109, a181\n"
    "v_mfma_f32_16x16x32_bf16 v[144:147], v[208:211], v[236:239], v[144:147]\n"
    "s_waitcnt lgkmcnt(4)\n"
    "v_accvgpr_read_b32 v110, a182\n"
    "v_accvgpr_read_b32 v111, a183\n"
    "v_mfma_f32_16x16x32_bf16 v[148:151], v[212:215], v[236:239], v[148:151]\n"
    "s_waitcnt lgkmcnt(2)\n"
    "v_mul_f32 v100, v100, v241\n"
    "v_mul_f32 v101, v101, v241\n"
    "v_mfma_f32_16x16x32_bf16 v[152:155], a[248:251], v[236:239], v[152:155]\n"
    "s_waitcnt lgkmcnt(0)\n"
    "v_mul_f32 v102, v102, v241\n"
    "v_mul_f32 v103, v103, v241\n"
    "v_mfma_f32_16x16x32_bf16 v[156:159], a[252:255], v[236:239], v[156:159]\n"
    "v_mul_f32 v108, v108, v241\n"
    "v_mul_f32 v109, v109, v241\n"
    "v_mfma_f32_16x16x32_bf16 v[188:191], a[112:115], v[236:239], v[188:191]\n"
    "v_mul_f32 v110, v110, v241\n"
    "v_mul_f32 v111, v111, v241\n"
    "v_cvt_pk_bf16_f32 v52, v100, v101\n"
    "v_cvt_pk_bf16_f32 v53, v102, v103\n"
    "v_cvt_pk_bf16_f32 v54, v108, v109\n"
    "v_cvt_pk_bf16_f32 v55, v110, v111\n"
    "buffer_store_dwordx4 v[52:55], v255, s[48:51], s60 offen offset:128 sc1\n"
    "v_accvgpr_read_b32 v116, a184\n"
    "v_accvgpr_read_b32 v117, a185\n"
    "v_accvgpr_read_b32 v118, a186\n"
    "v_accvgpr_read_b32 v119, a187\n"
    "v_accvgpr_read_b32 v124, a188\n"
    "v_accvgpr_read_b32 v125, a189\n"
    "v_accvgpr_read_b32 v126, a190\n"
    "v_accvgpr_read_b32 v127, a191\n"
    "v_mul_f32 v116, v116, v241\n"
    "v_mul_f32 v117, v117, v241\n"
    "v_mul_f32 v118, v118, v241\n"
    "v_mul_f32 v119, v119, v241\n"
    "v_mul_f32 v124, v124, v241\n"
    "v_mul_f32 v125, v125, v241\n"
    "v_mul_f32 v126, v126, v241\n"
    "v_mul_f32 v127, v127, v241\n"
    "v_cvt_pk_bf16_f32 v60, v116, v117\n"
    "v_cvt_pk_bf16_f32 v61, v118, v119\n"
    "v_cvt_pk_bf16_f32 v62, v124, v125\n"
    "v_cvt_pk_bf16_f32 v63, v126, v127\n"
    "buffer_store_dwordx4 v[60:63], v255, s[48:51], s60 offen offset:192 sc1\n"
    "v_mov_b32 v246, v188\n"
    "v_rcp_f32 v246, v246\n"
    "s_nop 0\n"
    "v_pk_mul_f32 v[128:129], v[128:129], v[246:247] op_sel_hi:[1,0]\n"
    "v_pk_mul_f32 v[130:131], v[130:131], v[246:247] op_sel_hi:[1,0]\n"
    "v_pk_mul_f32 v[132:133], v[132:133], v[246:247] op_sel_hi:[1,0]\n"
    "v_pk_mul_f32 v[134:135], v[134:135], v[246:247] op_sel_hi:[1,0]\n"
    "v_cvt_pk_bf16_f32 v128, v128, v129\n"
    "v_cvt_pk_bf16_f32 v129, v130, v131\n"
    "v_cvt_pk_bf16_f32 v130, v132, v133\n"
    "v_cvt_pk_bf16_f32 v131, v134, v135\n"
    "buffer_store_dwordx4 v[128:131], v255, s[48:51], s59 offen offset:0 sc1\n"
    "v_pk_mul_f32 v[136:137], v[136:137], v[246:247] op_sel_hi:[1,0]\n"
    "v_pk_mul_f32 v[138:139], v[138:139], v[246:247] op_sel_hi:[1,0]\n"
    "v_pk_mul_f32 v[140:141], v[140:141], v[246:247] op_sel_hi:[1,0]\n"
    "v_pk_mul_f32 v[142:143], v[142:143], v[246:247] op_sel_hi:[1,0]\n"
    "v_cvt_pk_bf16_f32 v136, v136, v137\n"
    "v_cvt_pk_bf16_f32 v137, v138, v139\n"
    "v_cvt_pk_bf16_f32 v138, v140, v141\n"
    "v_cvt_pk_bf16_f32 v139, v142, v143\n"
    "buffer_store_dwordx4 v[136:139], v255, s[48:51], s59 offen offset:64 sc1\n"
    "v_pk_mul_f32 v[144:145], v[144:145], v[246:247] op_sel_hi:[1,0]\n"
    "v_pk_mul_f32 v[146:147], v[146:147], v[246:247] op_sel_hi:[1,0]\n"
    "v_pk_mul_f32 v[148:149], v[148:149], v[246:247] op_sel_hi:[1,0]\n"
    "v_pk_mul_f32 v[150:151], v[150:151], v[246:247] op_sel_hi:[1,0]\n"
    "v_cvt_pk_bf16_f32 v144, v144, v145\n"
    "v_cvt_pk_bf16_f32 v145, v146, v147\n"
    "v_cvt_pk_bf16_f32 v146, v148, v149\n"
    "v_cvt_pk_bf16_f32 v147, v150, v151\n"
    "buffer_store_dwordx4 v[144:147], v255, s[48:51], s59 offen offset:128 sc1\n"
    "v_pk_mul_f32 v[152:153], v[152:153], v[246:247] op_sel_hi:[1,0]\n"
    "v_pk_mul_f32 v[154:155], v[154:155], v[246:247] op_sel_hi:[1,0]\n"
    "v_pk_mul_f32 v[156:157], v[156:157], v[246:247] op_sel_hi:[1,0]\n"
    "v_pk_mul_f32 v[158:159], v[158:159], v[246:247] op_sel_hi:[1,0]\n"
    "v_cvt_pk_bf16_f32 v152, v152, v153\n"
    "v_cvt_pk_bf16_f32 v153, v154, v155\n"
    "v_cvt_pk_bf16_f32 v154, v156, v157\n"
    "v_cvt_pk_bf16_f32 v155, v158, v159\n"
    "buffer_store_dwordx4 v[152:155], v255, s[48:51], s59 offen offset:192 sc1\n"
    "v_mfma_f32_16x16x32_bf16 v[224:227], v[0:3], v[8:11], 0\n"
    "v_mfma_f32_16x16x32_bf16 v[224:227], v[16:19], v[24:27], v[224:227]\n"
    "v_mfma_f32_16x16x32_bf16 v[224:227], v[32:35], v[40:43], v[224:227]\n"
    "v_mfma_f32_16x16x32_bf16 v[224:227], v[48:51], v[56:59], v[224:227]\n"
    "v_mfma_f32_16x16x32_bf16 v[224:227], v[64:67], v[72:75], v[224:227]\n"
    "v_mfma_f32_16x16x32_bf16 v[224:227], v[80:83], v[88:91], v[224:227]\n"
    "v_mfma_f32_16x16x32_bf16 v[224:227], v[96:99], v[104:107], v[224:227]\n"
    "v_mfma_f32_16x16x32_bf16 v[224:227], v[112:115], v[120:123], v[224:227]\n"
    "v_mfma_f32_16x16x32_bf16 v[224:227], v[4:7], v[12:15], v[224:227]\n"
    "v_mfma_f32_16x16x32_bf16 v[224:227], v[20:23], v[28:31], v[224:227]\n"
    "v_mfma_f32_16x16x32_bf16 v[224:227], v[36:39], v[44:47], v[224:227]\n"
    "v_mfma_f32_16x16x32_bf16 v[224:227], v[52:55], v[60:63], v[224:227]\n"
    "v_mfma_f32_16x16x32_bf16 v[224:227], v[128:131], v[136:139], v[224:227]\n"
    "v_mfma_f32_16x16x32_bf16 v[224:227], v[144:147], v[152:155], v[224:227]\n"
    "v_min3_f32 v216, v240, v241, v242\n"
    "v_min3_f32 v216, v216, v243, v244\n"
    "v_min3_f32 v216, v216, v245, v246\n"
    "v_max3_f32 v217, v240, v241, v242\n"
    "v_max3_f32 v217, v217, v243, v244\n"
    "v_max3_f32 v217, v217, v245, v246\n"
    "v_cmp_nle_f32 vcc, s34, v216\n"
    "s_or_b64 s[62:63], s[62:63], vcc\n"
    "v_cmp_nge_f32 vcc, s35, v217\n"
    "s_or_b64 s[62:63], s[62:63], vcc\n"
    "s_nop 7\n"
    "v_add_f32 v224, v224, v225\n"
    "v_add_f32 v226, v226, v227\n"
    "s_movk_i32 s68, 0x207\n"
    "v_add_f32 v224, v224, v226\n"
    "v_cmp_class_f32 vcc, v224, s68\n"
    "s_or_b64 s[62:63], s[62:63], vcc\n"
    "LEND_%=:\n"
    "s_or_b32 %[flag], s62, s63\n"
    : [flag] "=s"(flag)
    : [blk] "s"(blockIdx.x), [wid] "s"(wid), [ka] "s"(__builtin_amdgcn_kernarg_segment_ptr()), [lds] "s"((unsigned)(size_t)lds)
    : "v0", "v1", "v2", "v3", "v4", "v5", "v6", "v7", "v8", "v9", "v10", "v11", "v12", "v13", "v14", "v15", "v16", "v17", "v18", "v19", "v20", "v21", "v22", "v23", "v24", "v25", "v26", "v27", "v28", "v29", "v30", "v31", "v32", "v33", "v34", "v35", "v36", "v37", "v38", "v39", "v40", "v41", "v42", "v43", "v44", "v45", "v46", "v47", "v48", "v49", "v50", "v51", "v52", "v53", "v54", "v55", "v56", "v57", "v58", "v59", "v60", "v61", "v62", "v63", "v64", "v65", "v66", "v67", "v68", "v69", "v70", "v71", "v72", "v73", "v74", "v75", "v76", "v77", "v78", "v79", "v80", "v81", "v82", "v83", "v84", "v85", "v86", "v87", "v88", "v89", "v90", "v91", "v92", "v93", "v94", "v95", "v96", "v97", "v98", "v99", "v100", "v101", "v102", "v103", "v104", "v105", "v106", "v107", "v108", "v109", "v110", "v111", "v112", "v113", "v114", "v115", "v116", "v117", "v118", "v119", "v120", "v121", "v122", "v123", "v124", "v125", "v126", "v127", "v128", "v129", "v130", "v131", "v132", "v133", "v134", "v135", "v136", "v137", "v138", "v139", "v140", "v141", "v142", "v143", "v144", "v145", "v146", "v147", "v148", "v149", "v150", "v151", "v152", "v153", "v154", "v155", "v156", "v157", "v158", "v159", "v160", "v161", "v162", "v163", "v164", "v165", "v166", "v167", "v168", "v169", "v170", "v171", "v172", "v173", "v174", "v175", "v176", "v177", "v178", "v179", "v180", "v181", "v182", "v183", "v184", "v185", "v186", "v187", "v188", "v189", "v190", "v191", "v192", "v193", "v194", "v195", "v196", "v197", "v198", "v199", "v200", "v201", "v202", "v203", "v204", "v205", "v206", "v207", "v208", "v209", "v210", "v211", "v212", "v213", "v214", "v215", "v216", "v217", "v218", "v219", "v220", "v221", "v222", "v223", "v224", "v225", "v226", "v227", "v228", "v229", "v230", "v231", "v232", "v233", "v234", "v235", "v236", "v237", "v238", "v239", "v240", "v241", "v242", "v243", "v244", "v245", "v246", "v247", "v248", "v249", "v250", "v251", "v252", "v253", "v254", "v255", "a0", "a1", "a2", "a3", "a4", "a5", "a6", "a7", "a8", "a9", "a10", "a11", "a12", "a13", "a14", "a15", "a16", "a17", "a18", "a19", "a20", "a21", "a22", "a23", "a24", "a25", "a26", "a27", "a28", "a29", "a30", "a31", "a32", "a33", "a34", "a35", "a36", "a37", "a38", "a39", "a40", "a41", "a42", "a43", "a44", "a45", "a46", "a47", "a48", "a49", "a50", "a51", "a52", "a53", "a54", "a55", "a56", "a57", "a58", "a59", "a60", "a61", "a62", "a63", "a64", "a65", "a66", "a67", "a68", "a69", "a70", "a71", "a72", "a73", "a74", "a75", "a76", "a77", "a78", "a79", "a80", "a81", "a82", "a83", "a84", "a85", "a86", "a87", "a88", "a89", "a90", "a91", "a92", "a93", "a94", "a95", "a96", "a97", "a98", "a99", "a100", "a101", "a102", "a103", "a104", "a105", "a106", "a107", "a108", "a109", "a110", "a111", "a112", "a113", "a114", "a115", "a116", "a117", "a118", "a119", "a120", "a121", "a122", "a123", "a124", "a125", "a126", "a127", "a128", "a129", "a130", "a131", "a132", "a133", "a134", "a135", "a136", "a137", "a138", "a139", "a140", "a141", "a142", "a143", "a144", "a145", "a146", "a147", "a148", "a149", "a150", "a151", "a152", "a153", "a154", "a155", "a156", "a157", "a158", "a159", "a160", "a161", "a162", "a163", "a164", "a165", "a166", "a167", "a168", "a169", "a170", "a171", "a172", "a173", "a174", "a175", "a176", "a177", "a178", "a179", "a180", "a181", "a182", "a183", "a184", "a185", "a186", "a187", "a188", "a189", "a190", "a191", "a192", "a193", "a194", "a195", "a196", "a197", "a198", "a199", "a200", "a201", "a202", "a203", "a204", "a205", "a206", "a207", "a208", "a209", "a210", "a211", "a212", "a213", "a214", "a215", "a216", "a217", "a218", "a219", "a220", "a221", "a222", "a223", "a224", "a225", "a226", "a227", "a228", "a229", "a230", "a231", "a232", "a233", "a234", "a235", "a236", "a237", "a238", "a239", "a240", "a241", "a242", "a243", "a244", "a245", "a246", "a247", "a248", "a249", "a250", "a251", "a252", "a253", "a254", "a255", "s16", "s17", "s18", "s19", "s20", "s21", "s22", "s23", "s24", "s25", "s26", "s27", "s28", "s29", "s30", "s31", "s34", "s35", "s36", "s37", "s38", "s39", "s40", "s41", "s42", "s43", "s44", "s45", "s46", "s47", "s48", "s49", "s50", "s51", "s52", "s53", "s54", "s55", "s56", "s57", "s58", "s59", "s60", "s61", "s62", "s63", "s64", "s65", "s66", "s67", "s68", "s69", "s70", "s71", "s72", "s73", "s74", "s75", "s76", "s77", "s78", "s79", "s80", "s81", "s82", "s83", "s84", "s85", "s86", "s87", "s88", "s89", "s90", "s91", "s92", "s93", "s94", "s95", "s96", "s97", "s98", "s99", "vcc", "scc", "memory");
  if (__builtin_expect(flag != 0, 0)) {
    unsigned lane;
    asm volatile("s_waitcnt vmcnt(0)\n\tv_mbcnt_lo_u32_b32 %0, -1, 0\n\tv_mbcnt_hi_u32_b32 %0, -1, %0" : "=v"(lane) :: "memory");
    const size_t head = ((blockIdx.x & 7u) << 5) | (blockIdx.x >> 3);
    const size_t row0 = head * 496 + wid * 16;
    true_max_rows(q + row0 * 128, k + head * 131072, v + head * 131072, o + row0 * 128, wid == 3 ? 7 : 8, lane);
  }
}

extern "C" void kernel_launch(void* const* d_in, const int* in_sizes, int n_in, void* d_out, int out_size, void* d_ws, size_t ws_size,
                              hipStream_t stream) {
  hipLaunchKernelGGL(mha_fwd, dim3(256), dim3(256), 0, stream, (const unsigned short*)d_in[0], (const unsigned short*)d_in[1],
                     (const unsigned short*)d_in[2], (unsigned short*)d_out);
}
```
